```python
import torch

def _softmax(x, axis=-1):
    return torch.softmax(x, dim=axis)
def _gelu(x):
    return torch.nn.functional.gelu(x, approximate="tanh")
def _silu(x):
    return torch.nn.functional.silu(x)
def _sigmoid(x):
    return torch.sigmoid(x)
def _layernorm(x, axis=-1, eps=1e-5):
    m = x.mean(dim=axis, keepdim=True)
    v = x.var(dim=axis, keepdim=True, unbiased=False)
    return (x - m) / torch.sqrt(v + eps)
def _scatter_add(tgt, idx, src, axis=0):
    return tgt.clone().index_add_(axis, idx, src)
def _segment_sum(src, seg, n):
    out = torch.zeros((n,) + src.shape[1:], dtype=src.dtype)
    return out.index_add_(0, seg, src)
def _pad(x, pw):
    flat = tuple(v for pair in reversed(pw) for v in pair)
    return torch.nn.functional.pad(x, flat)

kernel_name = 'recurrent_scan__spectral_fnet__moe_arch_mod__prompt'

def setup_inputs():

    _w = (lambda a: (lambda t: t.to(torch.bfloat16) if t.is_floating_point() else t)(torch.as_tensor(a)))
    def _A_setup():
        B, S, D = 1, 2048, 1024
        ns = torch.arange(S)[:, None] * torch.arange(S)[None, :]
        nd = torch.arange(D)[:, None] * torch.arange(D)[None, :]
        Ws_re = torch.cos(-2.0 * torch.pi * ns / S).to(float)
        Ws_im = torch.sin(-2.0 * torch.pi * ns / S).to(float)
        Wd_re = torch.cos(-2.0 * torch.pi * nd / D).to(float)
        Wd_im = torch.sin(-2.0 * torch.pi * nd / D).to(float)
        return {
            "x": _w((lambda *s: torch.randn(*s, dtype=torch.bfloat16) * 0.05)(B, S, D)),
            "dft_seq_re": _w(Ws_re),
            "dft_seq_im": _w(Ws_im),
            "dft_hid_re": _w(Wd_re),
            "dft_hid_im": _w(Wd_im),
        }
    _A_in = _A_setup()

    def _B_setup():
        B, S, D, F, C = 1, 2048, 1024, 3584, 1
        return {
            "x": _w((lambda *s: torch.randn(*s, dtype=torch.bfloat16) * 0.05)(B, S, D)),
            "w_r": _w((lambda *s: torch.randn(*s, dtype=torch.bfloat16) * 0.05)(D)),
            "w_gate": _w((lambda *s: torch.randn(*s, dtype=torch.bfloat16) * 0.05)(D, F)),
            "w_up": _w((lambda *s: torch.randn(*s, dtype=torch.bfloat16) * 0.05)(D, F)),
            "w_down": _w((lambda *s: torch.randn(*s, dtype=torch.bfloat16) * 0.05)(F, D)),
        }
    _B_in = _B_setup()

    return {
        "A_x": _A_in["x"],
        "A_dft_seq_re": _A_in["dft_seq_re"],
        "A_dft_seq_im": _A_in["dft_seq_im"],
        "A_dft_hid_re": _A_in["dft_hid_re"],
        "A_dft_hid_im": _A_in["dft_hid_im"],
        "B_x": _B_in["x"],
        "B_w_r": _B_in["w_r"],
        "B_w_gate": _B_in["w_gate"],
        "B_w_up": _B_in["w_up"],
        "B_w_down": _B_in["w_down"],
    }

def reference(A_x, A_dft_seq_re, A_dft_seq_im, A_dft_hid_re, A_dft_hid_im, B_x, B_w_r, B_w_gate, B_w_up, B_w_down):
    def _A_ref(x, dft_seq_re, dft_seq_im, dft_hid_re, dft_hid_im):
        a = torch.einsum("bsd,ed->bse", x, dft_hid_re)
        b = torch.einsum("bsd,ed->bse", x, dft_hid_im)
        y = torch.einsum("ts,bse->bte", dft_seq_re, a) - torch.einsum("ts,bse->bte", dft_seq_im, b)
        return y

    def _B_ref(x, w_r, w_gate, w_up, w_down):
        B, S, D = x.shape
        C = 1
        r = x @ w_r
        idx = torch.argsort(-r, axis=-1)[:, :C]
        idx_e = (lambda x, n, axis: torch.repeat_interleave(x, n, dim=axis))(idx.reshape(B, C, 1), D, axis=2)
        x_sel = (lambda x, i, axis: torch.take_along_dim(x, i, dim=axis))(x, idx_e, axis=1)
        g = _silu(x_sel @ w_gate)
        u = x_sel @ w_up
        y = (g * u) @ w_down
        r_sel = _sigmoid((lambda x, i, axis: torch.take_along_dim(x, i, dim=axis))(r, idx, axis=1))
        y = y * r_sel.reshape(B, C, 1)
        b_off = (torch.arange(B) * S).reshape(B, 1)
        flat_idx = (idx + b_off).reshape(B * C)
        zero = (x * 0.0).reshape(B * S, D)
        upd = _scatter_add(zero, flat_idx, y.reshape(B * C, D), axis=0)
        return x + upd.reshape(B, S, D)

    x = A_x
    xn = _layernorm(x, axis=-1)
    s = _A_ref(xn, A_dft_seq_re, A_dft_seq_im, A_dft_hid_re, A_dft_hid_im)
    g = _silu(_B_ref(xn, B_w_r, B_w_gate, B_w_up, B_w_down))
    return x + s * g
```

```cpp
#define SP0 0
#define ST_SL 10
#define KFLIP 0
#define KX2 0
#define W4 0
#define CODE_LINES 334
#include <hip/hip_runtime.h>
#include <hip/hip_bf16.h>
#include <stdint.h>

typedef __attribute__((ext_vector_type(8))) short bf16x8;
typedef __attribute__((ext_vector_type(4))) float f32x4;
typedef __attribute__((ext_vector_type(2))) float f32x2;
typedef __attribute__((ext_vector_type(4))) unsigned u32x4;
typedef __attribute__((ext_vector_type(2))) unsigned u32x2;
typedef __attribute__((ext_vector_type(2))) __bf16 bf16x2;
typedef unsigned short u16;
typedef unsigned long long u64;
typedef __amdgpu_buffer_rsrc_t rsrc_t;
#define LAS __attribute__((address_space(3)))

constexpr int S = 2048, D = 1024, F = 3584;
constexpr int UB = 16384;
constexpr int SLOT = 3 * UB;
constexpr int L_MISC = 9 * UB;
constexpr int L_UPD = L_MISC + 64;
constexpr int L_MB = L_MISC + 512;
constexpr int L_XS = L_MB + 1536;
constexpr int L_CMB = L_XS + 512;
constexpr int L_ST = L_CMB + 512;
constexpr int L_DBG = L_ST + 6144;
constexpr int LDS_BYTES = L_DBG + 512;
constexpr int NBAR = 17 + 15 + 2;
constexpr int SC1 = 16, NT = 2;

constexpr unsigned WS_EPOCH = 0;
constexpr unsigned WS_STAT = 4096;
constexpr unsigned WS_CS = WS_STAT + S * 8;
constexpr unsigned WS_AMAX = WS_CS + 2 * D * 8;
constexpr unsigned WS_FLAG1 = WS_AMAX + 4096;
constexpr unsigned WS_FLAG2 = WS_FLAG1 + 32768;
constexpr unsigned WS_GU = WS_FLAG2 + 32768;
constexpr unsigned WS_PART = WS_GU + 8 * 2 * F * 8;
constexpr unsigned WS_XCH = WS_PART + 32 * D * 8;
constexpr int XB = 32768 + 24576;
#define ABT_OFF 0
#define ABT_PAD 0
constexpr unsigned ABT_ST = 128 * S * 2 + ABT_PAD;
constexpr unsigned WS_ABT = WS_XCH + 256 * XB + ABT_OFF;
constexpr unsigned WS_END = WS_ABT + 16 * ABT_ST;

#define STAMP(i)
#define BSTAMP()
#define HSTAMP()
#define STAMP_DECL
#define STAMP_ARG
#define STAMP_PASS
#define STAMP_OUT()

__device__ __forceinline__ float lo2f(unsigned v) { return __uint_as_float(v << 16); }
__device__ __forceinline__ float hi2f(unsigned v) { return __uint_as_float(v & 0xffff0000u); }
__device__ __forceinline__ unsigned pack2(float a, float b) {
  return __builtin_bit_cast(unsigned, __builtin_convertvector(f32x2{a, b}, bf16x2));
}
__device__ __forceinline__ float rbf(float f) { return lo2f(pack2(f, f)); }
__device__ __forceinline__ unsigned rfl(unsigned v) { return __builtin_amdgcn_readfirstlane(v); }
__device__ __forceinline__ rsrc_t mk_rsrc(const void* p, unsigned bytes) {
  return __builtin_amdgcn_make_buffer_rsrc((void*)p, 0, bytes, 0x00020000);
}
#define DPP_ADD(v, ctrl, rm, bc) v += __builtin_bit_cast(float, __builtin_amdgcn_update_dpp(0, __builtin_bit_cast(int, v), ctrl, rm, 0xf, bc))
#define DPP_MAXU(v, ctrl, rm, bc) v = max(v, (unsigned)__builtin_amdgcn_update_dpp(0, (int)v, ctrl, rm, 0xf, bc))
__device__ __forceinline__ unsigned wave_maxu(unsigned v) {
  DPP_MAXU(v, 0x111, 0xf, true); DPP_MAXU(v, 0x112, 0xf, true); DPP_MAXU(v, 0x114, 0xf, true); DPP_MAXU(v, 0x118, 0xf, true);
  DPP_MAXU(v, 0x142, 0xa, false); DPP_MAXU(v, 0x143, 0xc, false);
  return (unsigned)__builtin_amdgcn_readlane((int)v, 63);
}
template <int N> __device__ __forceinline__ void wait_vm() { asm volatile("s_waitcnt vmcnt(%0)" :: "n"(N) : "memory"); }
#define WAIT_LGKM0() asm volatile("s_waitcnt lgkmcnt(0)" ::: "memory")
#define BAR() do { asm volatile("" ::: "memory"); __builtin_amdgcn_s_barrier(); asm volatile("" ::: "memory"); } while (0)
#define MFMA(a, b, c) __builtin_amdgcn_mfma_f32_16x16x32_bf16(a, b, c, 0, 0, 0)
#define BF8(v) __builtin_bit_cast(bf16x8, v)
constexpr int SPIN_CAP = 1 << 20;
#define T1_T 0x0407060507060504ull
#define T2_T 0x1a19181716151413ull
#define GA_T 0x0740404040404040ull
#define UP_T 0x0a0a0a090a090908ull
#define WD_T 0x1416151413131716ull
__device__ __forceinline__ int sched(u64 tab, int te) { return (int)(tab >> (te * 8)) & 255; }
#define GROT 0
#define QLATE 2
#define WD_XE 0
#define RAMP_D 0
#define SPL 8
#define SB2 1
#define SB2_SL 0
#define E_JOBS 2
#define E_SLEEP 8
#define HPRIO 1
#define GG_AT 25
#define RAMP 3
#define TSL2 1
#define TSLEEP 3
#define TPOL ""
#define KDIR 2
#define K0_FIRST 1
#define SKIP_ON 0
#define BSL1 1
#define BSL2 1
#define LCHK 8
#define XE_AT 17
#define PIN4(a, b, c, d) asm volatile("" : "+v"(a), "+v"(b), "+v"(c), "+v"(d))
__device__ __forceinline__ unsigned vm_now() { const unsigned r = __builtin_amdgcn_s_getreg(7 | (31 << 11)); return (r & 15u) | ((r >> 18) & 0x30u); }
__device__ __forceinline__ unsigned mk_tag(unsigned epoch) { return rfl(epoch) * 0x9E3779B1u + 0x7F4A7C15u; }

template <int LD, int AUX, int MODE, int NP = 16>
__device__ __forceinline__ void issue(rsrc_t r, int voff, int koff, LAS char* dst) {
#pragma unroll
  for (int j = 0; j < NP; ++j) {
    const int so = MODE == 2 ? j * 1024 : MODE == 1 ? (64 * (j >> 3) + 32 * ((j >> 2) & 1) + 16 * (j & 1) + 4 * ((j >> 1) & 1)) * LD : j * 8 * LD;
    __builtin_amdgcn_raw_ptr_buffer_load_lds(r, (LAS void*)(dst + j * 1024), 16, voff, koff + so, 0, AUX);
  }
}
__device__ __forceinline__ int kstep(int tp1, int n) {
  const int dir = (KDIR == 2) ? ((tp1 & 2) ? -1 : 1) : 1;
  const int tile = n < 14 ? (tp1 ^ (KFLIP >> (n >> 1) & 1)) + dir * (n & ~1) : (tp1 & ~1) + dir * 14 + ((n - 14) >> 1);
  return (tile & 15) * 2 + (n & 1);
}

__device__ __forceinline__ void loader(int L, int lane, LAS char* lds, const u16* g1, const u16* gq, const char* gp, rsrc_t rws,
                                       int flag_off, int b, int tp1, int te STAMP_ARG) {
  STAMP_DECL;
  __builtin_amdgcn_s_setprio(2);
  const int tt = sched(T2_T, te);
  unsigned skip;
  {
    unsigned m = 0;
    if (SKIP_ON & 1) m |= 2u << sched(T1_T, te);
    if (SKIP_ON & 2) m |= 2u << tt;
    if (SKIP_ON & 8) m |= 2u << sched(GA_T, te);
    if (SKIP_ON & 16) m |= 2u << sched(UP_T, te);
    if (SKIP_ON & 32) m |= 2u << sched(WD_T, te);
    if (SKIP_ON & 64) m |= 1u << GG_AT;
    unsigned m1 = m & 0xff0u;
    m1 &= ~(m1 << 1 & m1 << 2);
    unsigned m2 = m & (0x1ffu << 19);
    m2 &= ~(m2 << 1);
    skip = m1 | m2;
  }
  const int rsub = lane >> 3, ch = ((lane & 7) ^ rsub) * 16;
  const rsrc_t r1 = mk_rsrc(g1, 128 * D * 2);
  const int v1 = (8 * (rsub >> 2) + (rsub & 3)) * (D * 2) + ch;
  const unsigned utab = L ? 0x8541u : 0x7230u;
  const rsrc_t rq = mk_rsrc(gq, 256 * S * 2);
  const int h = tp1 & 1;
  unsigned tch = 0;
  constexpr int TN1 = 3, TN2 = 4;
#define QTOUCH(o, so) asm volatile("buffer_load_dword %0, %1, %2, %3 offen" TPOL : "+v"(tch) : "v"(o), "s"(rq), "s"(so) : "memory")
  const int ta = L ? sched(T1_T, te) : -9;
  STAMP(1);
  const int ramp = RAMP + L * RAMP_D;
  int iss = 0, tpos = -1;
#pragma unroll 1
  for (int t = -3; t < 13; ++t) {
    if (t >= -1 && t != ramp) {
      const int y = t < ramp ? 1 : iss - 1 - t;
      if (t < tpos) { if (y == 2) wait_vm<32 + TN1>(); else if (y == 1) wait_vm<16 + TN1>(); else wait_vm<TN1>(); }
      else { if (y == 2) wait_vm<32>(); else if (y == 1) wait_vm<16>(); else wait_vm<0>(); }
      BAR();
    }
    if (t < 0 || !(skip >> t & 1)) {
#pragma unroll 1
      for (; iss < t + 4; ++iss) issue<D * 2, 0, 1>(r1, v1, iss * 128, lds + ((utab >> ((iss & 3) * 4)) & 15) * UB);
    }
    if (t == ta) {
      __builtin_amdgcn_s_sleep(TSLEEP);
      const int o_ = (te * 32 + (lane >> 1)) * (S * 2) + (lane & 1) * 128;
#pragma unroll
      for (int m = 0; m < TN1; ++m) QTOUCH(o_, kstep(tp1, 2 * m) * 128);
      tpos = iss;
    }
  }
  if (tpos > 13) wait_vm<32 + TN1>(); else wait_vm<32>();
  BAR();
  const int vq = rsub * (S * 2) + ch;
  const int qa = h * 128 * (S * 2), qb = ((1 - h) * 128 + L * 64) * (S * 2);
  LAS char* const dqb = lds + UB + L * 8192;
  if (L) {
#define QSTAGE(n, sl) do { const int k_ = kstep(tp1, n) * 128; issue<S * 2, 0, 0>(rq, vq, qa + k_, lds + (sl)); \
    issue<S * 2, 0, 0, 8>(rq, vq, qb + k_, dqb + (sl)); } while (0)
    constexpr int QL0 = QLATE, QL1 = QLATE < 3 ? QLATE + 1 : 3;
    if (QL0 == 0) QSTAGE(0, 0);
    if (tpos > 14) wait_vm<16 + (QL0 < 1 ? 24 : 0) + TN1>(); else wait_vm<16 + (QL0 < 1 ? 24 : 0)>();
    BAR();
    if (QL0 == 1) QSTAGE(0, 0);
    if (tpos > 15) wait_vm<(QL0 < 2 ? 24 : 0) + TN1>(); else wait_vm<(QL0 < 2 ? 24 : 0)>();
    if (QL1 == 1) QSTAGE(1, SLOT);
    BAR();
    if (QL0 == 2) QSTAGE(0, 0);
    if (QL1 == 2) QSTAGE(1, SLOT);
    BAR();
    if (QL0 == 3) QSTAGE(0, 0);
    if (QL1 == 3) QSTAGE(1, SLOT);
    int sl = 2 * SLOT;
#pragma unroll 1
    for (int n = 0, is2 = 2; n < 12; ++n) {
      if (is2 == n + 2) wait_vm<24>(); else wait_vm<0>();
      BAR();
      if (!(skip >> (17 + n) & 1)) {
#pragma unroll 1
        for (; is2 < n + 3; ++is2) { QSTAGE(is2, sl); sl = sl == 2 * SLOT ? 0 : sl + SLOT; }
      }
    }
    const int k2 = qa + (kstep(tp1, 14) ^ (tp1 & KX2)) * 128;
    wait_vm<24>();
    BAR();
    issue<S * 2, 0, 0>(rq, vq, k2, lds + 6 * UB);
    wait_vm<16>();
    BAR();
    issue<S * 2, 0, 0>(rq, vq, k2 + 128, lds);
    issue<S * 2, 0, 0>(rq, vq, k2 ^ 256, lds + UB);
    wait_vm<32>();
    BAR();
    issue<S * 2, 0, 0>(rq, vq, k2 ^ 384, lds + 3 * UB);
    wait_vm<16>();
    BAR();
    wait_vm<0>();
    BAR();
    asm volatile("" : "+v"(tch));
  } else {
#define QHALF(n, sl) issue<S * 2, 0, 0, 8>(rq, vq, qb + kstep(tp1, n) * 128, dqb + (sl))
    const rsrc_t rp = mk_rsrc(gp, 128 * S * 2);
    const int vp = lane * 16;
    constexpr int QL0 = QLATE, QL1 = QLATE < 3 ? QLATE + 1 : 3;
    if (QL0 == 0) QHALF(0, 0);
    wait_vm<16 + (QL0 < 1 ? 8 : 0)>();
    BAR();
    if (QL0 == 1) QHALF(0, 0);
    if (QL1 == 1) QHALF(1, SLOT);
    wait_vm<(QL0 < 2 ? 8 : 0) + (QL1 < 2 ? 8 : 0)>();
    BAR();
    if (QL0 == 2) QHALF(0, 0);
    if (QL1 == 2) QHALF(1, SLOT);
    BAR();
    if (QL0 == 3) QHALF(0, 0);
    if (QL1 == 3) QHALF(1, SLOT);
    wait_vm<8>();
    BAR();
    QHALF(2, 2 * SLOT);
    wait_vm<8>();
    BAR();
    QHALF(3, 0);
    STAMP(2);
    const unsigned tag = rfl(((volatile LAS unsigned*)(lds + L_MISC))[2]);
    u64 mask = 0;
    unsigned fl = 0;
    const int fo = flag_off + (lane >> 2) * 128 + (lane & 3) * 4;
    const int fo1 = flag_off + (kstep(tp1, 2) >> 1) * 128 + (lane & 3) * 4;
    {
      unsigned f1, f2;
      asm volatile("buffer_load_dword %0, %2, %3, 0 offen sc1\n\ts_sleep 3\n\tbuffer_load_dword %1, %2, %3, 0 offen sc1\n\ts_sleep 3"
                   : "=&v"(f1), "=&v"(f2) : "v"(fo1), "s"(rws) : "memory");
#pragma unroll 1
      for (int spins = 0; spins < SPIN_CAP; ++spins) {
        asm volatile("buffer_load_dword %0, %2, %3, 0 offen sc1\n\ts_waitcnt vmcnt(2)" : "+v"(fl), "+v"(f1) : "v"(fo1), "s"(rws) : "memory");
        if (__all(f1 == tag)) break;
        asm volatile("buffer_load_dword %0, %2, %3, 0 offen sc1\n\ts_waitcnt vmcnt(2)" : "+v"(f1), "+v"(f2) : "v"(fo1), "s"(rws) : "memory");
        if (__all(f2 == tag)) break;
        asm volatile("buffer_load_dword %0, %2, %3, 0 offen sc1\n\ts_waitcnt vmcnt(2)" : "+v"(f2), "+v"(fl) : "v"(fo1), "s"(rws) : "memory");
        if (__all(fl == tag)) break;
      }
      STAMP(3);
      issue<0, SC1, 2>(rp, vp, kstep(tp1, 2) * UB, lds + 2 * SLOT + 2 * UB);
      asm volatile("buffer_load_dword %0, %3, %4, 0 offen sc1" : "+v"(fl), "+v"(f1), "+v"(f2) : "v"(fo), "s"(rws) : "memory");
      issue<0, SC1, 2>(rp, vp, kstep(tp1, 3) * UB, lds + 2 * UB);
      asm volatile("s_waitcnt vmcnt(16)" : "+v"(fl), "+v"(f1), "+v"(f2) :: "memory");
    }
#define NEED(need) do { if (mask != ~0ull) { mask |= __ballot(fl == tag); \
      _Pragma("unroll 1") for (int spins = 0; (mask & (need)) != (need) && spins < SPIN_CAP; ++spins) { \
        asm volatile("buffer_load_dword %0, %1, %2, 0 offen sc1\n\ts_waitcnt vmcnt(0)" : "+v"(fl) : "v"(fo), "s"(rws) : "memory"); \
        mask |= __ballot(fl == tag); } \
      if (mask != ~0ull) asm volatile("buffer_load_dword %0, %1, %2, 0 offen sc1" : "+v"(fl) : "v"(fo), "s"(rws) : "memory"); } } while (0)
    int sl = SLOT, tp2 = 0;
#pragma unroll 1
    for (int n = 2, is2 = 4; n < 12; ++n) {
      BAR();
      const bool sk = skip >> (17 + n) & 1;
      if (!sk) {
        NEED(3ull << (2 * kstep(tp1, is2)) | 3ull << (2 * kstep(tp1, n + 2)));
#pragma unroll 1
        for (; is2 < n + 3; ++is2) {
          QHALF(is2, sl);
          issue<0, SC1, 2>(rp, vp, kstep(tp1, is2) * UB, lds + sl + 2 * UB);
          sl = sl == 2 * SLOT ? 0 : sl + SLOT;
        }
      }
      if (n + 17 == tt) {
        __builtin_amdgcn_s_sleep(TSL2);
        const int o_ = te < 6 ? ((te & 1) * 128 + (lane >> 1)) * (S * 2) + (kstep(tp1, 8 + (te & 6)) + (lane & 1)) * 128
                              : (h * 128 + (te & 1) * 64 + (lane >> 2)) * (S * 2) + (kstep(tp1, 14) + (lane & 3)) * 128;
#pragma unroll
        for (int j = 0; j < TN2; ++j) QTOUCH(o_, j * (te < 6 ? 32 * S * 2 : 16 * S * 2));
        tp2 = is2;
      }
      if (n + 1 < tp2) { if (sk) wait_vm<TN2>(); else wait_vm<24 + TN2>(); }
      else { if (sk) wait_vm<0>(); else wait_vm<24>(); }
      asm volatile("" : "+v"(fl) :: "memory");
    }
    const int k2 = kstep(tp1, 14) ^ (tp1 & KX2);
    BAR();
    NEED(255ull << (2 * (k2 & ~3)));
    issue<0, SC1, 2>(rp, vp, k2 * UB, lds + 8 * UB);
    issue<0, SC1, 2>(rp, vp, (k2 + 1) * UB, lds + 7 * UB);
    asm volatile("s_waitcnt vmcnt(32)" : "+v"(fl) :: "memory");
    BAR();
    issue<0, SC1, 2>(rp, vp, (k2 ^ 2) * UB, lds + 2 * UB);
    wait_vm<32>();
    BAR();
    issue<0, SC1, 2>(rp, vp, (k2 ^ 3) * UB, lds + 5 * UB);
    wait_vm<16>();
    BAR();
    wait_vm<0>();
    BAR();
    {
      const int f2o = WS_FLAG2 + (b ^ (4 + (lane & 12) % 12)) * 128 + (lane & 3) * 4;
      unsigned f1, f2, got = 0;
#define GOT(f) do { const unsigned m_ = (unsigned)__ballot(f == tag); \
        const unsigned g_ = ((m_ & 15u) == 15u) | (((m_ >> 4) & 15u) == 15u) << 1 | (((m_ >> 8) & 15u) == 15u) << 2; \
        if (g_ & ~got) { got |= g_; if (lane == 0) ((volatile LAS unsigned*)(lds + L_MISC))[12] = got; } } while (0)
      __builtin_amdgcn_s_sleep(E_SLEEP);
      asm volatile("buffer_load_dword %0, %2, %3, 0 offen sc1\n\ts_sleep 3\n\tbuffer_load_dword %1, %2, %3, 0 offen sc1\n\ts_sleep 3"
                   : "=&v"(f1), "=&v"(f2) : "v"(f2o), "s"(rws) : "memory");
#pragma unroll 1
      for (int spins = 0; spins < SPIN_CAP && got != 7; ++spins) {
        asm volatile("buffer_load_dword %0, %2, %3, 0 offen sc1\n\ts_waitcnt vmcnt(2)" : "+v"(fl), "+v"(f1) : "v"(f2o), "s"(rws) : "memory");
        GOT(f1);
        asm volatile("s_sleep 2\n\tbuffer_load_dword %0, %2, %3, 0 offen sc1\n\ts_waitcnt vmcnt(2)" : "+v"(f1), "+v"(f2) : "v"(f2o), "s"(rws) : "memory");
        GOT(f2);
        asm volatile("s_sleep 2\n\tbuffer_load_dword %0, %2, %3, 0 offen sc1\n\ts_waitcnt vmcnt(2)" : "+v"(f2), "+v"(fl) : "v"(f2o), "s"(rws) : "memory");
        GOT(fl);
        __builtin_amdgcn_s_sleep(2);
      }
      STAMP(4);
      asm volatile("s_waitcnt vmcnt(0)" : "+v"(fl), "+v"(f1), "+v"(f2), "+v"(tch) :: "memory");
    }
  }
  STAMP_OUT();
}

#define LDF(off) (*reinterpret_cast<const bf16x8*>(lds + (off)))
struct Frag { bf16x8 p[4], q[4]; };
__device__ __forceinline__ void load_frag(Frag& f, const char* lds, int poff, int qoff) {
  f.p[0] = LDF(poff);
#pragma unroll
  for (int i = 0; i < 4; ++i) f.q[i] = LDF(qoff + i * 2048);
#pragma unroll
  for (int i = 1; i < 4; ++i) f.p[i] = LDF(poff + i * 2048);
}
__device__ __forceinline__ void mma(f32x4 (&acc)[4][4], const Frag& f) {
#pragma unroll
  for (int i = 0; i < 4; ++i)
#pragma unroll
    for (int j = 0; j < 4; ++j) acc[i][j] = MFMA(f.p[i], f.q[j], acc[i][j]);
}
#define SGB(m, n) __builtin_amdgcn_sched_group_barrier(m, n, 0)
#define SCHED_HALF() do { _Pragma("unroll") for (int q_ = 0; q_ < 8; ++q_) { SGB(0x8, 1); SGB(0x100, 1); SGB(0x8, 1); } __builtin_amdgcn_sched_barrier(0); } while (0)

#define SCHED_HALF2() do { _Pragma("unroll") for (int q_ = 0; q_ < 8; ++q_) { SGB(0x8, 1); SGB(0x100, 1); } SGB(0x8, 8); __builtin_amdgcn_sched_barrier(0); } while (0)

__device__ __forceinline__ float silu_fast(float z) { return z * __builtin_amdgcn_rcpf(1.0f + __expf(-z)); }
__device__ __forceinline__ u32x4 pack8(const f32x4& a, const f32x4& c) {
  return u32x4{pack2(a[0], a[1]), pack2(a[2], a[3]), pack2(c[0], c[1]), pack2(c[2], c[3])};
}
__device__ __forceinline__ unsigned ln_pair(unsigned x, float nmb, float rden) {
  const unsigned d = pack2(lo2f(x) + nmb, hi2f(x) + nmb);
  return pack2(lo2f(d) * rden, hi2f(d) * rden);
}

template <bool BARRIER, bool JOBNEXT, bool ZERO, class X>
__device__ __forceinline__ void big_half(f32x4 (&a0)[4][4], f32x4 (&a1)[4][4], const bf16x8 (&pc)[4], bf16x8 (&pn)[4], bf16x8 (&q)[8],
                                         const char* lds, int qc, int pa, int qn, int qn2, X extra) {
  const f32x4 zero = {0.f, 0.f, 0.f, 0.f};
#pragma unroll
  for (int g = 0; g < 8; ++g) {
    if (g == 0) { q[5] = LDF(qc + UB + 2048); q[6] = LDF(qc + UB + 4096); }
    if (g == 1) q[7] = LDF(qc + UB + 6144);
    if (g == 3) { pn[0] = LDF(pa); pn[1] = LDF(pa + 2048); }
    if (g == 4) { pn[2] = LDF(pa + (JOBNEXT ? 1024 : 4096)); pn[3] = LDF(pa + (JOBNEXT ? 3072 : 6144)); }
    if (g == 5) { q[0] = LDF(qn); q[1] = LDF(qn + 2048); }
    if (g == 6) { q[2] = LDF(qn + 4096); q[3] = LDF(qn + 6144); }
    if (g == 7) { q[4] = LDF(qn2); if (JOBNEXT) q[5] = LDF(qn2 + 2048); }
#pragma unroll
    for (int i = 0; i < 4; ++i) {
      const int ii = (g & 1) ? 3 - i : i;
      if (g < 4) a0[ii][g] = MFMA(pc[ii], q[g], ZERO ? zero : a0[ii][g]); else a1[ii][g - 4] = MFMA(pc[ii], q[g], ZERO ? zero : a1[ii][g - 4]);
    }
    SGB(0x8, 1); SGB(0x100, 1); SGB(0x8, 1); SGB(0x100, 1); SGB(0x8, 2);
    __builtin_amdgcn_sched_barrier(0);
    if (g == 2 && BARRIER) { WAIT_LGKM0(); BAR(); }
    extra(g);
  }
}

__device__ __forceinline__ void consumer(int wid, int lane, int b, char* lds, rsrc_t rws, rsrc_t rout,
                                         const char* code, int tp1, int tq1 STAMP_ARG) {
  STAMP_DECL;
  const int z = tq1 >> 3, te = tq1 & 7;
  const int wp = wid >> 1, wq = wid & 1, fr = lane & 15, fq = lane >> 4;
  const int c0 = (fq ^ (fr & 7)) * 16, c1 = ((fq + 4) ^ (fr & 7)) * 16;
  const int q0 = (wq * 64 + fr) * 128 + c0, q1 = (wq * 64 + fr) * 128 + c1;
  volatile LAS unsigned* misc = (volatile LAS unsigned*)(lds + L_MISC);
  f32x4 acc[4][4];
#pragma unroll
  for (int i = 0; i < 4; ++i)
#pragma unroll
    for (int j = 0; j < 4; ++j) acc[i][j] = f32x4{0.f, 0.f, 0.f, 0.f};
  unsigned tag;
  u32x4 xv[4];
  {
    Frag f0, f1;
    const int xrow = wq * 64 + 16 * ((fr >> 2) & 1) + 4 * (fr >> 3) + (fr & 3);
    const int xr = xrow * 128 + (((z * 4 + fq) ^ (xrow & 7)) * 16);
#define XV(u) do { _Pragma("unroll") for (int k = 0; k < 4; ++k) \
      xv[k] = *reinterpret_cast<const u32x4*>(lds + (u) * UB + xr + (k >> 1) * 4096 + (k & 1) * 1024); } while (0)
    const int p0 = (wp * 64 + fr) * 128 + c0, p1 = (wp * 64 + fr) * 128 + c1;
    unsigned tch = 0;
    if (wid == 3 && lane < (CODE_LINES + 7) / 8) {
      __builtin_amdgcn_s_sleep(8);
      const int co = min(((b >> 3) & 7) * ((CODE_LINES + 7) / 8) + lane, CODE_LINES - 1) * 128;
      asm volatile("global_load_dword %0, %1, off" : "+v"(tch) : "v"(code + co) : "memory");
    }
    BAR();
    load_frag(f0, lds, p0, UB + q0);
#pragma unroll 1
    for (int t = 0; t < 15; ++t) {
      const int sh = (t & 3) * 4, sn = ((t + 1) & 3) * 4;
      if (__builtin_expect(t == 2 * te + wp, 0)) XV((0x7230u >> sh) & 15);
      load_frag(f1, lds, ((0x7230u >> sh) & 15) * UB + p1, ((0x8541u >> sh) & 15) * UB + q1);
      mma(acc, f0);
      SCHED_HALF2();
      WAIT_LGKM0();
      BAR();
      load_frag(f0, lds, ((0x7230u >> sn) & 15) * UB + p0, ((0x8541u >> sn) & 15) * UB + q0);
      mma(acc, f1);
      SCHED_HALF();
    }
    if (2 * te + wp == 15) XV(7);
    load_frag(f1, lds, 7 * UB + p1, 8 * UB + q1);
    const u32x2 rdy = *(const LAS u32x2*)(lds + L_MISC + 16);
    mma(acc, f0);
    SCHED_HALF();
    asm volatile("s_waitcnt vmcnt(0) lgkmcnt(0)" : "+v"(tch) :: "memory");
    BAR();
    if (!rfl(rdy[0] & rdy[1])) {
#pragma unroll 1
      for (int spins = 0; spins < SPIN_CAP && !rfl(misc[4] & misc[5]); ++spins) __builtin_amdgcn_s_sleep(1);
    }
    asm volatile("" ::: "memory");
    f32x4 mb[2][2], rd[2][2]; float cs[4];
    const unsigned tagv = *(const LAS unsigned*)(lds + L_MISC + 8);
#pragma unroll
    for (int m = 0; m < 2; ++m)
#pragma unroll
      for (int h = 0; h < 2; ++h) {
        mb[m][h] = *reinterpret_cast<const f32x4*>(lds + L_MB + (wp * 64 + m * 32 + fq * 8 + h * 4) * 4);
        rd[m][h] = *reinterpret_cast<const f32x4*>(lds + L_MB + 512 + (wp * 64 + m * 32 + fq * 8 + h * 4) * 4);
      }
#pragma unroll
    for (int j = 0; j < 4; ++j)
      cs[j] = *reinterpret_cast<const float*>(lds + L_MB + 1024 + (wq * 64 + 32 * (j >> 1) + 8 * (fr >> 2) + 4 * (j & 1) + (fr & 3)) * 4);
    mma(acc, f1);
#pragma unroll
    for (int q_ = 0; q_ < 13; ++q_) { SGB(0x100, 1); SGB(0x8, 1); }
    __builtin_amdgcn_sched_barrier(0);
    STAMP(2);
    tag = rfl(tagv);
    const float sgn = z ? -1.f : 1.f;
    const int flip = z ? 0 : 4096;
    const int ao = WS_ABT + tq1 * ABT_ST + ((tp1 * 2 + wp) * 8 + wq * 4) * 2048 + lane * 16;
    char* own = lds + wp * SLOT + 2 * UB + wq * 8192 + lane * 16;
#pragma unroll
    for (int m = 0; m < 2; ++m) {
#pragma unroll
      for (int h = 0; h < 2; ++h) rd[m][h] *= sgn;
#pragma unroll
      for (int j = 0; j < 4; ++j) {
        const f32x4 v0 = (mb[m][0] * cs[j] + acc[2 * m][j]) * rd[m][0], v1 = (mb[m][1] * cs[j] + acc[2 * m + 1][j]) * rd[m][1];
        const u32x4 o = pack8(v0, v1);
        const int fo = (j < 2 ? flip : -flip) + j * 2048 + m * 1024;
        __builtin_amdgcn_raw_buffer_store_b128(o, rws, ao + fo, 0, SC1);
        *reinterpret_cast<u32x4*>(own + fo) = o;
      }
    }
    STAMP(3);
  }
  f32x4 acb[4][4];
  bf16x8 pa[4], pb[4], q[8];
  const int pp = wp * 8192 + lane * 16;
  const int xo = ((tp1 * 128 + wq * 64 + fr) * D + te * 128 + wp * 64 + z * 32 + fq * 8) * 2;
  bool sent = false;
#define FLAG(off, n) do { if (!sent && vm_now() <= (n)) { sent = true; asm volatile("" ::: "memory"); \
    if (lane == 0) __builtin_amdgcn_raw_buffer_store_b32(tag, rws, off, 0, SC1); STAMP(FST); } } while (0)
#define FST 4
  const int f1o = WS_FLAG1 + (tq1 * 16 + tp1) * 128 + wid * 4;
  auto flag1 = [&](int g) { if (g & 1) FLAG(f1o, 0); };
  WAIT_LGKM0();
  BAR();
  STAMP(5);
  pa[0] = LDF(2 * UB + pp); q[0] = LDF(q0); pa[1] = LDF(2 * UB + pp + 2048); pa[2] = LDF(2 * UB + pp + 4096); pa[3] = LDF(2 * UB + pp + 6144);
  q[1] = LDF(q0 + 2048); q[2] = LDF(q0 + 4096); q[3] = LDF(q0 + 6144); q[4] = LDF(q0 + UB);
  __builtin_amdgcn_sched_barrier(0);
  auto none = [](int) {};
  big_half<false, false, true>(acc, acb, pa, pb, q, lds, q0, 2 * UB + pp + 1024, q1, q1 + UB, flag1);
  big_half<true, false, false>(acc, acb, pb, pa, q, lds, q1, SLOT + 2 * UB + pp, SLOT + q0, SLOT + q0 + UB, flag1);
  big_half<false, false, false>(acc, acb, pa, pb, q, lds, SLOT + q0, SLOT + 2 * UB + pp + 1024, SLOT + q1, SLOT + q1 + UB, flag1);
  if (!sent) { wait_vm<0>(); FLAG(f1o, 0); }
  {
    int sl = SLOT;
#pragma unroll 1
    for (int n = 1; n < 13; ++n) {
      const int sn = sl == 2 * SLOT ? 0 : sl + SLOT;
      big_half<true, false, false>(acc, acb, pb, pa, q, lds, sl + q1, sn + 2 * UB + pp, sn + q0, sn + q0 + UB, none);
      big_half<false, false, false>(acc, acb, pa, pb, q, lds, sn + q0, sn + 2 * UB + pp + 1024, sn + q1, sn + q1 + UB, none);
      sl = sn;
    }
  }
  big_half<true, true, false>(acc, acb, pb, pa, q, lds, SLOT + q1, 8 * UB + pp, 6 * UB + q0, 6 * UB + q1, none);
  STAMP(10);
  const int xw = WS_XCH + wid * 4096 + lane * 16, xl = WS_XCH + 32768 + wid * 6144 + lane * 16;
  const int f2o = WS_FLAG2 + b * 128 + wid * 4;
#define PU(k) ((k) == 0 ? 8 : (k) == 1 ? 7 : (k) == 2 ? 2 : 5)
#define QU(k) ((k) == 0 ? 6 : (k) == 1 ? 0 : (k) == 2 ? 1 : 3)
#define JOB(K, I0, pc, pn, KN, IN, EXTRA) do { _Pragma("unroll") for (int g = 0; g < 8; ++g) { \
    if (g < 2) q[g + 6] = LDF(QU(K) * UB + q1 + (g + 2) * 2048); \
    if ((KN) >= 0) { \
      if (g >= 2) q[g - 2] = LDF(QU(KN) * UB + (g < 6 ? q0 + (g - 2) * 2048 : q1 + (g - 6) * 2048)); \
      if (g < 4) pn[g] = LDF(PU(KN) * UB + pp + ((IN) + (g & 1)) * 2048 + (g >> 1) * 1024); } \
    acc[I0][g & 3] = MFMA(pc[(g >> 2) * 2], q[g], acc[I0][g & 3]); \
    acc[(I0) + 1][g & 3] = MFMA(pc[(g >> 2) * 2 + 1], q[g], acc[(I0) + 1][g & 3]); \
    EXTRA(g); \
    SGB(0x8, 1); SGB(0x100, 2); SGB(0x8, 1); \
    __builtin_amdgcn_sched_barrier(0); } } while (0)
#define X_NONE(g)
  u32x4 e1[4], e2[4], ot[5]; u32x2 o5;
#define SEND_E(j) __builtin_amdgcn_raw_buffer_store_b128(pack8(acb[(j) < 4 ? 2 : 0][(j) & 3], acb[(j) < 4 ? 3 : 1][(j) & 3]), rws, \
                                                         xw + b * XB + ((j) >> 2) * 16384 + ((j) & 3) * 1024, 0, SC1)
#define X_E1(g) if ((g) & 1) SEND_E((g) >> 1)
#define X_E2(g) if ((g) & 1) SEND_E(4 + ((g) >> 1))
#define X_E3(g)
#define LT(i) pack2(acc[((i) >> 1) & 1][((i) >> 2) & 3][((i) & 1) * 2], acc[((i) >> 1) & 1][((i) >> 2) & 3][((i) & 1) * 2 + 1])
#define X_LATE(g) if ((g) < 5) __builtin_amdgcn_raw_buffer_store_b128(u32x4{LT(3 * (g)), LT(3 * (g) + 1), LT(3 * (g) + 2), tag}, rws, xl + b * XB + (g) * 1024, 0, SC1); \
  else if ((g) == 5) __builtin_amdgcn_raw_buffer_store_b64(u32x2{LT(15), tag}, rws, xl + b * XB + 5120 - lane * 8, 0, SC1)
#undef FST
#define FST 9
  BAR();
  sent = false;
  JOB(0, 0, pa, pb, 1, 0, X_E1);
  JOB(1, 0, pb, pa, 2, 0, X_E2);
  JOB(2, 0, pa, pb, 0, 2, X_E3);
  BAR();
  JOB(0, 2, pb, pa, 3, 0, X_NONE);
  JOB(3, 0, pa, pb, 1, 2, X_NONE);
  JOB(1, 2, pb, pa, 2, 2, X_LATE);
  FLAG(f2o, 6);
  JOB(2, 2, pa, pb, 3, 2, X_NONE);
  FLAG(f2o, 6);
  JOB(3, 2, pb, pa, -1, 0, X_NONE);
  FLAG(f2o, 6);
  STAMP(6);
  unsigned got = 0;
#define EMASK 7
#define RECV(v, n, off) do { _Pragma("unroll") for (int k_ = 0; k_ < (n); ++k_) \
    v[k_] = __builtin_amdgcn_raw_buffer_load_b128(rws, (off) + (k_ & 3) * 1024, (k_ >> 2) * 4096, SC1); asm volatile("" ::: "memory"); } while (0)
#define RECV_LATE() do { o5 = __builtin_amdgcn_raw_buffer_load_b64(rws, xl + (b ^ 4) * XB + 5120 - lane * 8, 0, SC1); RECV(ot, 5, xl + (b ^ 4) * XB); } while (0)
#define TRY() do { const unsigned m_ = rfl(misc[12]) & ~got; if (m_) { got |= m_; asm volatile("" ::: "memory"); \
    if (m_ & 1) RECV_LATE(); \
    if (m_ & 2 & EMASK) RECV(e1, 4, xw + (b ^ 8) * XB); \
    if (m_ & 4 & EMASK) RECV(e2, 4, xw + (b ^ 12) * XB + 16384); } } while (0)
  f32x2 gf[4][4];
#define TM(k) (*reinterpret_cast<const float*>(lds + L_MB + (wq * 64 + (k) * 16 + fr) * 4))
#define TR(k) (*reinterpret_cast<const float*>(lds + L_MB + 512 + (wq * 64 + (k) * 16 + fr) * 4))
#pragma unroll
  for (int k = 0; k < 4; ++k) {
    const float tm = TM(k), tr = TR(k);
#pragma unroll
    for (int c = 0; c < 4; ++c) {
      const unsigned n = ln_pair(xv[k][c], tm, tr); gf[k][c] = f32x2{silu_fast(lo2f(n)), silu_fast(hi2f(n))};
      if (k == 0) { asm volatile("" : "+v"(gf[k][c])); FLAG(f2o, 6); }
    }
    PIN4(gf[k][0], gf[k][1], gf[k][2], gf[k][3]);
    if (k == 0 && !sent) { wait_vm<6>(); FLAG(f2o, 6); }
    TRY();
  }
  STAMP(1);
#pragma unroll 1
  for (int spins = 0; spins < SPIN_CAP && got != 7; ++spins) TRY();
  STAMP(7);
  {
    unsigned sel = 0;
#pragma unroll 1
    for (int spins = 0; spins < SPIN_CAP; ++spins) { sel = rfl(misc[0]); if (sel) break; __builtin_amdgcn_s_sleep(2); }
    const int idx = (int)sel - 1;
    if ((idx >> 6) == tp1 * 2 + wq) {
#pragma unroll 1
      for (int spins = 0; spins < SPIN_CAP; ++spins) { if (rfl(misc[1])) break; __builtin_amdgcn_s_sleep(2); }
      asm volatile("" ::: "memory");
      const LAS float* upd = (const LAS float*)(lds + L_UPD) + wp * 32 + fq * 8;
#pragma unroll
      for (int k = 0; k < 4; ++k) {
        if (((idx >> 4) & 3) == k && (idx & 15) == fr) {
#pragma unroll
          for (int c = 0; c < 4; ++c) {
            const unsigned n = ln_pair(xv[k][c], TM(k), TR(k));
            const unsigned u = pack2(lo2f(n) + upd[2 * c], hi2f(n) + upd[2 * c + 1]);
            gf[k][c] = f32x2{silu_fast(lo2f(u)), silu_fast(hi2f(u))};
          }
        }
      }
    }
  }
#pragma unroll 1
  for (int spins = 0; spins < SPIN_CAP / 16; ++spins) {
    if (__all(((ot[0][3] ^ tag) | (ot[1][3] ^ tag) | (ot[2][3] ^ tag) | (ot[3][3] ^ tag) | (ot[4][3] ^ tag) | (o5[1] ^ tag)) == 0)) break;
    asm volatile("" ::: "memory");
    RECV_LATE();
  }
  STAMP(11);
#pragma unroll
  for (int k = 0; k < 4; ++k) {
    u32x4 ov;
#pragma unroll
    for (int c = 0; c < 4; ++c) {
      const unsigned a1 = e1[k][c], a2 = e2[k][c], a3 = 4 * k + c < 15 ? ot[(4 * k + c) / 3 % 5][(4 * k + c) % 3] : o5[0];
      const f32x2 sv = (f32x2{acc[2 + (c >> 1)][k][(c & 1) * 2], acc[2 + (c >> 1)][k][(c & 1) * 2 + 1]} + f32x2{lo2f(a1), hi2f(a1)}) +
                       (f32x2{lo2f(a2), hi2f(a2)} + f32x2{lo2f(a3), hi2f(a3)});
      const f32x2 r = sv * gf[k][c] + f32x2{lo2f(xv[k][c]), hi2f(xv[k][c])};
      ov[c] = pack2(r[0], r[1]);
    }
    __builtin_amdgcn_raw_buffer_store_b128(ov, rout, xo + k * 16 * D * 2, 0, 18);
  }
  STAMP(8);
  STAMP_OUT();
}

__device__ __forceinline__ unsigned now_cyc() { return (unsigned)__builtin_amdgcn_s_memtime(); }
#define BUD1 400
#define BUD1B 300
#define BUDT 2200
#define AM_SL 5
#define GV_AT 19
#define GV_SL 8
#define BUD2 750
__device__ __forceinline__ unsigned budget(int nb) { return nb <= 12 ? BUD1 : nb <= 16 ? BUD1B : nb <= 18 ? 450 : nb == 19 ? BUDT : nb <= 31 ? BUD2 : 150; }
#define JOIN() do { BAR(); HSTAMP(); ++nb; t0 = now_cyc(); } while (0)
#define CHECK() do { if (nb < lim && now_cyc() - t0 > budget(nb)) JOIN(); } while (0)
#define TICK() do { CHECK(); __builtin_amdgcn_s_sleep(1); asm volatile("" ::: "memory"); } while (0)
#define WAITVM(n) do { _Pragma("unroll 1") while (vm_now() > (n)) TICK(); } while (0)
#define UNTIL(n) do { _Pragma("unroll 1") while (nb < (n)) JOIN(); } while (0)

__device__ __forceinline__ void helper(int hw, int lane, int b, char* lds, rsrc_t rws, rsrc_t rx, const u16* wrow, const u16* wr,
                                       const u16* wgu, const u16* wdn, const u16* gq, int tp1, int tq1 STAMP_ARG) {
  STAMP_DECL;
  int nb = 0, lim = 17;
  unsigned t0 = 0, tch = 0;
  volatile LAS unsigned* misc = (volatile LAS unsigned*)(lds + L_MISC);
  __builtin_amdgcn_s_setprio(3);
  const int z = tq1 >> 3, te = tq1 & 7;
  const int rg = b / 28, cg = b - rg * 28;
  const int fg = b >> 3, eg = b & 7;
  const int lc = lane & 15, lq = lane >> 4;
  u32x4 w[32], wd[14], tb;
  const rsrc_t rgu = mk_rsrc(wgu + (size_t)(rg & 7) * 128 * F + cg * 128, 128 * F * 2);
  const int gu_at = sched(hw ? UP_T : GA_T, te), wd_at = sched(WD_T, te);
#define LOADW() do { if (b < 224) LOADW_(); } while (0)
#define LOADW_() do { { _Pragma("unroll") for (int k = 0; k < 32; ++k) \
    { w[k] = __builtin_amdgcn_raw_buffer_load_b128(rgu, lq * 32 * F * 2 + lc * 16, k * F * 2, NT); if (LCHK && (k & (LCHK - 1)) == LCHK - 1) CHECK(); } } } while (0)
  unsigned tag, key, xe, sg = 0; int idx;
  if (hw == 1 && lane < 16) misc[lane] = 0u;
  {
    const rsrc_t rw = mk_rsrc(wrow, 4 * D * 2), rr = mk_rsrc(wr, D * 2);
    const int row0 = tp1 * 128 + tq1 * 8 + hw * 4;
    const int vo = (lane & 3) * (D * 2) + (lane >> 2) * 16;
    u32x4 st[24];
    __builtin_amdgcn_s_sleep(ST_SL);
    const unsigned epoch = __builtin_amdgcn_raw_buffer_load_b32(rws, WS_EPOCH, 0, SC1);
#define LOADST(i0, i1) _Pragma("unroll") for (int i = i0; i < (i1); ++i) { \
      st[i & 7] = __builtin_amdgcn_raw_buffer_load_b128(rx, row0 * (D * 2) + vo, (i & 7) * 256, 0); \
      st[8 + (i & 7)] = __builtin_amdgcn_raw_buffer_load_b128(rw, vo, (i & 7) * 256, 0); }
    LOADST(SP0, SP0 + SPL);
    STAMP(9);
    WAIT_LGKM0();
    JOIN();
    UNTIL(SB2);
    if (SB2_SL) __builtin_amdgcn_s_sleep(SB2_SL);
    LOADST(SP0 + SPL, SP0 + 8);
#pragma unroll
    for (int i = 0; i < 8; ++i) st[16 + i] = __builtin_amdgcn_raw_buffer_load_b128(rr, (lq * 4 + (lane & 3)) * 16, i * 256, 0);
    if (hw == 1) {
      const int l6 = lane < 5 ? lane : 0;
      const int o_ = l6 == 0 ? WS_STAT + (tp1 * 128 + tq1 * 8) * 8 : l6 == 1 ? WS_CS + (tq1 * 128 + tp1 * 8) * 8 : l6 == 2 ? WS_AMAX + b * 16 :
                     l6 == 3 ? WS_FLAG1 + (tq1 * 16 + tp1) * 128 : WS_FLAG2 + b * 128;
      asm volatile("buffer_load_dword %0, %1, %2, 0 offen sc1" : "+v"(tch) : "v"(o_), "s"(rws) : "memory");
    }
    WAITVM(0);
    STAMP(1);
    tag = mk_tag(epoch);
    f32x4 d1 = {0.f, 0.f, 0.f, 0.f}, d2 = d1, d3 = d1, d4 = d1;
#pragma unroll
    for (int i = 0; i < 8; ++i) {
      u32x4 wv = st[16 + i];
      if (lc >= 4) wv = u32x4{0x3f803f80u, 0x3f803f80u, 0x3f803f80u, 0x3f803f80u};
      d1 = MFMA(BF8(st[i]), BF8(wv), d1); d2 = MFMA(BF8(st[i]), BF8(st[i]), d2);
      d3 = MFMA(BF8(st[8 + i]), BF8(wv), d3); d4 = MFMA(BF8(wv), BF8(wv), d4);
      if (i & 1) { PIN4(d1, d2, d3, d4); CHECK(); }
    }
    char* sc = lds + L_ST + hw * 3072;
    *reinterpret_cast<f32x4*>(sc + lane * 16) = d1;
    *reinterpret_cast<f32x4*>(sc + 1024 + lane * 16) = d2;
    *reinterpret_cast<f32x4*>(sc + 2048 + lane * 16) = d3;
    const float sw = __builtin_bit_cast(float, __builtin_amdgcn_readlane(__builtin_bit_cast(int, (d4[0] + d4[1]) + (d4[2] + d4[3])), 4));
    const int r = lane & 3, qq = (lane >> 2) & 3;
    float v = *reinterpret_cast<const float*>(sc + (lq == 0 ? qq : lq == 1 ? 4 : lq == 2 ? 64 + r + 4 * qq : 128 + 4) * 16 + qq * 256 + r * 4);
    DPP_ADD(v, 0x128, 0xf, false); DPP_ADD(v, 0x124, 0xf, false);
    CHECK();
    float sx = __shfl(v, lane + 16), sq = __shfl(v, lane + 32);
    asm volatile("" : "+v"(sx), "+v"(sq));
    CHECK();
    const float mn = sx * (1.0f / D);
    const float var = fmaxf(fmaf(-mn, mn, sq * (1.0f / D)), 0.f);
    const float mbv = rbf(mn), den = rbf(__builtin_amdgcn_sqrtf(rbf(rbf(var) + 1e-5f)));
    const float rs = (v - mbv * sw) * __builtin_amdgcn_rcpf(den);
    const unsigned rb = pack2(rs, rs) & 0xffffu;
    unsigned srt = (rb & 0x8000u) ? (~rb & 0xffffu) : (rb | 0x8000u);
    if (rs != rs) srt = 0u;
    asm volatile("" : "+v"(srt));
    CHECK();
    if (lane < 4 || (lane >= 48 && lane < 52))
      __builtin_amdgcn_raw_buffer_store_b64(u32x2{lane < 4 ? pack2(mbv, den) : __float_as_uint(v), tag}, rws,
                                            lane < 4 ? WS_STAT + (row0 + r) * 8 : WS_CS + (tq1 * 128 + tp1 * 8 + hw * 4 + r) * 8, 0, SC1);
    CHECK();
    if (lane < 4) misc[80 + hw * 4 + r] = pack2(mbv, den);
    if (lane < 4) __hip_atomic_fetch_max((LAS unsigned*)(lds + L_MISC) + 8, (srt << 16) | (unsigned)(0xffff - row0 - r), __ATOMIC_RELAXED, __HIP_MEMORY_SCOPE_WORKGROUP);
    CHECK();
    if (lane == 0) {
      misc[2] = tag;
      if (__hip_atomic_fetch_add((LAS unsigned*)(lds + L_MISC) + 9, 1u, __ATOMIC_RELAXED, __HIP_MEMORY_SCOPE_WORKGROUP) == 1u)
      {
        const unsigned k_ = misc[8];
        __builtin_amdgcn_raw_buffer_store_b128(u32x4{k_, tag, misc[80 + (~k_ & 7)], tag}, rws, WS_AMAX + b * 16, 0, SC1);
      }
    }
  }
  STAMP(2);
  const rsrc_t rd = mk_rsrc(wdn + (size_t)(fg * 112 + hw * 56) * D + eg * 128, 56 * D * 2);
#define LOADD() do { _Pragma("unroll") for (int k = 0; k < 14; ++k) \
    wd[k] = __builtin_amdgcn_raw_buffer_load_b128(rd, lq * 14 * D * 2 + lc * 16, k * D * 2, NT); } while (0)
#define AM_AT 13
#define BURST1() do { UNTIL(gu_at + 1); __builtin_amdgcn_s_sleep(BSL1); BURST0(); } while (0)
#define BURST0() do { \
    if (b >= 224) { \
      const rsrc_t rq = mk_rsrc(gq, 256 * S * 2); \
      const int o_ = (W4 & 4) ? (hw * 128 + (tp1 & 1) * 64 + (lane >> 2)) * (S * 2) + ((kstep(tp1, 6) & ~3) + (lane & 3)) * 128 \
                              : (hw * 128 + (lane >> 1)) * (S * 2) + (kstep(tp1, 6) + (lane & 1)) * 128; \
      _Pragma("unroll") for (int j = 0; j < 4; ++j) QTOUCH(o_, j * ((W4 & 4) ? 16 : 32) * S * 2); \
    } \
    LOADW(); CHECK(); } while (0)
  if (gu_at < AM_AT) BURST1();
  if (!WD_XE && wd_at < 16) { UNTIL(wd_at + 1); LOADD(); CHECK(); }
  UNTIL(AM_AT);
  if (AM_SL) __builtin_amdgcn_s_sleep(AM_SL);
  const int tbo = (hw ? WS_CS + tq1 * 1024 : WS_STAT + tp1 * 1024) + lane * 16;
  key = 0;
#pragma unroll 1
  for (int spins = 0;; ++spins) {
    u32x4 am[4];
    unsigned bad = 0;
    asm volatile("buffer_load_dwordx4 %0, %5, %7, 0 offen sc1\n\ts_cmp_eq_u32 %8, 0\n\ts_cbranch_scc1 1f\n\t"
                 "buffer_load_dwordx4 %1, %6, %7, 0 offen sc1\n\tbuffer_load_dwordx4 %2, %6, %7, 0 offen offset:1024 sc1\n\t"
                 "buffer_load_dwordx4 %3, %6, %7, 0 offen offset:2048 sc1\n\tbuffer_load_dwordx4 %4, %6, %7, 0 offen offset:3072 sc1\n1:"
                 : "=&v"(tb), "=&v"(am[0]), "=&v"(am[1]), "=&v"(am[2]), "=&v"(am[3])
                 : "v"(tbo), "v"(WS_AMAX + lane * 16), "s"(rws), "s"(hw) : "memory", "scc");
    int left = 0;
    if (gu_at == 0x40 && spins == 0) { LOADW_(); left = 32; }
    WAITVM(left);
    asm volatile("" : "+v"(tb), "+v"(am[0]), "+v"(am[1]), "+v"(am[2]), "+v"(am[3]) :: "memory");
    if (hw) {
      key = 0;
#pragma unroll
      for (int q = 0; q < 4; ++q) { bad |= (am[q][1] ^ tag) | (am[q][3] ^ tag); if (am[q][0] > key) { key = am[q][0]; sg = am[q][2]; } }
    }
    bad |= (tb[1] ^ tag) | (tb[3] ^ tag);
    if (__all(bad == 0) || spins > SPIN_CAP / 64) break;
    if (nb < lim) JOIN();
  }
  STAMP(3);
  if (hw == 1) {
    *(LAS u32x2*)(lds + L_MB + 1024 + lane * 8) = u32x2{tb[0], tb[2]};
    const unsigned mk = wave_maxu(key);
    if (key == mk) { misc[3] = sg; misc[0] = min(0xffff - (int)(mk & 0xffffu), S - 1) + 1; }
    key = mk;
    if (b == 0 && lane == 0) __builtin_amdgcn_raw_buffer_store_b32(tag, rws, WS_EPOCH, 0, SC1);
  } else {
    *(LAS f32x2*)(lds + L_MB + lane * 8) = f32x2{-lo2f(tb[0]), -lo2f(tb[2])};
    *(LAS f32x2*)(lds + L_MB + 512 + lane * 8) = f32x2{__builtin_amdgcn_rcpf(hi2f(tb[0])), __builtin_amdgcn_rcpf(hi2f(tb[2]))};
  }
  WAIT_LGKM0();
  if (lane == 0) misc[4 + hw] = 1u;
  if (gu_at >= AM_AT && gu_at < 0x40) BURST1();
  lim = NBAR;
  UNTIL(XE_AT);
#pragma unroll 1
  for (int spins = 0; spins < SPIN_CAP && !rfl(misc[0]); ++spins) TICK();
  idx = (int)rfl(misc[0]) - 1;
  sg = misc[3];
  xe = __builtin_amdgcn_raw_buffer_load_b32(rx, (idx * D + (rg & 7) * 128) * 2 + lane * 4, 0, 0);
  CHECK();
  WAITVM(0);
  char* xsl = lds + L_XS + hw * 256;
  *(LAS unsigned*)(xsl + lane * 4) = ln_pair(xe, -lo2f(sg), __builtin_amdgcn_rcpf(hi2f(sg)));
  u32x4 xp[4];
#pragma unroll
  for (int i = 0; i < 4; ++i) xp[i] = *(const LAS u32x4*)(xsl + lq * 64 + i * 16);
  unsigned sel[4];
#pragma unroll
  for (int d = 0; d < 4; ++d) {
    const unsigned src = (lc & 2) ? 0x0302u : 0x0100u;
    sel[d] = (lc >> 2) == d ? ((lc & 1) ? (src << 16 | 0x0c0cu) : (0x0c0c0000u | src)) : 0x0c0c0c0cu;
  }
#define DIAG(v) BF8((u32x4{__builtin_amdgcn_perm(0u, v, sel[0]), __builtin_amdgcn_perm(0u, v, sel[1]), \
                           __builtin_amdgcn_perm(0u, v, sel[2]), __builtin_amdgcn_perm(0u, v, sel[3])}))
#define GEMV(w, n) f32x4 ae = {0.f, 0.f, 0.f, 0.f}, ao = ae; \
    __builtin_amdgcn_s_setprio(HPRIO); \
    _Pragma("unroll") for (int k = 0; k < (n); k += 2) { \
      const bf16x8 dg = DIAG(xp[k >> 3][(k >> 1) & 3]); \
      ae = MFMA(dg, BF8(w[k]), ae); ao = MFMA(dg, BF8(w[k + 1]), ao); \
      if ((k & 2) == 2) { asm volatile("" : "+v"(ae), "+v"(ao)); CHECK(); } } \
    __builtin_amdgcn_s_setprio(3); \
    const f32x2 a = {ae[0] + ao[2], ae[1] + ao[3]}
  const int tl = (lane & 3) * 16 + (lane >> 2);
  PIN4(xp[0], xp[1], xp[2], xp[3]);
  STAMP(7);
  CHECK();
  if (b < 224) {
    UNTIL(GV_AT);
    if (GV_SL) __builtin_amdgcn_s_sleep(GV_SL);
    GEMV(w, 32);
    const float c0 = __shfl(a[0], tl), c1 = __shfl(a[1], tl);
    __builtin_amdgcn_raw_buffer_store_b128(u32x4{__float_as_uint(c0), tag, __float_as_uint(c1), tag}, rws,
                                           WS_GU + ((rg * 2 + hw) * F + cg * 128) * 8 + lane * 16, 0, SC1);
  }
  STAMP(4);
  if (!WD_XE && wd_at >= 16) {
    UNTIL(wd_at + 1);
    __builtin_amdgcn_s_sleep(BSL2);
    LOADD();
    CHECK();
  }
  UNTIL(GG_AT);
  if (SKIP_ON & 64) __builtin_amdgcn_s_sleep(BSL2);
  {
    const int l56 = lane < 56 ? lane : 55, gi = l56 >= 28, pr = l56 - gi * 28;
    const int guo = WS_GU + (gi * F + fg * 112 + hw * 56 + pr * 2) * 8;
    float s0, s1;
#pragma unroll 1
    for (int spins = 0;; ++spins) {
#pragma unroll
      for (int k = 0; k < 8; ++k) w[k] = __builtin_amdgcn_raw_buffer_load_b128(rws, guo, k * 2 * F * 8, SC1);
      CHECK();
      WAITVM(0);
      unsigned bad = 0; s0 = 0.f; s1 = 0.f;
#pragma unroll
      for (int k = 0; k < 8; ++k) { bad |= (w[k][1] ^ tag) | (w[k][3] ^ tag); s0 += __uint_as_float(w[k][0]); s1 += __uint_as_float(w[k][2]); }
      if (__all(bad == 0) || spins > SPIN_CAP / 64) break;
      TICK(); TICK(); TICK(); TICK();
    }
    STAMP(8);
    const unsigned gp = pack2(s0, s1);
    const unsigned up = __shfl(gp, lane + 28);
    CHECK();
    const unsigned sl2 = pack2(silu_fast(lo2f(gp)), silu_fast(hi2f(gp)));
    const unsigned hp = pack2(lo2f(sl2) * lo2f(up), hi2f(sl2) * hi2f(up));
    const int q7 = lane / 7;
    if (lane < 28) *(LAS unsigned*)(xsl + q7 * 32 + (lane - q7 * 7) * 4) = hp;
    xp[0] = *(const LAS u32x4*)(xsl + lq * 32); xp[1] = *(const LAS u32x4*)(xsl + lq * 32 + 16);
    CHECK();
  }
  {
    GEMV(wd, 14);
    STAMP(5);
    if (hw == 1) {
      *(LAS f32x2*)(lds + L_CMB + lane * 8) = f32x2{a[0], a[1]};
      WAIT_LGKM0();
      if (lane == 0) misc[10] = 1u;
    } else {
#pragma unroll 1
      for (int spins = 0; spins < SPIN_CAP && !rfl(misc[10]); ++spins) TICK();
      const f32x2 o = *(const LAS f32x2*)(lds + L_CMB + lane * 8);
      const float c0 = __shfl(a[0] + o[0], tl), c1 = __shfl(a[1] + o[1], tl);
      __builtin_amdgcn_raw_buffer_store_b128(u32x4{__float_as_uint(c0), tag, __float_as_uint(c1), tag}, rws,
                                             WS_PART + (fg * D + eg * 128) * 8 + lane * 16, 0, SC1);
      STAMP(6);
    }
  }
  if (hw == 1 && (idx >> 7) == tp1) {
    const unsigned srt = key >> 16;
    const float r = lo2f((srt & 0x8000u) ? (srt & 0x7fffu) : (~srt & 0xffffu));
    const float rsel = rbf(1.0f / (1.0f + __expf(-r)));
    const int cp = lane & 31;
    const int e = te * 128 + (cp >> 4) * 64 + z * 32 + (cp & 15) * 2;
    const int po = WS_PART + ((lane >> 5) * D + e) * 8;
    float y0, y1;
#define FIN_AT 29
    UNTIL(FIN_AT);
#pragma unroll 1
    for (int spins = 0;; ++spins) {
#pragma unroll
      for (int k = 0; k < 16; ++k) w[k] = __builtin_amdgcn_raw_buffer_load_b128(rws, po, k * 2 * D * 8, SC1);
      CHECK();
      WAITVM(0);
      unsigned bad = 0; y0 = 0.f; y1 = 0.f;
#pragma unroll
      for (int k = 0; k < 16; ++k) { bad |= (w[k][1] ^ tag) | (w[k][3] ^ tag); y0 += __uint_as_float(w[k][0]); y1 += __uint_as_float(w[k][2]); }
      if (__all(bad == 0) || spins > SPIN_CAP / 64) break;
      TICK(); TICK(); TICK(); TICK();
    }
    y0 += __shfl_xor(y0, 32); y1 += __shfl_xor(y1, 32);
    if (lane < 32) *(LAS f32x2*)(lds + L_UPD + cp * 8) = f32x2{rbf(rbf(y0) * rsel), rbf(rbf(y1) * rsel)};
    WAIT_LGKM0();
    if (lane == 0) misc[1] = 1u;
    STAMP(6);
  }
  asm volatile("" : "+v"(tch));
  STAMP_OUT();
  WAITVM(0);
}

__global__ __launch_bounds__(512, 2) void fused(const u16* __restrict__ x, const u16* __restrict__ sre, const u16* __restrict__ sim,
                                                const u16* __restrict__ hre, const u16* __restrict__ him, const u16* __restrict__ wr,
                                                const u16* __restrict__ wg, const u16* __restrict__ wu, const u16* __restrict__ wdn,
                                                char* ws, u16* out) {
  extern __shared__ __attribute__((aligned(16))) char lds[];
  u64 pc;
  asm volatile("s_getpc_b64 %0" : "=s"(pc));
  const char* code = (const char*)(pc & ~255ull);
  const int wid = __builtin_amdgcn_readfirstlane(threadIdx.x >> 6), lane = threadIdx.x & 63;
  const int b = blockIdx.x, g = b & 7, i = b >> 3;
  const int tp1 = (g & 3) * 4 + ((i ^ GROT) & 3), tq1 = (g >> 2) * 8 + (i >> 2);
  const int z = tq1 >> 3, te = tq1 & 7;
  asm volatile("" :: "s"(x), "s"(sre), "s"(sim), "s"(hre), "s"(him), "s"(wr), "s"(wg), "s"(wu), "s"(wdn), "s"(ws), "s"(out));
  const rsrc_t rws = mk_rsrc(ws, WS_END);
  const u16* wtile = (z ? him : hre) + (size_t)te * 128 * D;
  if (__builtin_expect(wid == 4 || wid == 5, 1)) {
    const int L = wid - 4;
    loader(L, lane, (LAS char*)lds, L ? wtile : x + (size_t)tp1 * 128 * D, (z ? sim : sre) + (size_t)(tp1 & ~1) * 128 * S,
           ws + WS_ABT + (size_t)tq1 * ABT_ST, rws, WS_FLAG1 + tq1 * 2048, b, tp1, te STAMP_PASS);
  } else if (wid < 4) {
    consumer(wid, lane, b, lds, rws, mk_rsrc(out, S * D * 2), code, tp1, tq1 STAMP_PASS);
  } else {
    const int hw = wid - 6;
    helper(hw, lane, b, lds, rws, mk_rsrc(x, S * D * 2), wtile + (size_t)(tp1 * 8 + hw * 4) * D, wr, hw ? wu : wg, wdn,
           (z ? sim : sre) + (size_t)(tp1 & ~1) * 128 * S, tp1, tq1 STAMP_PASS);
  }
}

extern "C" void kernel_launch(void* const* d_in, const int* in_sizes, int n_in, void* d_out, int out_size,
                              void* d_ws, size_t ws_size, hipStream_t stream) {
  (void)hipFuncSetAttribute((const void*)fused, hipFuncAttributeMaxDynamicSharedMemorySize, LDS_BYTES);
  fused<<<256, 512, LDS_BYTES, stream>>>((const u16*)d_in[0], (const u16*)d_in[1], (const u16*)d_in[2], (const u16*)d_in[3],
                                         (const u16*)d_in[4], (const u16*)d_in[6], (const u16*)d_in[7], (const u16*)d_in[8],
                                         (const u16*)d_in[9], (char*)d_ws, (u16*)d_out);
}
```

```cpp
#define CODE_LINES 242
#include <hip/hip_runtime.h>
#include <hip/hip_bf16.h>
#include <stdint.h>

typedef __attribute__((ext_vector_type(8))) short bf16x8;
typedef __attribute__((ext_vector_type(4))) float f32x4;
typedef __attribute__((ext_vector_type(2))) float f32x2;
typedef __attribute__((ext_vector_type(4))) unsigned u32x4;
typedef __attribute__((ext_vector_type(2))) unsigned u32x2;
typedef __attribute__((ext_vector_type(2))) __bf16 bf16x2;
typedef unsigned short u16;
typedef unsigned long long u64;
typedef __amdgpu_buffer_rsrc_t rsrc_t;
#define LAS __attribute__((address_space(3)))

constexpr int S = 2048, D = 1024, F = 3584;
constexpr int UB = 16384;
constexpr int SLOT = 3 * UB;
constexpr int L_MISC = 9 * UB;
constexpr int L_UPD = L_MISC + 64;
constexpr int L_MB = L_MISC + 512;
constexpr int L_CMB = L_MB + 1536;
constexpr int LDS_BYTES = L_CMB + 512;
constexpr int NBAR = 17 + 16 + 1;
constexpr int SC1 = 16, NT = 2;

constexpr unsigned WS_EPOCH = 0;
constexpr unsigned WS_XP = 4096;
constexpr unsigned WS_WP = WS_XP + 16 * 8192;
constexpr unsigned WS_AMAX = WS_WP + 16 * 4096;
constexpr unsigned WS_FLAG1 = WS_AMAX + 32768;
constexpr unsigned WS_FLAG2 = WS_FLAG1 + 32768;
constexpr unsigned WS_GU = WS_FLAG2 + 32768;
constexpr unsigned WS_PART = WS_GU + 8 * 2 * F * 8;
constexpr unsigned WS_XCH = WS_PART + 32 * D * 8;
constexpr int XB = 32768 + 22528;
constexpr unsigned ABT_ST = 128 * S * 2;
constexpr unsigned WS_ABT = WS_XCH + 256 * XB;
constexpr unsigned WS_END = WS_ABT + 16 * ABT_ST;

#define STAMP(i)
#define HSTAMP()
#define HPRE()
#define HST(i)
#define STAMP_DECL
#define STAMP_ARG
#define STAMP_PASS
#define STAMP_OUT()

__device__ __forceinline__ float lo2f(unsigned v) { return __uint_as_float(v << 16); }
__device__ __forceinline__ float hi2f(unsigned v) { return __uint_as_float(v & 0xffff0000u); }
__device__ __forceinline__ unsigned pack2(float a, float b) {
  return __builtin_bit_cast(unsigned, __builtin_convertvector(f32x2{a, b}, bf16x2));
}
__device__ __forceinline__ float rbf(float f) { return lo2f(pack2(f, f)); }
__device__ __forceinline__ unsigned rfl(unsigned v) { return __builtin_amdgcn_readfirstlane(v); }
__device__ __forceinline__ rsrc_t mk_rsrc(const void* p, unsigned bytes) {
  return __builtin_amdgcn_make_buffer_rsrc((void*)p, 0, bytes, 0x00020000);
}
#define DPP_ADD(v, ctrl, rm, bc) v += __builtin_bit_cast(float, __builtin_amdgcn_update_dpp(0, __builtin_bit_cast(int, v), ctrl, rm, 0xf, bc))
#define DPP_MAXU(v, ctrl, rm, bc) v = max(v, (unsigned)__builtin_amdgcn_update_dpp(0, (int)v, ctrl, rm, 0xf, bc))
__device__ __forceinline__ unsigned wave_maxu(unsigned v) {
  DPP_MAXU(v, 0x111, 0xf, true); DPP_MAXU(v, 0x112, 0xf, true); DPP_MAXU(v, 0x114, 0xf, true); DPP_MAXU(v, 0x118, 0xf, true);
  DPP_MAXU(v, 0x142, 0xa, false); DPP_MAXU(v, 0x143, 0xc, false);
  return (unsigned)__builtin_amdgcn_readlane((int)v, 63);
}
template <int N> __device__ __forceinline__ void wait_vm() { asm volatile("s_waitcnt vmcnt(%0)" :: "n"(N) : "memory"); }
#define WAIT_LGKM0() asm volatile("s_waitcnt lgkmcnt(0)" ::: "memory")
#define BAR() do { asm volatile("" ::: "memory"); __builtin_amdgcn_s_barrier(); asm volatile("" ::: "memory"); } while (0)
#define MFMA(a, b, c) __builtin_amdgcn_mfma_f32_16x16x32_bf16(a, b, c, 0, 0, 0)
#define BF8(v) __builtin_bit_cast(bf16x8, v)
constexpr int SPIN_CAP = 1 << 20;
#define UP_T 0x0807060508070605ull
#define T1_T 0x0a0a09090a0a0909ull
#define GAD 1
#define NWA1 8
#define NWA 16
#define NWC 32
#define TM0 1
#define TM1 2
#define TMB 4
#define WB_AT 13
#define AM_AT 15
#define GV_SL 8
#define MB0 2
#define MBS 1
#define MBD 2
#define MBT 0x80402018ull
#define MBW 0xeeb740
#define BSL2 13
#define GGD 7
#define FIN_AT 34
#define EG 3
#define PEARLY 1
#define QL 0
__device__ __forceinline__ int sched(u64 tab, int te) { return (int)(tab >> (te * 8)) & 255; }
#define PIN4(a, b, c, d) asm volatile("" : "+v"(a), "+v"(b), "+v"(c), "+v"(d))
__device__ __forceinline__ unsigned vm_now() { const unsigned r = __builtin_amdgcn_s_getreg(7 | (31 << 11)); return (r & 15u) | ((r >> 18) & 0x30u); }
__device__ __forceinline__ unsigned mk_tag(unsigned epoch) { return rfl(epoch) * 0x9E3779B1u + 0x7F4A7C15u; }

template <int LD, int AUX, int MODE, int NP = 16>
__device__ __forceinline__ void issue(rsrc_t r, int voff, int koff, LAS char* dst) {
#pragma unroll
  for (int j = 0; j < NP; ++j) {
    const int so = MODE == 2 ? j * 1024 : MODE == 1 ? (64 * (j >> 3) + 32 * ((j >> 2) & 1) + 16 * (j & 1) + 4 * ((j >> 1) & 1)) * LD : j * 8 * LD;
    __builtin_amdgcn_raw_ptr_buffer_load_lds(r, (LAS void*)(dst + j * 1024), 16, voff, koff + so, 0, AUX);
  }
}
__device__ __forceinline__ unsigned ktab(int tp1) {
  const int j = tp1 & 3;
  return (tp1 * 0x01010101u ^ 0x03020100u) | (((tp1 & 12) ^ 4) * 0x10101010u ^ (j == 1 ? 0x30002010u : j == 2 ? 0x30100020u : 0x30201000u));
}
__device__ __forceinline__ int kstep(unsigned kt, int n) { return (int)(kt >> (n >> 1) * 4 & 15) * 2 + (n & 1); }
constexpr int QROWS = 1024 + 128;

__device__ __forceinline__ void loader(int L, int lane, LAS char* lds, const u16* g1, const u16* gq, const char* gp, rsrc_t rws,
                                       int flag_off, int b, int tp1, int te STAMP_ARG) {
  STAMP_DECL;
  __builtin_amdgcn_s_setprio(2);
  const int rsub = lane >> 3, ch = ((lane & 7) ^ rsub) * 16;
  {
    const rsrc_t r1 = mk_rsrc(g1, 128 * D * 2);
    const int v1 = (8 * (rsub >> 2) + (rsub & 3)) * (D * 2) + ch;
    const unsigned utab = L ? 0x8541u : 0x7230u;
    STAMP(1);
#pragma unroll 1
    for (int t = 0; t < 16; ++t) {
      int ko = ((t + 4 * (tp1 + te)) & 15) * 128;
      LAS char* dst = lds + ((utab >> ((t & 3) * 4)) & 15) * UB;
      asm volatile("" : "+s"(ko), "+s"(dst));
      if (__builtin_expect(t > 2, 1)) { wait_vm<32>(); BAR(); }
      issue<D * 2, 0, 1>(r1, v1, ko, dst);
    }
  }
  const rsrc_t rq = mk_rsrc(gq, QROWS * S * 2);
  const int h = tp1 >> 3;
  const unsigned kt = ktab(tp1);
  const int vq = rsub * (S * 2) + ch, vp = lane * 16;
  const int qa = h * 1024 * (S * 2), qb = ((1 - h) * 1024 + L * 64) * (S * 2);
  LAS char* const dqb = lds + UB + L * 8192;
  volatile LAS unsigned* misc = (volatile LAS unsigned*)(lds + L_MISC);
  unsigned fl = 0, tag;
  asm volatile("ds_read_b32 %0, %1\n\ts_waitcnt vmcnt(32) lgkmcnt(0)" : "=v"(tag) : "v"(L_MISC + 8) : "memory");
  tag = rfl(tag);
  BAR();
  int sl = 0;
#define POLL(o) asm volatile("buffer_load_dword %0, %1, %2, 0 offen sc1" : "+v"(fl) : "v"(o), "s"(rws) : "memory")
#define POLLW(o) asm volatile("buffer_load_dword %0, %1, %2, 0 offen sc1\n\ts_waitcnt vmcnt(0)" : "+v"(fl) : "v"(o), "s"(rws) : "memory")
  if (L) {
#pragma unroll 1
    for (int n = 0; n < 16; ++n) {
      const int k_ = kstep(kt, n) * 128;
      int ka = qa + (n == 15 ? k_ ^ 2048 : k_), kb = (n == 15 ? qa + 64 * (S * 2) : qb) + k_;
      asm volatile("" : "+s"(ka), "+s"(kb), "+s"(sl));
      if (__builtin_expect(n > 3, 1)) { wait_vm<24>(); BAR(); }
      else if (n == 1) { wait_vm<40>(); BAR(); }
      else if (n == 2) { wait_vm<48>(); BAR(); BAR(); }
      else if (n == 3) { wait_vm<48>(); BAR(); wait_vm<24>(); BAR(); }
      issue<S * 2, 0, 0>(rq, vq, ka, lds + sl);
      issue<S * 2, 0, 0, 8>(rq, vq, kb, dqb + sl);
      sl = sl == 2 * SLOT ? 0 : sl + SLOT;
    }
    const int f2o = WS_FLAG2 + (b ^ 4) * 128 + 16 + (lane & 3) * 4;
    wait_vm<24>();
    BAR();
    wait_vm<0>();
    BAR();
    BAR();
#pragma unroll 1
    for (int spins = 0; spins < SPIN_CAP; ++spins) {
      POLLW(f2o);
      if (__all(fl == tag)) break;
      __builtin_amdgcn_s_sleep(1);
    }
    STAMP(4);
    issue<0, SC1, 2, 22>(rws, vp, WS_XCH + (b ^ 4) * XB + 32768, lds + 6 * UB);
    wait_vm<0>();
    misc[12] = 1u;
    STAMP(5);
  } else {
#define QHALF(n, sl) issue<S * 2, 0, 0, 8>(rq, vq, qb + kstep(kt, n) * 128, dqb + (sl))
    const rsrc_t rp = mk_rsrc(gp, 128 * S * 2);
    u64 mask = ~(0xffffffffull << 32 * h | 0xc0000000cull << (kt >> 26 & 28));
    const int fo = flag_off + (lane >> 2) * 128 + (lane & 3) * 4;
    const int fo1 = flag_off + (kstep(kt, 2) >> 1) * 128 + (lane & 3) * 4;
    if (QL == 0) QHALF(0, 0);
    wait_vm<QL == 0 ? 24 : 16>();
    BAR();
    if (QL < 2) QHALF(1 - QL, (1 - QL) * SLOT);
    wait_vm<16 - 8 * QL>();
    BAR();
    if (QL > 0) QHALF(2 - QL, (2 - QL) * SLOT);
    BAR();
    if (QL < 2) QHALF(2, 2 * SLOT); else QHALF(1, SLOT);
    wait_vm<QL < 2 ? 16 : 8>();
    BAR();
    if (QL == 2) QHALF(2, 2 * SLOT);
    bool p2 = false;
    if (PEARLY) {
      POLLW(fo1);
      p2 = __all(fl == tag);
      if (p2) issue<0, SC1, 2>(rp, vp, kstep(kt, 2) * UB, lds + 2 * SLOT + 2 * UB);
    } else wait_vm<8>();
    BAR();
    QHALF(3, 0);
    STAMP(2);
    if (!p2) {
      unsigned f1, f2;
      asm volatile("buffer_load_dword %0, %2, %3, 0 offen sc1\n\ts_sleep 3\n\tbuffer_load_dword %1, %2, %3, 0 offen sc1\n\ts_sleep 3"
                   : "=&v"(f1), "=&v"(f2) : "v"(fo1), "s"(rws) : "memory");
#pragma unroll 1
      for (int spins = 0; spins < SPIN_CAP; ++spins) {
        asm volatile("buffer_load_dword %0, %2, %3, 0 offen sc1\n\ts_waitcnt vmcnt(2)" : "+v"(fl), "+v"(f1) : "v"(fo1), "s"(rws) : "memory");
        if (__all(f1 == tag)) break;
        asm volatile("buffer_load_dword %0, %2, %3, 0 offen sc1\n\ts_waitcnt vmcnt(2)" : "+v"(f1), "+v"(f2) : "v"(fo1), "s"(rws) : "memory");
        if (__all(f2 == tag)) break;
        asm volatile("buffer_load_dword %0, %2, %3, 0 offen sc1\n\ts_waitcnt vmcnt(2)" : "+v"(f2), "+v"(fl) : "v"(fo1), "s"(rws) : "memory");
        if (__all(fl == tag)) break;
      }
      issue<0, SC1, 2>(rp, vp, kstep(kt, 2) * UB, lds + 2 * SLOT + 2 * UB);
      asm volatile("s_waitcnt vmcnt(16)" : "+v"(fl), "+v"(f1), "+v"(f2) :: "memory");
    }
    STAMP(3);
    POLL(fo);
    issue<0, SC1, 2>(rp, vp, kstep(kt, 3) * UB, lds + 2 * UB);
    asm volatile("s_waitcnt vmcnt(16)" : "+v"(fl) :: "memory");
#define NEED(need) do { if (__builtin_expect(mask != ~0ull, 0)) { asm volatile("" : "+v"(fl) :: "memory"); mask |= __ballot(fl == tag); \
      _Pragma("unroll 1") for (int spins = 0; (mask & (need)) != (need) && spins < SPIN_CAP; ++spins) { POLLW(fo); mask |= __ballot(fl == tag); } \
      if (mask != ~0ull) POLL(fo); } } while (0)
    sl = SLOT;
#pragma unroll 1
    for (int n = 4; n < 16; ++n) {
      const int k_ = kstep(kt, n);
      int kp = n == 15 ? k_ ^ 16 : k_, kb = (n == 15 ? qa : qb) + k_ * 128;
      asm volatile("" : "+s"(kp), "+s"(kb), "+s"(sl));
      wait_vm<24>();
      BAR();
      NEED(n == 15 ? ~0ull : 3ull << (2 * k_));
      issue<0, SC1, 2>(rp, vp, kp * UB, lds + sl + 2 * UB);
      issue<S * 2, 0, 0, 8>(rq, vq, kb, dqb + sl);
      sl = sl == 2 * SLOT ? 0 : sl + SLOT;
    }
    wait_vm<24>();
    BAR();
    issue<0, SC1, 2>(rp, vp, kstep(kt, 15) * UB, lds + 5 * UB);
    const int f2o = WS_FLAG2 + (b ^ 2 ^ (lane & 4)) * 128 + (lane & 3) * 4;
    wait_vm<0>();
    BAR();
    BAR();
    unsigned got = 0;
#pragma unroll 1
    for (int spins = 0; spins < SPIN_CAP && got != 3; ++spins) {
      POLLW(f2o);
      const unsigned m_ = (unsigned)__ballot(fl == tag);
      const unsigned g_ = (((m_ & 15u) == 15u) | ((m_ & 0xf0u) == 0xf0u) << 1) & ~got;
#pragma unroll 1
      for (int i = 0; i < 2; ++i)
        if (g_ >> i & 1) issue<0, SC1, 2>(rws, vp, WS_XCH + (b ^ 2 ^ 4 * i) * XB + i * 16384, lds + (3 + i) * UB);
      got |= g_;
      if (!g_) __builtin_amdgcn_s_sleep(1);
    }
    STAMP(4);
    wait_vm<0>();
    misc[13] = 1u;
    STAMP(5);
  }
  STAMP_OUT();
}

#define LDF(off) (*reinterpret_cast<const bf16x8*>(lds + (off)))
struct Frag { bf16x8 p[4], q[4]; };
__device__ __forceinline__ void mma(f32x4 (&acc)[4][4], const Frag& f) {
#pragma unroll
  for (int i = 0; i < 4; ++i)
#pragma unroll
    for (int j = 0; j < 4; ++j) acc[i][j] = MFMA(f.p[i], f.q[j], acc[i][j]);
}
#define SGB(m, n) __builtin_amdgcn_sched_group_barrier(m, n, 0)
__device__ __forceinline__ float silu_fast(float z) { return z * __builtin_amdgcn_rcpf(1.0f + __expf(-z)); }
__device__ __forceinline__ u32x4 pack8(const f32x4& a, const f32x4& c) {
  return u32x4{pack2(a[0], a[1]), pack2(a[2], a[3]), pack2(c[0], c[1]), pack2(c[2], c[3])};
}
__device__ __forceinline__ unsigned ln_pair(unsigned x, float nmb, float rden) {
  const unsigned d = pack2(lo2f(x) + nmb, hi2f(x) + nmb);
  return pack2(lo2f(d) * rden, hi2f(d) * rden);
}

template <bool BARRIER, bool JOBNEXT, bool ZERO, class X, class Y>
__device__ __forceinline__ void big_half(f32x4 (&a0)[4][4], f32x4 (&a1)[4][4], const bf16x8 (&pc)[4], bf16x8 (&pn)[4], bf16x8 (&q)[8],
                                         const char* lds, int qc, int pa, int qn, int qn2, X extra, Y inner) {
  const f32x4 zero = {0.f, 0.f, 0.f, 0.f};
#pragma unroll
  for (int g = 0; g < 8; ++g) {
    if (g == 0) { q[5] = LDF(qc + UB + 2048); q[6] = LDF(qc + UB + 4096); }
    if (g == 1) q[7] = LDF(qc + UB + 6144);
    if (g == 3) { pn[0] = LDF(pa); pn[1] = LDF(pa + 2048); }
    if (g == 4) { pn[2] = LDF(pa + (JOBNEXT ? 1024 : 4096)); pn[3] = LDF(pa + (JOBNEXT ? 3072 : 6144)); }
    if (g == 5) { q[0] = LDF(qn); q[1] = LDF(qn + 2048); }
    if (g == 6) { q[2] = LDF(qn + 4096); q[3] = LDF(qn + 6144); }
    if (g == 7) { q[4] = LDF(qn2); if (JOBNEXT) q[5] = LDF(qn2 + 2048); }
#pragma unroll
    for (int i = 0; i < 4; ++i) {
      const int ii = (g & 1) ? 3 - i : i;
      if (g < 4) a0[ii][g] = MFMA(pc[ii], q[g], ZERO ? zero : a0[ii][g]); else a1[ii][g - 4] = MFMA(pc[ii], q[g], ZERO ? zero : a1[ii][g - 4]);
    }
    inner(g);
    if (JOBNEXT) { SGB(0x8, 1); SGB(0x100, 1); SGB(0x2, 2); SGB(0x8, 1); SGB(0x100, 1); SGB(0x2, 2); SGB(0x40, 1); SGB(0x8, 1); SGB(0x2, 2); SGB(0x8, 1); SGB(0x2, 2); SGB(0x40, 1); }
    else { SGB(0x8, 1); SGB(0x100, 1); SGB(0x8, 1); SGB(0x100, 1); SGB(0x8, 2); }
    __builtin_amdgcn_sched_barrier(0);
    if (g == 2 && BARRIER) { WAIT_LGKM0(); BAR(); }
    extra(g);
  }
}

__device__ __forceinline__ void consumer(int wid, int lane, int b, char* lds, rsrc_t rws, rsrc_t rout,
                                         const char* code, int tp1, int tq1 STAMP_ARG) {
  STAMP_DECL;
  const int z = tq1 >> 3, te = tq1 & 7;
  const int wp = wid >> 1, wq = wid & 1, fr = lane & 15, fq = lane >> 4;
  const int c0 = (fq ^ (fr & 7)) * 16, c1 = ((fq + 4) ^ (fr & 7)) * 16;
  int q0 = (wq * 64 + fr) * 128 + c0, q1 = (wq * 64 + fr) * 128 + c1;
  volatile LAS unsigned* misc = (volatile LAS unsigned*)(lds + L_MISC);
  f32x4 acc[4][4];
#pragma unroll
  for (int i = 0; i < 4; ++i)
#pragma unroll
    for (int j = 0; j < 4; ++j) acc[i][j] = f32x4{0.f, 0.f, 0.f, 0.f};
  unsigned tag;
  u32x4 xv[4];
  {
    Frag f0, f1;
    const int xrow = wq * 64 + 16 * ((fr >> 2) & 1) + 4 * (fr >> 3) + (fr & 3);
    const int xr = xrow * 128 + (((z * 4 + fq) ^ (xrow & 7)) * 16);
#define XV(u) do { _Pragma("unroll") for (int k = 0; k < 4; ++k) \
      xv[k] = *reinterpret_cast<const u32x4*>(lds + (u) * UB + xr + (k >> 1) * 4096 + (k & 1) * 1024); } while (0)
    const int p0 = (wp * 64 + fr) * 128 + c0, p1 = (wp * 64 + fr) * 128 + c1;
    unsigned tch = 0;
    if (wid == 3 && lane < (CODE_LINES + 7) / 8) {
      __builtin_amdgcn_s_sleep(8);
      const int co = min(((b >> 3) & 7) * ((CODE_LINES + 7) / 8) + lane, CODE_LINES - 1) * 128;
      asm volatile("global_load_dword %0, %1, off" : "+v"(tch) : "v"(code + co) : "memory");
    }
    const int xt = (2 * te + wp - 4 * (tp1 + te)) & 15;
#define G1H(fc, fn, po, qo, LOAD) do { _Pragma("unroll") for (int g = 0; g < 8; ++g) { \
      if (LOAD) { if (g == 0) { fn.q[0] = LDF(qo); fn.p[0] = LDF(po); } else if (g == 1) { fn.p[1] = LDF((po) + 2048); fn.p[2] = LDF((po) + 4096); } \
        else if (g == 2) { fn.p[3] = LDF((po) + 6144); fn.q[1] = LDF((qo) + 2048); } else if (g == 3) { fn.q[2] = LDF((qo) + 4096); fn.q[3] = LDF((qo) + 6144); } } \
      _Pragma("unroll") for (int e = 0; e < 2; ++e) { const int i0 = (g & 1) * 2 + e, ii = (g & 2) ? 3 - i0 : i0; \
        acc[ii][g >> 1] = MFMA(fc.p[ii], fc.q[g >> 1], acc[ii][g >> 1]); } \
      SGB(0x8, 1); SGB(0x100, 1); SGB(0x8, 1); SGB(0x100, 2); __builtin_amdgcn_sched_barrier(0); } } while (0)
#define XI(u) ((u) == 0 ? 0 : (u) == 1 ? 3 : (u) == 2 ? 2 : 7)
#define WI(u) ((u) == 0 ? 1 : (u) == 1 ? 4 : (u) == 2 ? 5 : 8)
#define XVC(t, u) do { if (__builtin_expect((t) == xt, 0)) { XV(XI(u)); PIN4(xv[0], xv[1], xv[2], xv[3]); } __builtin_amdgcn_sched_barrier(0); } while (0)
    int pA[2] = {p0, p1}, pB[2] = {p0 + 7 * UB, p1 + 7 * UB}, qA[2] = {q0 + UB, q1 + UB}, qB[2] = {q0 + 5 * UB, q1 + 5 * UB};
    PIN4(pA[0], pA[1], pB[0], pB[1]); PIN4(qA[0], qA[1], qB[0], qB[1]);
#define XA(u, h) ((u) == 3 ? pB[h] : pA[h] + XI(u) * UB)
#define WA(u, h) ((u) < 2 ? qA[h] + (u) * 3 * UB : qB[h] + ((u) - 2) * 3 * UB)
    BAR();
    XVC(0, 0);
    f0.q[0] = LDF(WA(0, 0));
#pragma unroll
    for (int i = 0; i < 4; ++i) f0.p[i] = LDF(XA(0, 0) + i * 2048);
#pragma unroll
    for (int i = 1; i < 4; ++i) f0.q[i] = LDF(WA(0, 0) + i * 2048);
    __builtin_amdgcn_sched_barrier(0);
    u32x2 rdy;
#pragma unroll 1
    for (int t = 0;; t += 4) {
#pragma unroll
      for (int u = 0; u < 4; ++u) {
        if (u == 3) rdy = *(const volatile LAS u32x2*)(lds + L_MISC + 16);
        G1H(f0, f1, XA(u, 1), WA(u, 1), true);
        WAIT_LGKM0();
        BAR();
        if (u == 3 && t == 12) goto g1_done;
        XVC(t + u + 1, (u + 1) & 3);
        G1H(f1, f0, XA((u + 1) & 3, 0), WA((u + 1) & 3, 0), true);
      }
    }
g1_done:
    asm volatile("s_waitcnt vmcnt(0)" : "+v"(tch) :: "memory");
    if (!rfl(rdy[0] & rdy[1])) {
#pragma unroll 1
      for (int spins = 0; spins < SPIN_CAP && !rfl(misc[4] & misc[5]); ++spins) __builtin_amdgcn_s_sleep(1);
    }
    asm volatile("" ::: "memory");
    f32x4 mb[2][2], rd[2][2]; float cs[4];
    const unsigned tagv = *(const LAS unsigned*)(lds + L_MISC + 8);
#pragma unroll
    for (int m = 0; m < 2; ++m)
#pragma unroll
      for (int h = 0; h < 2; ++h) {
        mb[m][h] = *reinterpret_cast<const f32x4*>(lds + L_MB + (wp * 64 + m * 32 + fq * 8 + h * 4) * 4);
        rd[m][h] = *reinterpret_cast<const f32x4*>(lds + L_MB + 512 + (wp * 64 + m * 32 + fq * 8 + h * 4) * 4);
      }
#pragma unroll
    for (int j = 0; j < 4; ++j)
      cs[j] = *reinterpret_cast<const float*>(lds + L_MB + 1024 + (wq * 64 + 32 * (j >> 1) + 8 * (fr >> 2) + 4 * (j & 1) + (fr & 3)) * 4);
    mma(acc, f1);
#pragma unroll
    for (int q_ = 0; q_ < 13; ++q_) { SGB(0x100, 1); SGB(0x8, 1); }
    __builtin_amdgcn_sched_barrier(0);
    STAMP(2);
    tag = rfl(tagv);
    const float sgn = z ? -1.f : 1.f;
    const int flip = z ? 0 : 4096;
    const int ao = WS_ABT + tq1 * ABT_ST + ((tp1 * 2 + wp) * 8 + wq * 4) * 2048 + lane * 16;
    char* own = lds + wp * SLOT + 2 * UB + wq * 8192 + lane * 16;
#pragma unroll
    for (int m = 0; m < 2; ++m) {
#pragma unroll
      for (int h = 0; h < 2; ++h) rd[m][h] *= sgn;
#pragma unroll
      for (int j = 0; j < 4; ++j) {
        const f32x4 v0 = (mb[m][0] * cs[j] + acc[2 * m][j]) * rd[m][0], v1 = (mb[m][1] * cs[j] + acc[2 * m + 1][j]) * rd[m][1];
        const u32x4 o = pack8(v0, v1);
        const int fo = (j < 2 ? flip : -flip) + j * 2048 + m * 1024;
        __builtin_amdgcn_raw_buffer_store_b128(o, rws, ao + fo, 0, SC1);
        *reinterpret_cast<u32x4*>(own + fo) = o;
      }
    }
    STAMP(3);
  }
  f32x4 acb[4][4];
  bf16x8 pa[4], pb[4], q[8];
  int pp = wp * 8192 + lane * 16;
  bool sent = false;
#define FLAG(off, n) do { if (!sent && vm_now() <= (n)) { sent = true; wait_vm<n>(); \
    if (lane == 0) __builtin_amdgcn_raw_buffer_store_b32(tag, rws, off, 0, SC1); STAMP(FST); } } while (0)
#define FST 4
  const int f1o = WS_FLAG1 + (tq1 * 16 + tp1) * 128 + wid * 4;
  auto flag1 = [&](int g) { if (g & 1) FLAG(f1o, 0); };
  WAIT_LGKM0();
  BAR();
  STAMP(5);
  pa[0] = LDF(2 * UB + pp); q[0] = LDF(q0); pa[1] = LDF(2 * UB + pp + 2048); pa[2] = LDF(2 * UB + pp + 4096); pa[3] = LDF(2 * UB + pp + 6144);
  q[1] = LDF(q0 + 2048); q[2] = LDF(q0 + 4096); q[3] = LDF(q0 + 6144); q[4] = LDF(q0 + UB);
  __builtin_amdgcn_sched_barrier(0);
  auto none = [](int) {};
  big_half<false, false, true>(acc, acb, pa, pb, q, lds, q0, 2 * UB + pp + 1024, q1, q1 + UB, flag1, none);
  big_half<true, false, false>(acc, acb, pb, pa, q, lds, q1, SLOT + 2 * UB + pp, SLOT + q0, SLOT + q0 + UB, flag1, none);
  big_half<false, false, false>(acc, acb, pa, pb, q, lds, SLOT + q0, SLOT + 2 * UB + pp + 1024, SLOT + q1, SLOT + q1 + UB, flag1, none);
  if (!sent) { wait_vm<0>(); FLAG(f1o, 0); }
  PIN4(q0, q1, pp, lane);
  {
    int sl = SLOT;
#pragma unroll 1
    for (int n = 1; n < 14; ++n) {
      const int sn = sl == 2 * SLOT ? 0 : sl + SLOT, sw = n == 13 ? UB : 0;
      big_half<true, false, false>(acc, acb, pb, pa, q, lds, sl + q1, sn + 2 * UB + pp, sn + q0, sn + q0 + UB, none, none);
      big_half<false, false, false>(acc, acb, pa, pb, q, lds, sn + q0, sn + 2 * UB + pp + 1024, sn + sw + q1, sn + UB - sw + q1, none, none);
      sl = sn;
    }
  }
  int xw = WS_XCH + b * XB + wid * 4096 + lane * 16;
  const int xl = WS_XCH + 32768 + wid * 5632 + lane * 16;
  asm volatile("" : "+v"(xw));
#define SEND_E(j) __builtin_amdgcn_raw_buffer_store_b128(pack8(acb[(j) & 1 ? 0 : 2][(j) >> 1], acb[(j) & 1 ? 1 : 3][(j) >> 1]), rws, xw + ((j) & 1) * 16384 + ((j) >> 1) * 1024, 0, SC1)
  auto send_e = [&](int g) { if (g >= EG && g < EG + 4) { SEND_E(2 * (g - EG) & 7); SEND_E(2 * (g - EG) + 1 & 7); } };
  big_half<true, true, false>(acb, acc, pb, pa, q, lds, 2 * SLOT - UB + q1, 2 * UB + pp, q0, q1, none, send_e);
  STAMP(10);
  const int f2o = WS_FLAG2 + b * 128 + wid * 4;
#define PU(k) ((k) == 0 ? 2 : 5)
#define QU(k) (k)
#define JOB(KP, K, I0, pc, pn, KN, IN, EXTRA) do { _Pragma("unroll") for (int g = 0; g < 8; ++g) { \
    if (g < 2 && (KP) != (K)) q[g + 6] = LDF(QU(K) * UB + q1 + (g + 2) * 2048); \
    if ((KN) >= 0) { \
      if (g >= 2 && (KN) != (K)) q[g - 2] = LDF(QU(KN) * UB + (g < 6 ? q0 + (g - 2) * 2048 : q1 + (g - 6) * 2048)); \
      if (g < 4) pn[g] = LDF(PU(KN) * UB + pp + ((IN) + (g & 1)) * 2048 + (g >> 1) * 1024); } \
    acc[I0][g & 3] = MFMA(pc[(g >> 2) * 2], q[g], acc[I0][g & 3]); \
    acc[(I0) + 1][g & 3] = MFMA(pc[(g >> 2) * 2 + 1], q[g], acc[(I0) + 1][g & 3]); \
    EXTRA(g); \
    SGB(0x8, 1); SGB(0x100, 2); SGB(0x8, 1); \
    __builtin_amdgcn_sched_barrier(0); } } while (0)
#define X_NONE(g)
  u32x4 e1[4], e2[4], ot[5]; u32x2 o5;
#define LT(i) pack2(acc[((i) >> 1) & 1][((i) >> 2) & 3][((i) & 1) * 2], acc[((i) >> 1) & 1][((i) >> 2) & 3][((i) & 1) * 2 + 1])
#define X_LATE(g) if ((g) < 5) __builtin_amdgcn_raw_buffer_store_b128(u32x4{LT(3 * (g)), LT(3 * (g) + 1), LT(3 * (g) + 2), tag}, rws, xl + b * XB + (g) * 1024, 0, SC1); \
  else if ((g) == 5) __builtin_amdgcn_raw_buffer_store_b64(u32x2{LT(15), tag}, rws, xl + b * XB + 5120 - lane * 8, 0, SC1); \
  else if ((g) == 6) __builtin_amdgcn_raw_buffer_store_b32(tag, rws, lane ? -1 : f2o + 16, 0, SC1)
#undef FST
#define FST 9
  sent = false;
  JOB(-1, 0, 0, pa, pb, 1, 0, X_NONE);
  JOB(0, 1, 0, pb, pa, 1, 2, X_NONE);
  FLAG(f2o, 0);
  JOB(1, 1, 2, pa, pb, 0, 2, X_LATE);
  BAR();
  FLAG(f2o, 7);
  float tm[4], tr[4]; unsigned selv;
  const char* tb = lds + L_MB + (wq * 64 + (lane & 15)) * 4;
#define X_TAB(g) if ((g) >= 2 && (g) < 6) { tm[((g) - 2) & 3] = *reinterpret_cast<const float*>(tb + ((g) - 2) * 64); tr[((g) - 2) & 3] = *reinterpret_cast<const float*>(tb + 512 + ((g) - 2) * 64); } \
  else if ((g) == 6) selv = misc[0]
  JOB(1, 0, 2, pb, pa, -1, 0, X_TAB);
#define FLAGU() FLAG(f2o, 7)
  FLAGU();
  STAMP(6);
#define RECV(v, n, off) do { _Pragma("unroll") for (int k_ = 0; k_ < (n); ++k_) \
    v[k_] = __builtin_amdgcn_raw_buffer_load_b128(rws, (off) + (k_ & 3) * 1024, (k_ >> 2) * 4096, SC1); asm volatile("" ::: "memory"); } while (0)
#define RECV_LATE() do { o5 = __builtin_amdgcn_raw_buffer_load_b64(rws, xl + (b ^ 4) * XB + 5120 - lane * 8, 0, SC1); RECV(ot, 5, xl + (b ^ 4) * XB); } while (0)
  f32x2 gf[4][4];
#pragma unroll
  for (int k = 0; k < 4; ++k) {
    const float tn = tm[k] * tr[k];
#pragma unroll
    for (int c = 0; c < 4; ++c) {
      const f32x2 n = f32x2{lo2f(xv[k][c]), hi2f(xv[k][c])} * tr[k] + tn, a = n * -1.44269504f;
      const f32x2 d = f32x2{__builtin_amdgcn_exp2f(a[0]), __builtin_amdgcn_exp2f(a[1])} + 1.0f;
      gf[k][c] = n * f32x2{__builtin_amdgcn_rcpf(d[0]), __builtin_amdgcn_rcpf(d[1])};
    }
    PIN4(gf[k][0], gf[k][1], gf[k][2], gf[k][3]);
    if (k < 3) FLAGU();
  }
  if (__builtin_expect(!sent, 0)) { wait_vm<7>(); FLAG(f2o, 7); }
  STAMP(1);
#pragma unroll 1
  for (int spins = 0; spins < SPIN_CAP && !rfl(misc[12]); ++spins) ;
  asm volatile("" ::: "memory");
  STAMP(7);
  {
    const char* ll = lds + 6 * UB + wid * 5632 + lane * 16;
#pragma unroll
    for (int j = 0; j < 5; ++j) ot[j] = *reinterpret_cast<const u32x4*>(ll + j * 1024);
    o5 = *reinterpret_cast<const u32x2*>(ll + 5120 - lane * 8);
  }
#define LBAD() (!__all(((ot[0][3] ^ tag) | (ot[1][3] ^ tag) | (ot[2][3] ^ tag) | (ot[3][3] ^ tag) | (ot[4][3] ^ tag) | (o5[1] ^ tag)) == 0))
  if (__builtin_expect(LBAD(), 0)) {
#pragma unroll 1
    for (int spins = 0; spins < SPIN_CAP / 16; ++spins) {
      asm volatile("" ::: "memory");
      RECV_LATE();
      if (!LBAD()) break;
    }
    asm volatile("" : "+v"(ot[0]), "+v"(ot[1]), "+v"(ot[2]), "+v"(ot[3]), "+v"(ot[4]), "+v"(o5));
  }
  f32x2 sv[4][4], xf[4][4];
#pragma unroll
  for (int k = 0; k < 4; ++k) {
#pragma unroll
    for (int c = 0; c < 4; ++c) {
      const unsigned a3 = 4 * k + c < 15 ? ot[(4 * k + c) / 3 % 5][(4 * k + c) % 3] : o5[0];
      sv[k][c] = f32x2{acc[2 + (c >> 1)][k][(c & 1) * 2], acc[2 + (c >> 1)][k][(c & 1) * 2 + 1]} + f32x2{lo2f(a3), hi2f(a3)};
      xf[k][c] = f32x2{lo2f(xv[k][c]), hi2f(xv[k][c])};
    }
    PIN4(sv[k][0], sv[k][1], sv[k][2], sv[k][3]);
    PIN4(xf[k][0], xf[k][1], xf[k][2], xf[k][3]);
  }
  {
    unsigned sel = rfl(selv);
    if (__builtin_expect(!sel || (int)(sel - 1 >> 6) == tp1 * 2 + wq, 0)) {
#pragma unroll 1
      for (int spins = 0; spins < SPIN_CAP && !sel; ++spins) { sel = rfl(misc[0]); __builtin_amdgcn_s_sleep(2); }
      const int idx = (int)sel - 1;
      if ((idx >> 6) == tp1 * 2 + wq) {
        const int ks = (idx >> 4) & 3;
        const u32x4 xs = ks == 0 ? xv[0] : ks == 1 ? xv[1] : ks == 2 ? xv[2] : xv[3];
        const float ms = ks == 0 ? tm[0] : ks == 1 ? tm[1] : ks == 2 ? tm[2] : tm[3], rs = ks == 0 ? tr[0] : ks == 1 ? tr[1] : ks == 2 ? tr[2] : tr[3];
        u32x4 n;
#pragma unroll
        for (int c = 0; c < 4; ++c) n[c] = ln_pair(xs[c], ms, rs);
        asm volatile("" : "+v"(n));
#pragma unroll 1
        for (int spins = 0; spins < SPIN_CAP && !rfl(misc[1]); ++spins) __builtin_amdgcn_s_sleep(1);
        asm volatile("" ::: "memory");
        const LAS float* upd = (const LAS float*)(lds + L_UPD) + wp * 32 + (lane >> 4) * 8;
#pragma unroll
        for (int c = 0; c < 4; ++c) {
          const unsigned u = pack2(lo2f(n[c]) + upd[2 * c], hi2f(n[c]) + upd[2 * c + 1]);
          const f32x2 gn = {silu_fast(lo2f(u)), silu_fast(hi2f(u))};
#pragma unroll
          for (int k = 0; k < 4; ++k) if (ks == k && (idx & 15) == (lane & 15)) gf[k][c] = gn;
        }
      }
    }
  }
  int xo = ((tp1 * 128 + wq * 64 + (lane & 15)) * D + te * 128 + wp * 64 + z * 32 + (lane >> 4) * 8) * 2, le = 3 * UB + wid * 4096 + lane * 16;
  asm volatile("" : "+v"(xo), "+v"(le));
#pragma unroll 1
  for (int spins = 0; spins < SPIN_CAP && !rfl(misc[13]); ++spins) ;
  asm volatile("" ::: "memory");
  STAMP(11);
#pragma unroll
  for (int k = 0; k < 4; ++k) { e1[k] = *reinterpret_cast<const u32x4*>(lds + le + k * 1024); e2[k] = *reinterpret_cast<const u32x4*>(lds + le + UB + k * 1024); }
#pragma unroll
  for (int k = 0; k < 4; ++k) {
    u32x4 ov;
#pragma unroll
    for (int c = 0; c < 4; ++c) {
      const f32x2 r = ((sv[k][c] + f32x2{lo2f(e1[k][c]), hi2f(e1[k][c])}) + f32x2{lo2f(e2[k][c]), hi2f(e2[k][c])}) * gf[k][c] + xf[k][c];
      ov[c] = pack2(r[0], r[1]);
    }
    __builtin_amdgcn_raw_buffer_store_b128(ov, rout, xo + k * 16 * D * 2, 0, SC1 | NT);
  }
  STAMP(8);
  STAMP_OUT();
}

#define BCAST(v, k) ((unsigned)__builtin_amdgcn_mov_dpp((int)(v), 0x150 + (k), 0xf, 0xf, true))
template <int K0, int K1, int N>
__device__ __forceinline__ void gemv(f32x4& ae, f32x4& ao, unsigned xs, const unsigned (&sel)[4], const u32x4 (&w)[N]) {
  if constexpr (K0 < K1) {
    const unsigned v = BCAST(xs, K0);
    const bf16x8 dg = BF8((u32x4{__builtin_amdgcn_perm(0u, v, sel[0]), __builtin_amdgcn_perm(0u, v, sel[1]),
                                 __builtin_amdgcn_perm(0u, v, sel[2]), __builtin_amdgcn_perm(0u, v, sel[3])}));
    ae = MFMA(dg, BF8(w[2 * K0]), ae); ao = MFMA(dg, BF8(w[2 * K0 + 1]), ao);
    gemv<K0 + 1, K1, N>(ae, ao, xs, sel, w);
  }
}

__device__ __forceinline__ void helper(int hw, int lane, int b, char* lds, rsrc_t rws, rsrc_t rx, const u16* wr,
                                       const u16* wgu, const u16* wdn, const u16* gq, const char* code, int tp1, int tq1 STAMP_ARG) {
  STAMP_DECL;
  int nb = 0, lim = 17;
  unsigned tch = 0;
  volatile LAS unsigned* misc = (volatile LAS unsigned*)(lds + L_MISC);
  __builtin_amdgcn_s_setprio(3);
  const int z = tq1 >> 3, te = tq1 & 7;
  const int lc = lane & 15, lq = lane >> 4;
  const int kb = (te + tp1) & 3, amo = WS_AMAX + (tq1 * 16 + tp1) * 128;
#define JOIN() do { HPRE(); BAR(); HSTAMP(); ++nb; } while (0)
#define NEXT() do { if (nb < lim) JOIN(); } while (0)
#define UNTIL(m) do { _Pragma("unroll 1") while (nb < (m)) JOIN(); } while (0)
#define POLLVM(m) do { _Pragma("unroll 1") while (vm_now() > (m) && nb < lim) JOIN(); wait_vm<m>(); } while (0)
  unsigned tag, key, sg; int idx;
  float sw;
  {
    const rsrc_t rr = mk_rsrc(wr, D * 2);
    u32x4 wf[8];
#pragma unroll
    for (int i = 0; i < 8; ++i) wf[i] = __builtin_amdgcn_raw_buffer_load_b128(rr, lq * 16 + i * 64, kb * 512, 0);
    const unsigned epoch = __builtin_amdgcn_raw_buffer_load_b32(rws, lane == 1 ? amo : lane == 2 ? (hw ? amo + (WS_FLAG1 - WS_AMAX) : WS_FLAG2 + b * 128) : WS_EPOCH, 0, SC1);
    const u32x4 ra = __builtin_amdgcn_raw_buffer_load_b128(rr, lane * 16, 0, 0), rb2 = __builtin_amdgcn_raw_buffer_load_b128(rr, lane * 16 + 1024, 0, 0);
    misc[lc] = 0u;
    u32x4 ones = {0x3f803f80u, 0x3f803f80u, 0x3f803f80u, 0x3f803f80u};
    const int sx = ((te >> 2) + 2 * z) * 32 + hw * 16, sv = (tp1 >> 2) * 32 + hw * 16;
    int xa[2], wa[2];
    xa[0] = (sx + lc) * 128 + (lq ^ (lc & 7)) * 16; wa[0] = xa[0] + (sv - sx) * 128 + UB;
    xa[1] = xa[0] ^ 64; wa[1] = wa[0] ^ 64;
    asm volatile("" : "+v"(ones));
    f32x4 d1 = {0.f, 0.f, 0.f, 0.f}, d2 = d1, d3 = d1, d4 = d1;
    HST(9);
#pragma unroll
    for (int u = 0; u < 4; ++u) {
      if (u == 2) { wa[0] += 4 * UB; wa[1] += 4 * UB; }
      if (u == 3) { xa[0] += 7 * UB; xa[1] += 7 * UB; }
      PIN4(xa[0], xa[1], wa[0], wa[1]);
      WAIT_LGKM0();
      JOIN();
      bf16x8 xf[2], vf[2];
#pragma unroll
      for (int h = 0; h < 2; ++h) { xf[h] = LDF(xa[h] + (u == 3 ? 0 : XI(u) * UB)); vf[h] = LDF(wa[h] + (u & 1) * 3 * UB); }
      __builtin_amdgcn_sched_barrier(0);
#pragma unroll
      for (int h = 0; h < 2; ++h) {
        d2 = MFMA(xf[h], xf[h], d2); d4 = MFMA(BF8(ones), xf[h], d4); d3 = MFMA(BF8(ones), vf[h], d3); d1 = MFMA(BF8(wf[2 * u + h]), xf[h], d1);
      }
      PIN4(d1, d2, d3, d4);
      if (u == 0) {
        tag = mk_tag(epoch);
        asm volatile("" : "+s"(tag));
        misc[2] = tag;
        const f32x4 d5 = MFMA(BF8(rb2), BF8(ones), MFMA(BF8(ra), BF8(ones), (f32x4{0.f, 0.f, 0.f, 0.f})));
        const int s4 = __builtin_bit_cast(int, (d5[0] + d5[1]) + (d5[2] + d5[3]));
#define RL(l) __builtin_bit_cast(float, __builtin_amdgcn_readlane(s4, l))
        sw = (RL(0) + RL(16)) + (RL(32) + RL(48));
        asm volatile("" : "+v"(sw));
      }
      __builtin_amdgcn_sched_barrier(0);
    }
    JOIN();
    const float sq = (lc & 2) ? ((lc & 1) ? d2[3] : d2[2]) : ((lc & 1) ? d2[1] : d2[0]);
    __builtin_amdgcn_raw_buffer_store_b128(u32x4{__float_as_uint(d4[0]), __float_as_uint(sq), __float_as_uint(d1[0]), tag}, rws,
                                           lq == lc >> 2 ? WS_XP + (tp1 * 4 + kb) * 2048 + (sx + lc) * 16 : -1, 0, SC1);
    __builtin_amdgcn_raw_buffer_store_b64(u32x2{__float_as_uint(d3[0]), tag}, rws, lane < 16 ? WS_WP + (tq1 * 4 + kb) * 1024 + (sv + lc) * 8 : -1, 0, SC1);
    __builtin_amdgcn_sched_barrier(0);
  }
  const int rg = b / 28, cg = b - rg * 28;
  const int fg = b >> 3, eg = b & 7;
  u32x4 w[32], wd[14];
  const rsrc_t rgu = mk_rsrc(wgu + (size_t)(rg & 7) * 128 * F + cg * 128, b < 224 ? 128 * F * 2 : 0);
  const rsrc_t rd = mk_rsrc(wdn + (size_t)(fg * 112 + hw * 56) * D + eg * 128, 56 * D * 2);
  const rsrc_t rq = mk_rsrc(gq, QROWS * S * 2);
  const unsigned kt = ktab(tp1);
  const int wvo = lq * 32 * F * 2 + lc * 16, dvo = lq * 14 * D * 2 + lc * 16;
#define LOADW(k0, k1) do { _Pragma("unroll") for (int k = k0; k < (k1); ++k) \
    w[k] = __builtin_amdgcn_raw_buffer_load_b128(rgu, wvo, k * F * 2, NT); asm volatile("" ::: "memory"); } while (0)
#define LOADD(k0, k1) do { _Pragma("unroll") for (int k = k0; k < (k1); ++k) \
    wd[k] = __builtin_amdgcn_raw_buffer_load_b128(rd, dvo, k * D * 2, NT); asm volatile("" ::: "memory"); } while (0)
  const int trow = (te >> 2) * 1024 + (te & 3) * 32;
  const int t1o = (trow + (lane >> 1)) * (S * 2) + (lane & 1) * 128;
#define QTOUCH(o, so) asm volatile("buffer_load_dword %0, %1, %2, %3 offen" : "+v"(tch) : "v"(o), "s"(rq), "s"(so) : "memory")
#define TCH(m) do { _Pragma("unroll") for (int i_ = 0; i_ < 8; ++i_) if (((m) >> i_ & 1) && hw == (i_ & 1)) { \
      if (i_ < 7) QTOUCH(t1o, kstep(kt, 2 * i_) * 128); \
      else QTOUCH(lane < 32 ? (trow + lane) * (S * 2) + kstep(kt, 14) * 128 : ((tp1 >> 3) * 1024 + te * 16 + lc) * (S * 2) + (kstep(kt, 15) ^ (lane & 16)) * 128, 0); } } while (0)
  const int srow = hw * 64 + (lane & 35) + 2 * (lane & 12) + ((lane >> 2) & 4);
  {
    const int gxo = WS_XP + tp1 * 8192 + (hw * 64 + lane) * 16, gwo = WS_WP + tq1 * 4096 + (hw * 64 + lane) * 8;
    u32x4 gx[4]; u32x2 gw[4];
#define GATHER() asm volatile("buffer_load_dwordx4 %0, %8, %10, 0 offen sc1\n\tbuffer_load_dwordx4 %1, %8, %10, 0 offen offset:2048 sc1\n\t" \
                 "buffer_load_dwordx4 %2, %8, %10, %11 offen sc1\n\tbuffer_load_dwordx4 %3, %8, %10, %11 offen offset:2048 sc1\n\t" \
                 "buffer_load_dwordx2 %4, %9, %10, 0 offen sc1\n\tbuffer_load_dwordx2 %5, %9, %10, 0 offen offset:1024 sc1\n\t" \
                 "buffer_load_dwordx2 %6, %9, %10, 0 offen offset:2048 sc1\n\tbuffer_load_dwordx2 %7, %9, %10, 0 offen offset:3072 sc1" \
                 : "=&v"(gx[0]), "=&v"(gx[1]), "=&v"(gx[2]), "=&v"(gx[3]), "=&v"(gw[0]), "=&v"(gw[1]), "=&v"(gw[2]), "=&v"(gw[3]) \
                 : "v"(gxo), "v"(gwo), "s"(rws), "s"(4096) : "memory")
    UNTIL(sched(UP_T, te));
    TCH(TM0);
    LOADW(0, NWA1);
    if (NWA > NWA1) { JOIN(); LOADW(NWA1, NWA); }
    UNTIL(sched(T1_T, te) - GAD);
    GATHER();
    UNTIL(sched(T1_T, te));
    TCH(TM1);
    LOADW(NWA, NWC);
    wait_vm<NWC - NWA>();
#pragma unroll 1
    for (int spins = 0;; ++spins) {
      asm volatile("" : "+v"(gx[0]), "+v"(gx[1]), "+v"(gx[2]), "+v"(gx[3]), "+v"(gw[0]), "+v"(gw[1]), "+v"(gw[2]), "+v"(gw[3]) :: "memory");
      unsigned bad = 0;
#pragma unroll
      for (int q = 0; q < 4; ++q) bad |= (gx[q][3] ^ tag) | (gw[q][1] ^ tag);
      if (__builtin_expect(__all(bad == 0) || spins > SPIN_CAP / 64, 1)) break;
      GATHER();
      NEXT();
      POLLVM(0);
    }
    HST(3);
#define SUM4(v, c) ((__uint_as_float(v[0][c]) + __uint_as_float(v[1][c])) + (__uint_as_float(v[2][c]) + __uint_as_float(v[3][c])))
    float sum = SUM4(gx, 0), sq = SUM4(gx, 1), dot = SUM4(gx, 2);
    *(LAS float*)(lds + L_MB + 1024 + srow * 4) = SUM4(gw, 0);
    asm volatile("" : "+v"(sum), "+v"(sq), "+v"(dot));
    const float mn = sum * (1.0f / D);
    const float var = fmaxf(fmaf(-mn, mn, sq * (1.0f / D)), 0.f);
    const float mbv = rbf(mn), den = rbf(__builtin_amdgcn_sqrtf(rbf(rbf(var) + 1e-5f))), rden = __builtin_amdgcn_rcpf(den);
    *(LAS float*)(lds + L_MB + srow * 4) = -mbv;
    *(LAS float*)(lds + L_MB + 512 + srow * 4) = rden;
    asm volatile("" ::: "memory");
    misc[4 + hw] = 1u;
    float rs = (dot - mbv * sw) * rden;
    sg = pack2(mbv, den);
    asm volatile("" : "+v"(rs), "+v"(sg));
    lim = NBAR;
    const unsigned rb = pack2(rs, rs) & 0xffffu;
    unsigned srt = (rb & 0x8000u) ? (~rb & 0xffffu) : (rb | 0x8000u);
    if (rs != rs) srt = 0u;
    key = (srt << 16) | (unsigned)(0xffff - tp1 * 128 - srow);
    const unsigned mk = wave_maxu(key);
    if (key == mk) __builtin_amdgcn_raw_buffer_store_b128(u32x4{key, tag, sg, tag}, rws, amo + hw * 16, 0, SC1);
    HST(2);
    UNTIL(WB_AT);
    TCH(TMB);
    LOADW(NWC, 32);
  }
  UNTIL(AM_AT);
#pragma unroll 1
  for (int spins = 0;; ++spins) {
    const u32x4 am = __builtin_amdgcn_raw_buffer_load_b128(rws, WS_AMAX + tq1 * 2048 + ((lane >> 1) & 15) * 128 + (lane & 1) * 16, 0, SC1);
    if (nb != 19) { NEXT(); POLLVM(0); }
    key = am[0]; sg = am[2];
    if (__builtin_expect(__all(((am[1] ^ tag) | (am[3] ^ tag)) == 0) || spins > SPIN_CAP / 64, 1)) break;
    if (nb == 19) JOIN();
  }
  {
    const unsigned mk = wave_maxu(key);
    sg = __builtin_amdgcn_readlane(sg, __builtin_ctzll(__ballot(key == mk)));
    key = mk;
  }
  idx = min(0xffff - (int)(key & 0xffffu), S - 1);
  const unsigned xe = __builtin_amdgcn_raw_buffer_load_b32(rx, (idx * D + (rg & 7) * 128) * 2 + lane * 4, 0, 0);
  if (lane == 0) misc[0] = idx + 1;
  if (b == 0 && lane == 0 && hw == 1) __builtin_amdgcn_raw_buffer_store_b32(tag, rws, WS_EPOCH, 0, SC1);
  unsigned sel[4];
  UNTIL(17);
#pragma unroll
  for (int d = 0; d < 4; ++d) {
    const unsigned src = (lc & 2) ? 0x0302u : 0x0100u;
    sel[d] = (lc >> 2) == d ? ((lc & 1) ? (src << 16 | 0x0c0cu) : (0x0c0c0000u | src)) : 0x0c0c0c0cu;
  }
  const int hc = fg * 112 + hw * 56 + (lq * 7 + min(lc & 7, 6)) * 2;
  int guo = WS_GU + ((lc >> 3) * F + (hc & ~127)) * 8 + ((hc & 6) * 8 + ((hc >> 3) & 15)) * 16;
  PIN4(sel[0], sel[1], sel[2], sel[3]);
  asm volatile("" : "+v"(guo));
  HST(7);
  UNTIL(19);
  {
    const bool idle = nb == 19;
    if (idle) { if (GV_SL) __builtin_amdgcn_s_sleep(GV_SL); } else { NEXT(); POLLVM(0); }
    const unsigned xs = ln_pair(xe, -lo2f(sg), __builtin_amdgcn_rcpf(hi2f(sg)));
    f32x4 ae = {0.f, 0.f, 0.f, 0.f}, ao = ae;
#define GCUT() do { asm volatile("" : "+v"(ae), "+v"(ao)); if (__builtin_expect(!idle, 0)) NEXT(); } while (0)
    gemv<0, 4>(ae, ao, xs, sel, w); GCUT(); gemv<4, 8>(ae, ao, xs, sel, w); GCUT(); gemv<8, 12>(ae, ao, xs, sel, w); GCUT(); gemv<12, 16>(ae, ao, xs, sel, w);
    if (b < 224) __builtin_amdgcn_raw_buffer_store_b128(u32x4{__float_as_uint(ae[0] + ao[2]), tag, __float_as_uint(ae[1] + ao[3]), tag}, rws,
                                                        WS_GU + ((rg * 2 + hw) * F + cg * 128) * 8 + lane * 16, 0, SC1);
  }
  HST(4);
  if (nb == 19) JOIN();
  {
    const int m0_ = 18 + MB0 + (te & MBS);
#define MB(i) do { if ((MBT >> 8 * (i) & 255) || (MBW >> 4 * (i) & 15) != (MBW >> 4 * (i) + 4 & 15)) { UNTIL(m0_ + MBD * (i)); if (BSL2) __builtin_amdgcn_s_sleep(BSL2); \
      TCH(MBT >> 8 * (i) & 255); LOADD(MBW >> 4 * (i) & 15, MBW >> 4 * (i) + 4 & 15); } } while (0)
    MB(0); MB(1); MB(2); MB(3); MB(4);
    if (!hw && lane < (CODE_LINES + 7) / 8) {
      const int co = min(((b >> 3) & 7) * ((CODE_LINES + 7) / 8) + lane, CODE_LINES - 1) * 128;
      asm volatile("global_load_dword %0, %1, off" : "+v"(tch) : "v"(code + co) : "memory");
    }
    UNTIL(m0_ + GGD);
  }
  {
    float s0, s1;
#pragma unroll 1
    for (int spins = 0;; ++spins) {
#pragma unroll
      for (int k = 0; k < 8; ++k) w[k] = __builtin_amdgcn_raw_buffer_load_b128(rws, guo, k * 2 * F * 8, SC1);
      NEXT();
      POLLVM(0);
      unsigned bad = 0; s0 = 0.f; s1 = 0.f;
#pragma unroll
      for (int k = 0; k < 8; ++k) { bad |= (w[k][1] ^ tag) | (w[k][3] ^ tag); s0 += __uint_as_float(w[k][0]); s1 += __uint_as_float(w[k][2]); }
      if (__builtin_expect(__all(bad == 0) || spins > SPIN_CAP / 64, 1)) break;
    }
    asm volatile("" : "+v"(s0), "+v"(s1));
    HST(8);
    NEXT();
    const unsigned gp = pack2(s0, s1);
    const unsigned up = (unsigned)__builtin_amdgcn_mov_dpp((int)gp, 0x128, 0xf, 0xf, true);
    const unsigned sl2 = pack2(silu_fast(lo2f(gp)), silu_fast(hi2f(gp)));
    const unsigned hp = pack2(lo2f(sl2) * lo2f(up), hi2f(sl2) * hi2f(up));
    f32x4 ae = {0.f, 0.f, 0.f, 0.f}, ao = ae;
    gemv<0, 2>(ae, ao, hp, sel, wd);
    asm volatile("" : "+v"(ae), "+v"(ao));
    NEXT();
    gemv<2, 7>(ae, ao, hp, sel, wd);
    f32x2 a = {ae[0] + ao[2], ae[1] + ao[3]};
    HST(5);
    if (hw == 1) {
      *(LAS f32x2*)(lds + L_CMB + lane * 8) = a;
      asm volatile("" ::: "memory");
      misc[10] = 1u;
    } else {
      f32x2 o;
#pragma unroll 1
      for (int spins = 0; spins < SPIN_CAP; ++spins) {
        NEXT();
        const unsigned f = misc[10];
        o = *(volatile LAS f32x2*)(lds + L_CMB + lane * 8);
        if (rfl(f)) break;
      }
      __builtin_amdgcn_raw_buffer_store_b128(u32x4{__float_as_uint(a[0] + o[0]), tag, __float_as_uint(a[1] + o[1]), tag}, rws,
                                             WS_PART + (fg * D + eg * 128) * 8 + lane * 16, 0, SC1);
      HST(6);
    }
  }
  if (hw == 1 && (idx >> 7) == tp1) {
    const unsigned srt = key >> 16;
    const float r = lo2f((srt & 0x8000u) ? (srt & 0x7fffu) : (~srt & 0xffffu));
    const float rsel = rbf(1.0f / (1.0f + __expf(-r)));
    const int cp = lane & 31;
    const int e = (cp >> 4) * 64 + z * 32 + (cp & 15) * 2;
    const int po = WS_PART + ((lane >> 5) * D + te * 128) * 8 + ((e & 6) * 8 + (e >> 3)) * 16;
    float y0, y1;
    UNTIL(FIN_AT);
#pragma unroll 1
    for (int spins = 0;; ++spins) {
#pragma unroll
      for (int k = 0; k < 16; ++k) w[k] = __builtin_amdgcn_raw_buffer_load_b128(rws, po, k * 2 * D * 8, SC1);
      NEXT();
      POLLVM(0);
      unsigned bad = 0;
#pragma unroll
      for (int k = 0; k < 16; ++k) bad |= (w[k][1] ^ tag) | (w[k][3] ^ tag);
      if (__all(bad == 0) || spins > SPIN_CAP / 64) break;
    }
    NEXT();
    y0 = 0.f; y1 = 0.f;
#pragma unroll
    for (int k = 0; k < 16; ++k) { y0 += __uint_as_float(w[k][0]); y1 += __uint_as_float(w[k][2]); }
    asm volatile("" : "+v"(y0), "+v"(y1));
    NEXT();
    { const auto p0 = __builtin_amdgcn_permlane32_swap(__float_as_uint(y0), __float_as_uint(y0), false, false);
      const auto p1 = __builtin_amdgcn_permlane32_swap(__float_as_uint(y1), __float_as_uint(y1), false, false);
      y0 = __uint_as_float(p0[0]) + __uint_as_float(p0[1]); y1 = __uint_as_float(p1[0]) + __uint_as_float(p1[1]); }
    *(LAS f32x2*)(lds + L_UPD + cp * 8) = f32x2{rbf(rbf(y0) * rsel), rbf(rbf(y1) * rsel)};
    asm volatile("" ::: "memory");
    misc[1] = 1u;
    HST(6);
  }
  HST(10);
  POLLVM(0);
  asm volatile("" : "+v"(tch));
  STAMP_OUT();
}

__global__ __launch_bounds__(512, 2) void fused(const u16* __restrict__ x, const u16* __restrict__ sre, const u16* __restrict__ sim,
                                                const u16* __restrict__ hre, const u16* __restrict__ him, const u16* __restrict__ wr,
                                                const u16* __restrict__ wg, const u16* __restrict__ wu, const u16* __restrict__ wdn,
                                                char* ws, u16* out) {
  extern __shared__ __attribute__((aligned(16))) char lds[];
  u64 pc;
  asm volatile("s_getpc_b64 %0" : "=s"(pc));
  const char* code = (const char*)(pc & ~255ull);
  const int wid = __builtin_amdgcn_readfirstlane(threadIdx.x >> 6), lane = threadIdx.x & 63;
  const int b = blockIdx.x, g = b & 7, i = b >> 3;
  const int tp1 = (g & 3) * 4 + (i & 3), tq1 = (g >> 2) * 8 + (i >> 2);
  const int z = tq1 >> 3, te = tq1 & 7;
  asm volatile("" :: "s"(x), "s"(sre), "s"(sim), "s"(hre), "s"(him), "s"(wr), "s"(wg), "s"(wu), "s"(wdn), "s"(ws), "s"(out));
  const rsrc_t rws = mk_rsrc(ws, WS_END);
  const u16* wtile = (z ? him : hre) + (size_t)te * 128 * D;
  if (__builtin_expect(wid == 4 || wid == 5, 1)) {
    const int L = wid - 4;
    loader(L, lane, (LAS char*)lds, L ? wtile : x + (size_t)tp1 * 128 * D, (z ? sim : sre) + (size_t)(tp1 & 7) * 128 * S,
           ws + WS_ABT + (size_t)tq1 * ABT_ST, rws, WS_FLAG1 + tq1 * 2048, b, tp1, te STAMP_PASS);
  } else if (wid < 4) {
    consumer(wid, lane, b, lds, rws, mk_rsrc(out, S * D * 2), code, tp1, tq1 STAMP_PASS);
  } else {
    const int hw = wid - 6;
    helper(hw, lane, b, lds, rws, mk_rsrc(x, S * D * 2), wr, hw ? wu : wg, wdn,
           (z ? sim : sre) + (size_t)(tp1 & 7) * 128 * S, code, tp1, tq1 STAMP_PASS);
  }
}

extern "C" void kernel_launch(void* const* d_in, const int* in_sizes, int n_in, void* d_out, int out_size,
                              void* d_ws, size_t ws_size, hipStream_t stream) {
  (void)hipFuncSetAttribute((const void*)fused, hipFuncAttributeMaxDynamicSharedMemorySize, LDS_BYTES);
  fused<<<256, 512, LDS_BYTES, stream>>>((const u16*)d_in[0], (const u16*)d_in[1], (const u16*)d_in[2], (const u16*)d_in[3],
                                         (const u16*)d_in[4], (const u16*)d_in[6], (const u16*)d_in[7], (const u16*)d_in[8],
                                         (const u16*)d_in[9], (char*)d_ws, (u16*)d_out);
}
```

```cpp
#define CODE_LINES 260
#include <hip/hip_runtime.h>
#include <hip/hip_bf16.h>
#include <stdint.h>

typedef __attribute__((ext_vector_type(8))) short bf16x8;
typedef __attribute__((ext_vector_type(4))) float f32x4;
typedef __attribute__((ext_vector_type(2))) float f32x2;
typedef __attribute__((ext_vector_type(4))) unsigned u32x4;
typedef __attribute__((ext_vector_type(2))) unsigned u32x2;
typedef __attribute__((ext_vector_type(2))) __bf16 bf16x2;
typedef unsigned short u16;
typedef unsigned long long u64;
typedef __amdgpu_buffer_rsrc_t rsrc_t;
#define LAS __attribute__((address_space(3)))

constexpr int S = 2048, D = 1024, F = 3584;
constexpr int UB = 16384;
constexpr int SLOT = 3 * UB;
constexpr int L_MISC = 9 * UB;
constexpr int L_UPD = L_MISC + 64;
constexpr int L_MB = L_MISC + 512;
constexpr int L_CMB = L_MB + 1536;
constexpr int L_RX = L_CMB + 512;
constexpr int LDS_BYTES = L_RX + 11264;
constexpr int NBAR = 17 + 14 + 4;
constexpr int SC1 = 16, NT = 2;

constexpr unsigned WS_EPOCH = 0;
constexpr unsigned WS_XP = 4096;
constexpr unsigned WS_WP = WS_XP + 16 * 8192;
constexpr unsigned WS_AMAX = WS_WP + 16 * 4096;
constexpr unsigned WS_FLAG1 = WS_AMAX + 32768;
constexpr unsigned WS_FLAG2 = WS_FLAG1 + 32768;
constexpr unsigned WS_GU = WS_FLAG2 + 32768;
constexpr unsigned WS_PART = WS_GU + 8 * 2 * F * 8;
constexpr unsigned WS_XCH = WS_PART + 32 * D * 8;
constexpr int XB = 32768 + 22528;
constexpr unsigned ABT_ST = 128 * S * 2;
constexpr unsigned WS_ABT = WS_XCH + 256 * XB;
constexpr unsigned WS_END = WS_ABT + 16 * ABT_ST;

#define STAMP(i)
#define HSTAMP()
#define HPRE()
#define HST(i)
#define STAMP_DECL
#define STAMP_ARG
#define STAMP_PASS
#define STAMP_OUT()

__device__ __forceinline__ float lo2f(unsigned v) { return __uint_as_float(v << 16); }
__device__ __forceinline__ float hi2f(unsigned v) { return __uint_as_float(v & 0xffff0000u); }
__device__ __forceinline__ unsigned pack2(float a, float b) {
  return __builtin_bit_cast(unsigned, __builtin_convertvector(f32x2{a, b}, bf16x2));
}
__device__ __forceinline__ float rbf(float f) { return lo2f(pack2(f, f)); }
__device__ __forceinline__ unsigned rfl(unsigned v) { return __builtin_amdgcn_readfirstlane(v); }
__device__ __forceinline__ rsrc_t mk_rsrc(const void* p, unsigned bytes) {
  return __builtin_amdgcn_make_buffer_rsrc((void*)p, 0, bytes, 0x00020000);
}
#define DPP_ADD(v, ctrl, rm, bc) v += __builtin_bit_cast(float, __builtin_amdgcn_update_dpp(0, __builtin_bit_cast(int, v), ctrl, rm, 0xf, bc))
#define DPP_MAXU(v, ctrl, rm, bc) v = max(v, (unsigned)__builtin_amdgcn_update_dpp(0, (int)v, ctrl, rm, 0xf, bc))
__device__ __forceinline__ unsigned wave_maxu(unsigned v) {
  DPP_MAXU(v, 0x111, 0xf, true); DPP_MAXU(v, 0x112, 0xf, true); DPP_MAXU(v, 0x114, 0xf, true); DPP_MAXU(v, 0x118, 0xf, true);
  DPP_MAXU(v, 0x142, 0xa, false); DPP_MAXU(v, 0x143, 0xc, false);
  return (unsigned)__builtin_amdgcn_readlane((int)v, 63);
}
template <int N> __device__ __forceinline__ void wait_vm() { asm volatile("s_waitcnt vmcnt(%0)" :: "n"(N) : "memory"); }
#define WAIT_LGKM0() asm volatile("s_waitcnt lgkmcnt(0)" ::: "memory")
#define BAR() do { asm volatile("" ::: "memory"); __builtin_amdgcn_s_barrier(); asm volatile("" ::: "memory"); } while (0)
#define MFMA(a, b, c) __builtin_amdgcn_mfma_f32_16x16x32_bf16(a, b, c, 0, 0, 0)
#define BF8(v) __builtin_bit_cast(bf16x8, v)
constexpr int SPIN_CAP = 1 << 20;
#define UP_T 0x0507060508070605ull
#define T1_T 0x090a09090a0a0909ull
#define GAD 0
#define NWA1 16
#define NWA 16
#define NWC 28
#define TM0 0
#define TM1 0
#define TMB 3
#define WB_AT 12
#define AM_AT 15
#define GV_SL 8
#define MB0 2
#define MBS 0
#define MBD 2
#define MBT 0x80402018ull
#define MBW 0xeeeeeb740ull
#define BSL2 13
#define GGD 7
#define FIN_AT 35
#define EG0 1
#define EGD 1
#define PEARLY 1
#define QL 1
#define T7 2
#define DTM 0
#define KEEP 1
#define FCHK 0
#define FBLK 2
#define WPOL NT
#define OPOL (SC1 | NT)
#define KM 6
#define QR 0
#define PI 0x2e32363c
#define RAMP 4
#define NWD 32
#define WD0 (int)(MBW & 15)
#define TM18 0
#define B0SL 2
#define SWT 0x100d0a070400ull
#define SWE (int)(SWT >> 56 ? SWT >> 56 : SWT >> 48 ? SWT >> 48 : SWT >> 40)
__device__ __forceinline__ int sched(u64 tab, int te) { return (int)(tab >> (te * 8)) & 255; }
#define PIN4(a, b, c, d) asm volatile("" : "+v"(a), "+v"(b), "+v"(c), "+v"(d))
__device__ __forceinline__ unsigned vm_now() { const unsigned r = __builtin_amdgcn_s_getreg(7 | (31 << 11)); return (r & 15u) | ((r >> 18) & 0x30u); }
__device__ __forceinline__ unsigned mk_tag(unsigned epoch) { return rfl(epoch) * 0x9E3779B1u + 0x7F4A7C15u; }

template <int LD, int AUX, int MODE, int NP = 16>
__device__ __forceinline__ void issue(rsrc_t r, int voff, int koff, LAS char* dst) {
#pragma unroll
  for (int j = 0; j < NP; ++j) {
    const int so = MODE == 2 ? j * 1024 : MODE == 1 ? (64 * (j >> 3) + 32 * ((j >> 2) & 1) + 16 * (j & 1) + 4 * ((j >> 1) & 1)) * LD : j * 8 * LD;
    __builtin_amdgcn_raw_ptr_buffer_load_lds(r, (LAS void*)(dst + j * 1024), 16, voff, koff + so, 0, AUX);
  }
}
__device__ __forceinline__ unsigned ktab(int tp1, int te) {
  const int j = tp1 & 3;
  const unsigned t = (tp1 * 0x01010101u ^ 0x03020100u) | (((tp1 & 12) ^ 4) * 0x10101010u ^ (j == 1 ? 0x30002010u : j == 2 ? 0x30100020u : 0x30201000u));
  const unsigned m = t >> 4 & 0xffffffu, r = (QR >> 4 * te & 7) * 4;
  return (t & 0xf000000fu) | ((m >> r | m << (24 - r)) & 0xffffffu) << 4;
}
__device__ __forceinline__ int rot1(int tp1, int tq1) { return KM == 4 ? 4 * (tp1 + tq1) & 15 : (8 * (tp1 + tq1) + 2 * (((tp1 >> 2) + 2 * (tq1 >> 3)) & 3)) & 15; }
__device__ __forceinline__ int kstep(unsigned kt, int n) { return (int)(kt >> (n >> 1) * 4 & 15) * 2 + (n & 1); }
constexpr int QROWS = 1024 + 128;

__device__ __forceinline__ void loader(int L, int lane, LAS char* lds, const u16* g1, const u16* gq, const char* gp, rsrc_t rws,
                                       int flag_off, int b, int tp1, int tq1 STAMP_ARG) {
  const int te = tq1 & 7;
  STAMP_DECL;
  __builtin_amdgcn_s_setprio(2);
  const int rsub = lane >> 3, ch = ((lane & 7) ^ rsub) * 16;
  {
    const rsrc_t r1 = mk_rsrc(g1, 128 * D * 2);
    const int v1 = (8 * (rsub >> 2) + (rsub & 3)) * (D * 2) + ch;
    const unsigned utab = L ? 0x8541u : 0x7230u;
    STAMP(1);
#pragma unroll 1
    for (int t = 0; t < 16; ++t) {
      int ko = ((t + rot1(tp1, tq1)) & 15) * 128;
      LAS char* dst = lds + ((utab >> ((t & 3) * 4)) & 15) * UB;
      asm volatile("" : "+s"(ko), "+s"(dst));
      if (__builtin_expect(t > RAMP, 1)) { wait_vm<32>(); BAR(); }
      else if (t >= 2 && t < RAMP) { wait_vm<16>(); BAR(); }
      issue<D * 2, 0, 1>(r1, v1, ko, dst);
    }
  }
  const rsrc_t rq = mk_rsrc(gq, QROWS * S * 2);
  const int h = tp1 >> 3;
  const unsigned kt = ktab(tp1, te);
  const int vq = rsub * (S * 2) + ch, vp = lane * 16;
  const int qa = h * 1024 * (S * 2), qb = ((1 - h) * 1024 + L * 64) * (S * 2);
  LAS char* const dqb = lds + UB + L * 8192;
  volatile LAS unsigned* misc = (volatile LAS unsigned*)(lds + L_MISC);
  unsigned fl = 0, tag;
  asm volatile("ds_read_b32 %0, %1\n\ts_waitcnt vmcnt(32) lgkmcnt(0)" : "=v"(tag) : "v"(L_MISC + 8) : "memory");
  tag = rfl(tag);
  BAR();
  int sl = 0;
#define POLL(o) asm volatile("buffer_load_dword %0, %1, %2, 0 offen sc1" : "+v"(fl) : "v"(o), "s"(rws) : "memory")
#define POLLW(o) asm volatile("buffer_load_dword %0, %1, %2, 0 offen sc1\n\ts_waitcnt vmcnt(0)" : "+v"(fl) : "v"(o), "s"(rws) : "memory")
  if (L) {
#pragma unroll 1
    for (int n = 0; n < 16; ++n) {
      const int k_ = kstep(kt, n) * 128;
      int ka = qa + (n == 15 ? k_ ^ 2048 : k_), kb = qb + k_;
      asm volatile("" : "+s"(ka), "+s"(kb), "+s"(sl));
      if (__builtin_expect(n > 3, 1)) { wait_vm<24>(); BAR(); }
      else if (n == 0) { wait_vm<16>(); BAR(); }
      else if (n == 1) { wait_vm<16>(); BAR(); }
      else if (n == 2) { BAR(); }
      else if (n == 3) { wait_vm<40>(); BAR(); wait_vm<24>(); BAR(); }
      issue<S * 2, 0, 0>(rq, vq, ka, lds + sl);
      if (__builtin_expect(n >= 2 && n < 15, 1)) issue<S * 2, 0, 0, 8>(rq, vq, kb, dqb + sl);
      sl = sl == 2 * SLOT ? 0 : sl + SLOT;
    }
    const int f2o = WS_FLAG2 + (b ^ 4) * 128 + 16 + (lane & 3) * 4;
    wait_vm<16>();
    BAR();
    wait_vm<0>();
    BAR();
    issue<S * 2, 0, 0, 8>(rq, vq, qa + 64 * (S * 2) + kstep(kt, 15) * 128, lds + 4 * UB + 8192);
    wait_vm<0>();
    BAR();
    BAR();
#pragma unroll 1
    for (int spins = 0; spins < SPIN_CAP; ++spins) {
      POLLW(f2o);
      if (__all(fl == tag)) break;
      __builtin_amdgcn_s_sleep(1);
    }
    STAMP(4);
    issue<0, SC1, 2, 11>(rws, vp, WS_XCH + (b ^ 4) * XB + 32768, lds + 7 * UB);
    issue<0, SC1, 2, 11>(rws, vp, WS_XCH + (b ^ 4) * XB + 32768 + 11264, lds + L_RX);
    wait_vm<0>();
    misc[12] = 1u;
    STAMP(5);
  } else {
#define QHALF(n, sl) issue<S * 2, 0, 0, 8>(rq, vq, qb + kstep(kt, n) * 128, dqb + (sl))
    const rsrc_t rp = mk_rsrc(gp, 128 * S * 2);
    u64 mask = ~(0xffffffffull << 32 * h | 0xc0000000cull << (kt >> 26 & 28));
    const int fo = flag_off + (lane >> 2) * 128 + (lane & 3) * 4;
    const int fo1 = flag_off + (kstep(kt, 2) >> 1) * 128 + (lane & 3) * 4;
#define QFULL(n, sl) issue<S * 2, 0, 0>(rq, vq, qb + kstep(kt, n) * 128, dqb + (sl))
    wait_vm<16>();
    BAR();
    if (QL == 1) QFULL(0, 0);
    wait_vm<QL == 1 ? 16 : 0>();
    BAR();
    if (QL == 1) QFULL(1, SLOT);
    if (QL == 2) QFULL(0, 0);
    BAR();
    if (QL == 1) QHALF(2, 2 * SLOT);
    if (QL == 3) QFULL(0, 0);
    if (QL >= 2) QFULL(1, SLOT);
    wait_vm<QL == 1 ? 24 : 16>();
    BAR();
    if (QL >= 2) QHALF(2, 2 * SLOT);
    bool p2 = false;
    if (PEARLY) {
      POLLW(fo1);
      p2 = __all(fl == tag);
      if (p2) issue<0, SC1, 2>(rp, vp, kstep(kt, 2) * UB, lds + 2 * SLOT + 2 * UB);
    } else wait_vm<8>();
    BAR();
    QHALF(3, 0);
    STAMP(2);
    if (!p2) {
      unsigned f1, f2;
      asm volatile("buffer_load_dword %0, %2, %3, 0 offen sc1\n\ts_sleep 3\n\tbuffer_load_dword %1, %2, %3, 0 offen sc1\n\ts_sleep 3"
                   : "=&v"(f1), "=&v"(f2) : "v"(fo1), "s"(rws) : "memory");
#pragma unroll 1
      for (int spins = 0; spins < SPIN_CAP; ++spins) {
        asm volatile("buffer_load_dword %0, %2, %3, 0 offen sc1\n\ts_waitcnt vmcnt(2)" : "+v"(fl), "+v"(f1) : "v"(fo1), "s"(rws) : "memory");
        if (__all(f1 == tag)) break;
        asm volatile("buffer_load_dword %0, %2, %3, 0 offen sc1\n\ts_waitcnt vmcnt(2)" : "+v"(f1), "+v"(f2) : "v"(fo1), "s"(rws) : "memory");
        if (__all(f2 == tag)) break;
        asm volatile("buffer_load_dword %0, %2, %3, 0 offen sc1\n\ts_waitcnt vmcnt(2)" : "+v"(f2), "+v"(fl) : "v"(fo1), "s"(rws) : "memory");
        if (__all(fl == tag)) break;
      }
      issue<0, SC1, 2>(rp, vp, kstep(kt, 2) * UB, lds + 2 * SLOT + 2 * UB);
      asm volatile("s_waitcnt vmcnt(16)" : "+v"(fl), "+v"(f1), "+v"(f2) :: "memory");
    }
    STAMP(3);
    POLL(fo);
    issue<0, SC1, 2>(rp, vp, kstep(kt, 3) * UB, lds + 2 * UB);
    asm volatile("s_waitcnt vmcnt(16)" : "+v"(fl) :: "memory");
#define NEED(need) do { if (__builtin_expect(mask != ~0ull, 0)) { asm volatile("" : "+v"(fl) :: "memory"); mask |= __ballot(fl == tag); \
      _Pragma("unroll 1") for (int spins = 0; (mask & (need)) != (need) && spins < SPIN_CAP; ++spins) { POLLW(fo); mask |= __ballot(fl == tag); } \
      if (mask != ~0ull) POLL(fo); } } while (0)
    sl = SLOT;
#pragma unroll 1
    for (int n = 4; n < 16; ++n) {
      const int k_ = kstep(kt, n);
      int kp = n == 15 ? k_ ^ 16 : k_, kb = qb + k_ * 128;
      asm volatile("" : "+s"(kp), "+s"(kb), "+s"(sl));
      wait_vm<24>();
      BAR();
      NEED(n == 15 ? ~0ull : 3ull << (2 * k_));
      issue<0, SC1, 2>(rp, vp, kp * UB, lds + sl + 2 * UB);
      if (__builtin_expect(n < 15, 1)) issue<S * 2, 0, 0, 8>(rq, vq, kb, dqb + sl);
      sl = sl == 2 * SLOT ? 0 : sl + SLOT;
    }
    wait_vm<16>();
    BAR();
    issue<0, SC1, 2>(rp, vp, kstep(kt, 15) * UB, lds + UB);
    wait_vm<16>();
    BAR();
    issue<S * 2, 0, 0, 8>(rq, vq, qa + kstep(kt, 15) * 128, lds + 4 * UB);
    const int f2o = WS_FLAG2 + (b ^ 2 ^ (lane & 4)) * 128 + (lane & 3) * 4;
    wait_vm<0>();
    BAR();
    BAR();
    unsigned got = 0;
#pragma unroll 1
    for (int spins = 0; spins < SPIN_CAP && got != 3; ++spins) {
      POLLW(f2o);
      const unsigned m_ = (unsigned)__ballot(fl == tag);
      const unsigned g_ = (((m_ & 15u) == 15u) | ((m_ & 0xf0u) == 0xf0u) << 1) & ~got;
#pragma unroll 1
      for (int i = 0; i < 2; ++i)
        if (g_ >> i & 1) issue<0, SC1, 2>(rws, vp, WS_XCH + (b ^ 2 ^ 4 * i) * XB + i * 16384, lds + (1 + 3 * i) * UB);
      got |= g_;
      if (!g_) __builtin_amdgcn_s_sleep(1);
    }
    STAMP(4);
    wait_vm<0>();
    misc[13] = 1u;
    STAMP(5);
  }
  STAMP_OUT();
}

#define LDF(off) (*reinterpret_cast<const bf16x8*>(lds + (off)))
struct Frag { bf16x8 p[4], q[4]; };
__device__ __forceinline__ void mma(f32x4 (&acc)[4][4], const Frag& f) {
#pragma unroll
  for (int i = 0; i < 4; ++i)
#pragma unroll
    for (int j = 0; j < 4; ++j) acc[i][j] = MFMA(f.p[i], f.q[j], acc[i][j]);
}
#define SGB(m, n) __builtin_amdgcn_sched_group_barrier(m, n, 0)
__device__ __forceinline__ float silu_fast(float z) { return z * __builtin_amdgcn_rcpf(1.0f + __expf(-z)); }
__device__ __forceinline__ u32x4 pack8(const f32x4& a, const f32x4& c) {
  return u32x4{pack2(a[0], a[1]), pack2(a[2], a[3]), pack2(c[0], c[1]), pack2(c[2], c[3])};
}
__device__ __forceinline__ unsigned ln_pair(unsigned x, float nmb, float rden) {
  const unsigned d = pack2(lo2f(x) + nmb, hi2f(x) + nmb);
  return pack2(lo2f(d) * rden, hi2f(d) * rden);
}

template <bool BARRIER, bool LAST, bool ZERO, class X>
__device__ __forceinline__ void big_half(f32x4 (&a0)[4][4], f32x4 (&a1)[4][4], const bf16x8 (&pc)[4], bf16x8 (&pn)[4], bf16x8 (&q)[8],
                                         const char* lds, int qc, int pa, int qn, int qn2, X extra) {
  const f32x4 zero = {0.f, 0.f, 0.f, 0.f};
#pragma unroll
  for (int g = 0; g < 8; ++g) {
    if (g == 0) { q[5] = LDF(qc + UB + 2048); q[6] = LDF(qc + UB + 4096); }
    if (g == 1) q[7] = LDF(qc + UB + 6144);
    if (g == 3) { pn[0] = LDF(pa); pn[1] = LDF(pa + 2048); }
    if (g == 4) { pn[2] = LDF(pa + 4096); pn[3] = LDF(pa + 6144); }
    if (g == 5) { q[0] = LDF(qn); q[1] = LDF(qn + 2048); }
    if (g == 6) { q[2] = LDF(qn + 4096); q[3] = LDF(qn + 6144); }
    if (g == 7 && !LAST) q[4] = LDF(qn2);
#pragma unroll
    for (int i = 0; i < 4; ++i) {
      const int ii = (g & 1) ? 3 - i : i;
      if (g < 4) a0[ii][g] = MFMA(pc[ii], q[g], ZERO ? zero : a0[ii][g]); else a1[ii][g - 4] = MFMA(pc[ii], q[g], ZERO ? zero : a1[ii][g - 4]);
    }
    SGB(0x8, 1); SGB(0x100, 1); SGB(0x8, 1); SGB(0x100, 1); SGB(0x8, 2);
    __builtin_amdgcn_sched_barrier(0);
    if (g == 2 && BARRIER) { WAIT_LGKM0(); BAR(); }
    extra(g);
  }
}

template <bool JOBNEXT>
__device__ __forceinline__ void small_step(f32x4 (&a)[4][4], bf16x8 (&p0)[4], bf16x8 (&p1)[4], bf16x8 (&q)[8], const char* lds,
                                           int ph, int qh, int pn, int qn, int qn2) {
#pragma unroll
  for (int g = 0; g < 8; ++g) {
    if (g == 0) { q[4] = LDF(qh); p1[0] = LDF(ph); }
    if (g == 1) { p1[1] = LDF(ph + 2048); p1[2] = LDF(ph + 4096); }
    if (g == 2) { p1[3] = LDF(ph + 6144); q[5] = LDF(qh + 2048); }
    if (g == 3) { q[6] = LDF(qh + 4096); q[7] = LDF(qh + 6144); }
    if (g == 4) { p0[0] = LDF(pn); q[0] = LDF(qn); }
    if (g == 5) { p0[1] = LDF(pn + 2048); p0[2] = LDF(pn + (JOBNEXT ? 1024 : 4096)); if (JOBNEXT) q[1] = LDF(qn + 2048); }
    if (g == 6) { p0[3] = LDF(pn + (JOBNEXT ? 3072 : 6144)); if (JOBNEXT) { q[2] = LDF(qn + 4096); q[3] = LDF(qn + 6144); } else q[1] = LDF(qn + 2048); }
    if (g == 7) { if (JOBNEXT) { q[4] = LDF(qn2); q[5] = LDF(qn2 + 2048); } else { q[2] = LDF(qn + 4096); q[3] = LDF(qn + 6144); } }
#pragma unroll
    for (int i = 0; i < 4; ++i) {
      const int ii = (g & 1) ? 3 - i : i;
      a[ii][g & 3] = MFMA(g < 4 ? p0[ii] : p1[ii], q[g], a[ii][g & 3]);
    }
    SGB(0x8, 1); SGB(0x100, 1); SGB(0x8, 1); SGB(0x100, 1); SGB(0x8, 1); SGB(0x100, 1); SGB(0x8, 1);
    __builtin_amdgcn_sched_barrier(0);
    if (g == 3) BAR();
  }
}

__device__ __forceinline__ void consumer(int wid, int lane, int b, char* lds, rsrc_t rws, rsrc_t rout,
                                         const char* code, int tp1, int tq1 STAMP_ARG) {
  STAMP_DECL;
  const int z = tq1 >> 3, te = tq1 & 7;
  const int wp = wid >> 1, wq = wid & 1, fr = lane & 15, fq = lane >> 4;
  const int c0 = (fq ^ (fr & 7)) * 16, c1 = ((fq + 4) ^ (fr & 7)) * 16;
  int q0 = (wq * 64 + fr) * 128 + c0, q1 = (wq * 64 + fr) * 128 + c1;
  volatile LAS unsigned* misc = (volatile LAS unsigned*)(lds + L_MISC);
  f32x4 acc[4][4];
#pragma unroll
  for (int i = 0; i < 4; ++i)
#pragma unroll
    for (int j = 0; j < 4; ++j) acc[i][j] = f32x4{0.f, 0.f, 0.f, 0.f};
  unsigned tag;
  asm volatile("" :: "s"(rout));
  u32x4 xv[4];
  {
    Frag f0, f1;
    const int xrow = wq * 64 + 16 * ((fr >> 2) & 1) + 4 * (fr >> 3) + (fr & 3);
    const int xr = xrow * 128 + (((z * 4 + fq) ^ (xrow & 7)) * 16);
#define XV(u) do { _Pragma("unroll") for (int k = 0; k < 4; ++k) \
      xv[k] = *reinterpret_cast<const u32x4*>(lds + (u) * UB + xr + (k >> 1) * 4096 + (k & 1) * 1024); } while (0)
    const int p0 = (wp * 64 + fr) * 128 + c0, p1 = (wp * 64 + fr) * 128 + c1;
    unsigned tch = 0;
    if (wid == 3 && lane < (CODE_LINES + 7) / 8) {
      __builtin_amdgcn_s_sleep(8);
      const int co = min(((b >> 3) & 7) * ((CODE_LINES + 7) / 8) + lane, CODE_LINES - 1) * 128;
      asm volatile("global_load_dword %0, %1, off" : "+v"(tch) : "v"(code + co) : "memory");
    }
    const int xt = (2 * te + wp - rot1(tp1, tq1)) & 15;
#define G1H(fc, fn, po, qo, LOAD) do { _Pragma("unroll") for (int g = 0; g < 8; ++g) { \
      if (LOAD) { if (g == 0) { fn.q[0] = LDF(qo); fn.p[0] = LDF(po); } else if (g == 1) { fn.p[1] = LDF((po) + 2048); fn.p[2] = LDF((po) + 4096); } \
        else if (g == 2) { fn.p[3] = LDF((po) + 6144); fn.q[1] = LDF((qo) + 2048); } else if (g == 3) { fn.q[2] = LDF((qo) + 4096); fn.q[3] = LDF((qo) + 6144); } } \
      _Pragma("unroll") for (int e = 0; e < 2; ++e) { const int i0 = (g & 1) * 2 + e, ii = (g & 2) ? 3 - i0 : i0; \
        acc[ii][g >> 1] = MFMA(fc.p[ii], fc.q[g >> 1], acc[ii][g >> 1]); } \
      SGB(0x8, 1); SGB(0x100, 1); SGB(0x8, 1); SGB(0x100, 2); __builtin_amdgcn_sched_barrier(0); } } while (0)
#define XI(u) ((u) == 0 ? 0 : (u) == 1 ? 3 : (u) == 2 ? 2 : 7)
#define WI(u) ((u) == 0 ? 1 : (u) == 1 ? 4 : (u) == 2 ? 5 : 8)
#define XVC(t, u) do { if (__builtin_expect((t) == xt, 0)) { XV(XI(u)); PIN4(xv[0], xv[1], xv[2], xv[3]); } __builtin_amdgcn_sched_barrier(0); } while (0)
    int pA[2] = {p0, p1}, pB[2] = {p0 + 7 * UB, p1 + 7 * UB}, qA[2] = {q0 + UB, q1 + UB}, qB[2] = {q0 + 5 * UB, q1 + 5 * UB};
    PIN4(pA[0], pA[1], pB[0], pB[1]); PIN4(qA[0], qA[1], qB[0], qB[1]);
#define XA(u, h) ((u) == 3 ? pB[h] : pA[h] + XI(u) * UB)
#define WA(u, h) ((u) < 2 ? qA[h] + (u) * 3 * UB : qB[h] + ((u) - 2) * 3 * UB)
    STAMP(1);
    BAR();
    XVC(0, 0);
    f0.q[0] = LDF(WA(0, 0));
#pragma unroll
    for (int i = 0; i < 4; ++i) f0.p[i] = LDF(XA(0, 0) + i * 2048);
#pragma unroll
    for (int i = 1; i < 4; ++i) f0.q[i] = LDF(WA(0, 0) + i * 2048);
    __builtin_amdgcn_sched_barrier(0);
    u32x2 rdy;
#pragma unroll 1
    for (int t = 0;; t += 4) {
#pragma unroll
      for (int u = 0; u < 4; ++u) {
        if (u == 3) rdy = *(const volatile LAS u32x2*)(lds + L_MISC + 16);
        G1H(f0, f1, XA(u, 1), WA(u, 1), true);
        WAIT_LGKM0();
        BAR();
        if (u == 3 && t == 12) goto g1_done;
        XVC(t + u + 1, (u + 1) & 3);
        G1H(f1, f0, XA((u + 1) & 3, 0), WA((u + 1) & 3, 0), true);
      }
    }
g1_done:
    asm volatile("s_waitcnt vmcnt(0)" : "+v"(tch) :: "memory");
    if (!rfl(rdy[0] & rdy[1])) {
#pragma unroll 1
      for (int spins = 0; spins < SPIN_CAP && !rfl(misc[4] & misc[5]); ++spins) __builtin_amdgcn_s_sleep(1);
    }
    asm volatile("" ::: "memory");
    f32x4 mb[2][2], rd[2][2]; float cs[4];
    const unsigned tagv = *(const LAS unsigned*)(lds + L_MISC + 8);
#pragma unroll
    for (int m = 0; m < 2; ++m)
#pragma unroll
      for (int h = 0; h < 2; ++h) {
        mb[m][h] = *reinterpret_cast<const f32x4*>(lds + L_MB + (wp * 64 + m * 32 + fq * 8 + h * 4) * 4);
        rd[m][h] = *reinterpret_cast<const f32x4*>(lds + L_MB + 512 + (wp * 64 + m * 32 + fq * 8 + h * 4) * 4);
      }
#pragma unroll
    for (int j = 0; j < 4; ++j)
      cs[j] = *reinterpret_cast<const float*>(lds + L_MB + 1024 + (wq * 64 + 32 * (j >> 1) + 8 * (fr >> 2) + 4 * (j & 1) + (fr & 3)) * 4);
    mma(acc, f1);
#pragma unroll
    for (int q_ = 0; q_ < 13; ++q_) { SGB(0x100, 1); SGB(0x8, 1); }
    __builtin_amdgcn_sched_barrier(0);
    STAMP(2);
    tag = rfl(tagv);
    const float sgn = z ? -1.f : 1.f;
    const int flip = z ? 0 : 4096;
    const int ao = WS_ABT + tq1 * ABT_ST + ((tp1 * 2 + wp) * 8 + wq * 4) * 2048 + lane * 16;
    char* own = lds + wp * SLOT + 2 * UB + wq * 8192 + lane * 16;
#pragma unroll
    for (int m = 0; m < 2; ++m) {
#pragma unroll
      for (int h = 0; h < 2; ++h) rd[m][h] *= sgn;
#pragma unroll
      for (int j = 0; j < 4; ++j) {
        const f32x4 v0 = (mb[m][0] * cs[j] + acc[2 * m][j]) * rd[m][0], v1 = (mb[m][1] * cs[j] + acc[2 * m + 1][j]) * rd[m][1];
        const u32x4 o = pack8(v0, v1);
        const int fo = (j < 2 ? flip : -flip) + j * 2048 + m * 1024;
        __builtin_amdgcn_raw_buffer_store_b128(o, rws, ao + fo, 0, SC1);
        *reinterpret_cast<u32x4*>(own + fo) = o;
      }
    }
    STAMP(3);
  }
  f32x4 acb[4][4];
  bf16x8 pa[4], pb[4], q[8];
  int pp = wp * 8192 + lane * 16;
  bool sent = false;
#define FLAG(off, n) do { if (!sent && vm_now() <= (n)) { sent = true; wait_vm<n>(); \
    if (lane == 0) __builtin_amdgcn_raw_buffer_store_b32(tag, rws, off, 0, SC1); STAMP(FST); } } while (0)
#define FST 4
  const int f1o = WS_FLAG1 + (tq1 * 16 + tp1) * 128 + wid * 4;
  auto flag1 = [&](int g) { if (g & 1) FLAG(f1o, 0); };
  WAIT_LGKM0();
  BAR();
  STAMP(5);
  pa[0] = LDF(2 * UB + pp); q[0] = LDF(q0); pa[1] = LDF(2 * UB + pp + 2048); pa[2] = LDF(2 * UB + pp + 4096); pa[3] = LDF(2 * UB + pp + 6144);
  q[1] = LDF(q0 + 2048); q[2] = LDF(q0 + 4096); q[3] = LDF(q0 + 6144); q[4] = LDF(q0 + UB);
  __builtin_amdgcn_sched_barrier(0);
  auto none = [](int) {};
  big_half<false, false, true>(acc, acb, pa, pb, q, lds, q0, 2 * UB + pp + 1024, q1, q1 + UB, flag1);
  big_half<true, false, false>(acc, acb, pb, pa, q, lds, q1, SLOT + 2 * UB + pp, SLOT + q0, SLOT + q0 + UB, flag1);
  big_half<false, false, false>(acc, acb, pa, pb, q, lds, SLOT + q0, SLOT + 2 * UB + pp + 1024, SLOT + q1, SLOT + q1 + UB, flag1);
  if (!sent) { wait_vm<0>(); FLAG(f1o, 0); }
  PIN4(q0, q1, pp, lane);
  {
    int sl = SLOT;
#pragma unroll 1
    for (int n = 1; n < 12; ++n) {
      const int sn = sl == 2 * SLOT ? 0 : sl + SLOT;
      big_half<true, false, false>(acc, acb, pb, pa, q, lds, sl + q1, sn + 2 * UB + pp, sn + q0, sn + q0 + UB, none);
      big_half<false, false, false>(acc, acb, pa, pb, q, lds, sn + q0, sn + 2 * UB + pp + 1024, sn + q1, sn + q1 + UB, none);
      sl = sn;
    }
  }
  int q0h = q0 + 4 * UB, q1h = q1 + 4 * UB, pph = pp + 5 * UB;
  asm volatile("" : "+v"(q0h), "+v"(q1h), "+v"(pph));
  big_half<true, true, false>(acc, acb, pb, pa, q, lds, q1, pph, q0h, 0, none);
  small_step<false>(acb, pa, pb, q, lds, pph + 1024, q1h, pph + 3 * UB, q0h + 3 * UB, 0);
  small_step<true>(acb, pa, pb, q, lds, pph + 3 * UB + 1024, q1h + 3 * UB, pph, 3 * UB + q0, 3 * UB + q1);
  int xw = WS_XCH + b * XB + wid * 4096 + lane * 16;
  const int xl = WS_XCH + 32768 + wid * 5632 + lane * 16;
  asm volatile("" : "+v"(xw));
#define SEND_E(j) __builtin_amdgcn_raw_buffer_store_b128(pack8(acb[(j) < 4 ? 2 : 0][(j) & 3], acb[(j) < 4 ? 3 : 1][(j) & 3]), rws, xw + ((j) >> 2) * 16384 + ((j) & 3) * 1024, 0, SC1)
  STAMP(10);
  const int f2o = WS_FLAG2 + b * 128 + wid * 4;
#define PA(k) ((k) == 0 ? pph : (k) == 1 ? pph + 3 * UB : (k) == 2 ? pp + 2 * UB : pp + UB)
#define QA0(k) ((k) == 0 ? q0 + 3 * UB : (k) == 1 ? q0h + 2 * UB : (k) == 2 ? q0 : q0h)
#define QA1(k) ((k) == 0 ? q1 + 3 * UB : (k) == 1 ? q1h + 2 * UB : (k) == 2 ? q1 : q1h)
#define JOB(K, I0, pc, qc, LC, pn, KN, IN, qx, LN, EXTRA) do { _Pragma("unroll") for (int g = 0; g < 8; ++g) { \
    if (g < 2 && (LC)) qc[g + 6] = LDF(QA1(K) + (g + 2) * 2048); \
    if ((KN) >= 0) { \
      if (g >= 2 && (LN)) qx[g - 2] = LDF(g < 6 ? QA0(KN) + (g - 2) * 2048 : QA1(KN) + (g - 6) * 2048); \
      if (g < 4) pn[g] = LDF(PA(KN) + ((IN) + (g & 1)) * 2048 + (g >> 1) * 1024); } \
    acc[I0][g & 3] = MFMA(pc[(g >> 2) * 2], qc[g], acc[I0][g & 3]); \
    acc[(I0) + 1][g & 3] = MFMA(pc[(g >> 2) * 2 + 1], qc[g], acc[(I0) + 1][g & 3]); \
    EXTRA(g); \
    SGB(0x8, 1); SGB(0x100, 1); SGB(0x2, 2); SGB(0x8, 1); SGB(0x100, 1); SGB(0x2, 2); \
    __builtin_amdgcn_sched_barrier(0); } \
    if ((KN) < 0) { PIN4(acc[2][0], acc[2][1], acc[2][2], acc[2][3]); PIN4(acc[3][0], acc[3][1], acc[3][2], acc[3][3]); } } while (0)
#define X_NONE(g)
  u32x4 e1[4], e2[4], ot[5]; u32x2 o5;
#define LT(i) pack2(acc[((i) >> 1) & 1][((i) >> 2) & 3][((i) & 1) * 2], acc[((i) >> 1) & 1][((i) >> 2) & 3][((i) & 1) * 2 + 1])
#define X_LATE(g) if ((g) < 5) __builtin_amdgcn_raw_buffer_store_b128(u32x4{LT(3 * (g)), LT(3 * (g) + 1), LT(3 * (g) + 2), tag}, rws, xl + b * XB + (g) * 1024, 0, SC1); \
  else if ((g) == 5) __builtin_amdgcn_raw_buffer_store_b64(u32x2{LT(15), tag}, rws, xl + b * XB + 5120 - lane * 8, 0, SC1); \
  else if ((g) == 6) __builtin_amdgcn_raw_buffer_store_b32(tag, rws, lane ? -1 : f2o + 16, 0, SC1)
#undef FST
#define FST 9
  sent = false;
#define X_E(n, g) if ((n) * 8 + (g) >= EG0 && ((n) * 8 + (g) - EG0) % EGD == 0 && ((n) * 8 + (g) - EG0) / EGD < 8) SEND_E(((n) * 8 + (g) - EG0) / EGD & 7)
#define X_E0(g) X_E(0, g)
#define X_E1(g) X_E(1, g)
  bf16x8 qb[8], qc_[8];
  JOB(0, 0, pa, q, 1, pb, 1, 0, q, 1, X_E0);
  JOB(1, 0, pb, q, 1, pa, 2, 0, qb, 1, X_E1);
  BAR();
  JOB(2, 0, pa, qb, 1, pb, 3, 0, qc_, 1, X_NONE);
#define FL(i, n) do { if ((i) < FBLK) { if (FCHK >> (i) & 1) FLAG(f2o, n); } else if ((i) == FBLK && !sent) { sent = true; wait_vm<n>(); \
    __builtin_amdgcn_raw_buffer_store_b32(tag, rws, lane ? -1 : f2o, 0, SC1); STAMP(FST); } } while (0)
  FL(0, 0);
  JOB(3, 0, pb, qc_, 1, pa, 3, 2, q, 0, X_NONE);
  FL(1, 0);
  JOB(3, 2, pa, qc_, 0, pb, 2, 2, qb, !KEEP, X_LATE);
  BAR();
  FL(2, 7);
  JOB(2, 2, pb, qb, !KEEP, pa, 1, 2, q, !KEEP, X_NONE);
  FL(3, 7);
  JOB(1, 2, pa, q, !KEEP, pb, 0, 2, qb, 1, X_NONE);
  FL(4, 7);
  float tm[4], tr[4]; unsigned selv;
  const char* tb = lds + L_MB + (wq * 64 + (lane & 15)) * 4;
#define X_TAB(g) if ((g) >= 2 && (g) < 6) { tm[((g) - 2) & 3] = *reinterpret_cast<const float*>(tb + ((g) - 2) * 64); tr[((g) - 2) & 3] = *reinterpret_cast<const float*>(tb + 512 + ((g) - 2) * 64); } \
  else if ((g) == 6) selv = misc[0]
  JOB(0, 2, pb, qb, 1, pa, -1, 0, q, 0, X_TAB);
#define FLAGU() do { if (__builtin_expect(!sent, 0)) FLAG(f2o, 7); } while (0)
  FLAGU();
  STAMP(6);
#define RECV(v, n, off) do { _Pragma("unroll") for (int k_ = 0; k_ < (n); ++k_) \
    v[k_] = __builtin_amdgcn_raw_buffer_load_b128(rws, (off) + (k_ & 3) * 1024, (k_ >> 2) * 4096, SC1); asm volatile("" ::: "memory"); } while (0)
#define RECV_LATE() do { o5 = __builtin_amdgcn_raw_buffer_load_b64(rws, xl + (b ^ 4) * XB + 5120 - lane * 8, 0, SC1); RECV(ot, 5, xl + (b ^ 4) * XB); } while (0)
  PIN4(xv[0], xv[1], xv[2], xv[3]);
  f32x2 gf[4][4];
#pragma unroll
  for (int k = 0; k < 4; ++k) {
    const float tn = tm[k] * tr[k];
#pragma unroll
    for (int c = 0; c < 4; ++c) {
      const f32x2 n = f32x2{lo2f(xv[k][c]), hi2f(xv[k][c])} * tr[k] + tn, a = n * -1.44269504f;
      const f32x2 d = f32x2{__builtin_amdgcn_exp2f(a[0]), __builtin_amdgcn_exp2f(a[1])} + 1.0f;
      gf[k][c] = n * f32x2{__builtin_amdgcn_rcpf(d[0]), __builtin_amdgcn_rcpf(d[1])};
    }
    PIN4(gf[k][0], gf[k][1], gf[k][2], gf[k][3]);
    if (k < 3) FLAGU();
  }
  if (__builtin_expect(!sent, 0)) { wait_vm<7>(); FLAG(f2o, 7); }
#pragma unroll 1
  for (int spins = 0; spins < SPIN_CAP && !rfl(misc[12]); ++spins) ;
  asm volatile("" ::: "memory");
  STAMP(7);
  {
    const char* ll = lds + (wp ? L_RX : 7 * UB) + wq * 5632 + lane * 16;
#pragma unroll
    for (int j = 0; j < 5; ++j) ot[j] = *reinterpret_cast<const u32x4*>(ll + j * 1024);
    o5 = *reinterpret_cast<const u32x2*>(ll + 5120 - lane * 8);
  }
#define LBAD() (!__all(((ot[0][3] ^ tag) | (ot[1][3] ^ tag) | (ot[2][3] ^ tag) | (ot[3][3] ^ tag) | (ot[4][3] ^ tag) | (o5[1] ^ tag)) == 0))
  if (__builtin_expect(LBAD(), 0)) {
#pragma unroll 1
    for (int spins = 0; spins < SPIN_CAP / 16; ++spins) {
      asm volatile("" ::: "memory");
      RECV_LATE();
      if (!LBAD()) break;
    }
    asm volatile("" : "+v"(ot[0]), "+v"(ot[1]), "+v"(ot[2]), "+v"(ot[3]), "+v"(ot[4]), "+v"(o5));
  }
  f32x2 sv[4][4], xf[4][4];
#pragma unroll
  for (int k = 0; k < 4; ++k) {
#pragma unroll
    for (int c = 0; c < 4; ++c) {
      const unsigned a3 = 4 * k + c < 15 ? ot[(4 * k + c) / 3 % 5][(4 * k + c) % 3] : o5[0];
      sv[k][c] = f32x2{acc[2 + (c >> 1)][k][(c & 1) * 2], acc[2 + (c >> 1)][k][(c & 1) * 2 + 1]} + f32x2{lo2f(a3), hi2f(a3)};
      xf[k][c] = f32x2{lo2f(xv[k][c]), hi2f(xv[k][c])};
    }
    PIN4(sv[k][0], sv[k][1], sv[k][2], sv[k][3]);
    PIN4(xf[k][0], xf[k][1], xf[k][2], xf[k][3]);
  }
  {
    unsigned sel = rfl(selv);
    if (__builtin_expect(!sel || (int)(sel - 1 >> 6) == tp1 * 2 + wq, 0)) {
#pragma unroll 1
      for (int spins = 0; spins < SPIN_CAP && !sel; ++spins) { sel = rfl(misc[0]); __builtin_amdgcn_s_sleep(2); }
      const int idx = (int)sel - 1;
      if ((idx >> 6) == tp1 * 2 + wq) {
        const int ks = (idx >> 4) & 3;
        const u32x4 xs = ks == 0 ? xv[0] : ks == 1 ? xv[1] : ks == 2 ? xv[2] : xv[3];
        const float ms = ks == 0 ? tm[0] : ks == 1 ? tm[1] : ks == 2 ? tm[2] : tm[3], rs = ks == 0 ? tr[0] : ks == 1 ? tr[1] : ks == 2 ? tr[2] : tr[3];
        u32x4 n;
#pragma unroll
        for (int c = 0; c < 4; ++c) n[c] = ln_pair(xs[c], ms, rs);
        asm volatile("" : "+v"(n));
#pragma unroll 1
        for (int spins = 0; spins < SPIN_CAP && !rfl(misc[1]); ++spins) __builtin_amdgcn_s_sleep(1);
        asm volatile("" ::: "memory");
        const LAS float* upd = (const LAS float*)(lds + L_UPD) + wp * 32 + (lane >> 4) * 8;
#pragma unroll
        for (int c = 0; c < 4; ++c) {
          const unsigned u = pack2(lo2f(n[c]) + upd[2 * c], hi2f(n[c]) + upd[2 * c + 1]);
          const f32x2 gn = {silu_fast(lo2f(u)), silu_fast(hi2f(u))};
#pragma unroll
          for (int k = 0; k < 4; ++k) if (ks == k && (idx & 15) == (lane & 15)) gf[k][c] = gn;
        }
      }
    }
  }
  int xo = ((tp1 * 128 + wq * 64 + (lane & 15)) * D + te * 128 + wp * 64 + z * 32 + (lane >> 4) * 8) * 2, le = UB + wid * 4096 + lane * 16;
  asm volatile("" : "+v"(xo), "+v"(le));
#pragma unroll 1
  for (int spins = 0; spins < SPIN_CAP && !rfl(misc[13]); ++spins) ;
  asm volatile("" ::: "memory");
  STAMP(11);
#pragma unroll
  for (int k = 0; k < 4; ++k) { e1[k] = *reinterpret_cast<const u32x4*>(lds + le + k * 1024); e2[k] = *reinterpret_cast<const u32x4*>(lds + le + 3 * UB + k * 1024); }
#pragma unroll
  for (int k = 0; k < 4; ++k) {
    u32x4 ov;
#pragma unroll
    for (int c = 0; c < 4; ++c) {
      const f32x2 r = ((sv[k][c] + f32x2{lo2f(e1[k][c]), hi2f(e1[k][c])}) + f32x2{lo2f(e2[k][c]), hi2f(e2[k][c])}) * gf[k][c] + xf[k][c];
      ov[c] = pack2(r[0], r[1]);
    }
    __builtin_amdgcn_raw_buffer_store_b128(ov, rout, xo + k * 16 * D * 2, 0, OPOL);
  }
  STAMP(8);
  STAMP_OUT();
}

#define BCAST(v, k) ((unsigned)__builtin_amdgcn_mov_dpp((int)(v), 0x150 + (k), 0xf, 0xf, true))
template <int K0, int K1, int N>
__device__ __forceinline__ void gemv(f32x4& ae, f32x4& ao, unsigned xs, const unsigned (&sel)[4], const u32x4 (&w)[N]) {
  if constexpr (K0 < K1) {
    const unsigned v = BCAST(xs, K0);
    const bf16x8 dg = BF8((u32x4{__builtin_amdgcn_perm(0u, v, sel[0]), __builtin_amdgcn_perm(0u, v, sel[1]),
                                 __builtin_amdgcn_perm(0u, v, sel[2]), __builtin_amdgcn_perm(0u, v, sel[3])}));
    ae = MFMA(dg, BF8(w[2 * K0]), ae); ao = MFMA(dg, BF8(w[2 * K0 + 1]), ao);
    gemv<K0 + 1, K1, N>(ae, ao, xs, sel, w);
  }
}

__device__ __forceinline__ void helper(int hw, int lane, int b, char* lds, rsrc_t rws, rsrc_t rx, const u16* wr,
                                       const u16* wgu, const u16* wdn, const u16* gq, const char* code, int tp1, int tq1 STAMP_ARG) {
  STAMP_DECL;
  int nb = 0, lim = 17;
  unsigned tch = 0;
  volatile LAS unsigned* misc = (volatile LAS unsigned*)(lds + L_MISC);
  __builtin_amdgcn_s_setprio(3);
  const int z = tq1 >> 3, te = tq1 & 7;
  const int lc = lane & 15, lq = lane >> 4;
  const int r1 = rot1(tp1, tq1), kb = r1 >> 2, amo = WS_AMAX + (tq1 * 16 + tp1) * 128;
#define JOIN() do { HPRE(); BAR(); HSTAMP(); ++nb; } while (0)
#define NEXT() do { if (nb < lim) JOIN(); } while (0)
#define UNTIL(m) do { _Pragma("unroll 1") while (nb < (m)) JOIN(); } while (0)
#define POLLVM(m) do { _Pragma("unroll 1") while (vm_now() > (m) && nb < lim) JOIN(); wait_vm<m>(); } while (0)
  unsigned tag, key, sg; int idx;
  float sw;
  const int rg = b / 28, cg = b - rg * 28;
  const int fg = b >> 3, eg = b & 7;
  u32x4 w[32], wd[14];
  const rsrc_t rgu = mk_rsrc(wgu + (size_t)(rg & 7) * 128 * F + cg * 128, b < 224 ? 128 * F * 2 : 0);
  const rsrc_t rd = mk_rsrc(wdn + (size_t)(fg * 112 + hw * 56) * D + eg * 128, 56 * D * 2);
  const int wvo = lq * 32 * F * 2 + lc * 16, dvo = lq * 14 * D * 2 + lc * 16;
#define LOADW(k0, k1) do { _Pragma("unroll") for (int k = k0; k < (k1); ++k) \
    w[k] = __builtin_amdgcn_raw_buffer_load_b128(rgu, wvo, k * F * 2, WPOL); asm volatile("" ::: "memory"); } while (0)
#define LOADD(k0, k1) do { _Pragma("unroll") for (int k = k0; k < (k1); ++k) \
    wd[k] = __builtin_amdgcn_raw_buffer_load_b128(rd, dvo, k * D * 2, WPOL); asm volatile("" ::: "memory"); } while (0)
  {
    const rsrc_t rr = mk_rsrc(wr, D * 2);
    u32x4 wf[8];
#pragma unroll
    for (int i = 0; i < 8; ++i) wf[i] = __builtin_amdgcn_raw_buffer_load_b128(rr, lq * 16 + (i & 3) * 64, ((r1 + (i >> 2) * 2) & 15) * 128, 0);
    const unsigned epoch = __builtin_amdgcn_raw_buffer_load_b32(rws, lane == 1 ? amo : lane == 2 ? (hw ? amo + (WS_FLAG1 - WS_AMAX) : WS_FLAG2 + b * 128) : WS_EPOCH, 0, SC1);
    const u32x4 ra = __builtin_amdgcn_raw_buffer_load_b128(rr, lane * 16, 0, 0), rb2 = __builtin_amdgcn_raw_buffer_load_b128(rr, lane * 16 + 1024, 0, 0);
    misc[lc] = 0u;
    u32x4 ones = {0x3f803f80u, 0x3f803f80u, 0x3f803f80u, 0x3f803f80u};
    const int sx = (KM == 4 ? (te >> 2) + 2 * z : te >> 1) * 32 + hw * 16, sv = (KM == 4 ? tp1 >> 2 : tp1 >> 1 & 3) * 32 + hw * 16;
    int xa[2], wa[2];
    xa[0] = (sx + lc) * 128 + (lq ^ (lc & 7)) * 16; wa[0] = xa[0] + (sv - sx) * 128 + UB;
    xa[1] = xa[0] ^ 64; wa[1] = wa[0] ^ 64;
    asm volatile("" : "+v"(ones));
    f32x4 d1 = {0.f, 0.f, 0.f, 0.f}, d2 = d1, d3 = d1, d4 = d1;
    HST(9);
#pragma unroll
    for (int u = 0; u < 4; ++u) {
      if (u == 2) { wa[0] += 4 * UB; wa[1] += 4 * UB; }
      if (u == 3) { xa[0] += 7 * UB; xa[1] += 7 * UB; }
      PIN4(xa[0], xa[1], wa[0], wa[1]);
      WAIT_LGKM0();
      JOIN();
      const unsigned tj = (unsigned)__builtin_amdgcn_s_memrealtime();
      bf16x8 xf[2], vf[2];
#pragma unroll
      for (int h = 0; h < 2; ++h) { xf[h] = LDF(xa[h] + (u == 3 ? 0 : XI(u) * UB)); vf[h] = LDF(wa[h] + (u & 1) * 3 * UB); }
      __builtin_amdgcn_sched_barrier(0);
      if (u) LOADW((int)(SWT >> 8 * u & 63), (int)(SWT >> 8 * u + 8 & 63));
#pragma unroll
      for (int h = 0; h < 2; ++h) {
        d2 = MFMA(xf[h], xf[h], d2); d4 = MFMA(BF8(ones), xf[h], d4); d3 = MFMA(BF8(ones), vf[h], d3); d1 = MFMA(BF8(wf[2 * u + h]), xf[h], d1);
      }
      PIN4(d1, d2, d3, d4);
      if (u == 0) {
        tag = mk_tag(epoch);
        asm volatile("" : "+s"(tag));
        misc[2] = tag;
        const f32x4 d5 = MFMA(BF8(rb2), BF8(ones), MFMA(BF8(ra), BF8(ones), (f32x4{0.f, 0.f, 0.f, 0.f})));
        const int s4 = __builtin_bit_cast(int, (d5[0] + d5[1]) + (d5[2] + d5[3]));
#define RL(l) __builtin_bit_cast(float, __builtin_amdgcn_readlane(s4, l))
        sw = (RL(0) + RL(16)) + (RL(32) + RL(48));
        asm volatile("" : "+v"(sw));
        LOADW(0, (int)(SWT >> 8 & 63));
      }
      if (PI >> 8 * u & 255) {
#pragma unroll 1
        while ((unsigned)__builtin_amdgcn_s_memrealtime() - tj < (PI >> 8 * u & 255)) ;
      }
      __builtin_amdgcn_sched_barrier(0);
    }
    JOIN();
    const float sq = (lc & 2) ? ((lc & 1) ? d2[3] : d2[2]) : ((lc & 1) ? d2[1] : d2[0]);
    __builtin_amdgcn_raw_buffer_store_b128(u32x4{__float_as_uint(d4[0]), __float_as_uint(sq), __float_as_uint(d1[0]), tag}, rws,
                                           lq == lc >> 2 ? WS_XP + (tp1 * 4 + kb) * 2048 + (sx + lc) * 16 : -1, 0, SC1);
    __builtin_amdgcn_raw_buffer_store_b64(u32x2{__float_as_uint(d3[0]), tag}, rws, lane < 16 ? WS_WP + (tq1 * 4 + kb) * 1024 + (sv + lc) * 8 : -1, 0, SC1);
    __builtin_amdgcn_sched_barrier(0);
    LOADW((int)(SWT >> 32 & 63), (int)(SWT >> 40 & 63));
    if (SWT >> 48) { JOIN(); LOADW((int)(SWT >> 40 & 63), (int)(SWT >> 48 & 63)); }
    if (SWT >> 56) { JOIN(); LOADW((int)(SWT >> 48 & 63), (int)(SWT >> 56 & 63)); }
  }
  const rsrc_t rq = mk_rsrc(gq, QROWS * S * 2);
  const unsigned kt = ktab(tp1, te);
  const int trow = (te >> 2) * 1024 + (te & 3) * 32 + hw * 16;
  const int t1o = (trow + ((lane >> 1) & 15)) * (S * 2) + (lane & 1) * 128;
#define QTOUCH0(o, so) asm volatile("buffer_load_dword %0, %1, %2, %3 offen" : "+v"(tch) : "v"(o), "s"(rq), "s"(so) : "memory")
#define QTOUCH1(o, so) asm volatile("buffer_load_dword %0, %1, %2, %3 offen nt\n\tbuffer_load_dword %0, %1, %2, %3 offen" : "+v"(tch) : "v"(o), "s"(rq), "s"(so) : "memory")
#define QTOUCH(o, so) do { if (DTM >> i_ & 1) QTOUCH1(o, so); else QTOUCH0(o, so); } while (0)
#define TCH(m) do { _Pragma("unroll") for (int i_ = 0; i_ < 8; ++i_) if ((m) >> i_ & 1) { \
      if (i_ < 7) QTOUCH(t1o, kstep(kt, 2 * i_) * 128); \
      else QTOUCH(lane & 16 ? ((tp1 >> 3) * 1024 + te * 16 + lc) * (S * 2) + (kstep(kt, 15) ^ hw * 16) * 128 : (trow + lc) * (S * 2) + kstep(kt, 14) * 128, 0); } } while (0)
  const int srow = hw * 64 + (lane & 35) + 2 * (lane & 12) + ((lane >> 2) & 4);
  {
    const int gxo = WS_XP + tp1 * 8192 + (hw * 64 + lane) * 16, gwo = WS_WP + tq1 * 4096 + (hw * 64 + lane) * 8;
    u32x4 gx[4]; u32x2 gw[4];
#define GATHER() asm volatile("buffer_load_dwordx4 %0, %8, %10, 0 offen sc1\n\tbuffer_load_dwordx4 %1, %8, %10, 0 offen offset:2048 sc1\n\t" \
                 "buffer_load_dwordx4 %2, %8, %10, %11 offen sc1\n\tbuffer_load_dwordx4 %3, %8, %10, %11 offen offset:2048 sc1\n\t" \
                 "buffer_load_dwordx2 %4, %9, %10, 0 offen sc1\n\tbuffer_load_dwordx2 %5, %9, %10, 0 offen offset:1024 sc1\n\t" \
                 "buffer_load_dwordx2 %6, %9, %10, 0 offen offset:2048 sc1\n\tbuffer_load_dwordx2 %7, %9, %10, 0 offen offset:3072 sc1" \
                 : "=&v"(gx[0]), "=&v"(gx[1]), "=&v"(gx[2]), "=&v"(gx[3]), "=&v"(gw[0]), "=&v"(gw[1]), "=&v"(gw[2]), "=&v"(gw[3]) \
                 : "v"(gxo), "v"(gwo), "s"(rws), "s"(4096) : "memory")
    UNTIL(sched(UP_T, te));
#define TCH7(i) do { if (T7 >= 0 && te == 7) { const int i_ = T7 & 7, o_ = (lane >> 1) * (S * 2) + (lane & 1) * 128, so_ = ((i) * 1024 + hw * 64) * (S * 2) + kstep(kt, 2 * T7 & 15) * 128; \
      QTOUCH(o_, so_); QTOUCH(o_, so_ + 32 * S * 2); } } while (0)
    TCH(TM0);
    TCH7(0);
    LOADW(SWE, NWA1);
    if (NWA > NWA1) { JOIN(); LOADW(NWA1, NWA); }
    UNTIL(sched(T1_T, te) - GAD);
    GATHER();
    UNTIL(sched(T1_T, te));
    TCH(TM1);
    LOADW(NWA, NWC);
    wait_vm<NWC - NWA + __builtin_popcount(TM1) + __builtin_popcount(TM1 & DTM)>();
    TCH7(1);
#pragma unroll 1
    for (int spins = 0;; ++spins) {
      asm volatile("" : "+v"(gx[0]), "+v"(gx[1]), "+v"(gx[2]), "+v"(gx[3]), "+v"(gw[0]), "+v"(gw[1]), "+v"(gw[2]), "+v"(gw[3]) :: "memory");
      unsigned bad = 0;
#pragma unroll
      for (int q = 0; q < 4; ++q) bad |= (gx[q][3] ^ tag) | (gw[q][1] ^ tag);
      if (__builtin_expect(__all(bad == 0) || spins > SPIN_CAP / 64, 1)) break;
      GATHER();
      NEXT();
      POLLVM(0);
    }
    HST(3);
#define SUM4(v, c) ((__uint_as_float(v[0][c]) + __uint_as_float(v[1][c])) + (__uint_as_float(v[2][c]) + __uint_as_float(v[3][c])))
    float sum = SUM4(gx, 0), sq = SUM4(gx, 1), dot = SUM4(gx, 2);
    *(LAS float*)(lds + L_MB + 1024 + srow * 4) = SUM4(gw, 0);
    asm volatile("" : "+v"(sum), "+v"(sq), "+v"(dot));
    const float mn = sum * (1.0f / D);
    const float var = fmaxf(fmaf(-mn, mn, sq * (1.0f / D)), 0.f);
    const float mbv = rbf(mn), den = rbf(__builtin_amdgcn_sqrtf(rbf(rbf(var) + 1e-5f))), rden = __builtin_amdgcn_rcpf(den);
    *(LAS float*)(lds + L_MB + srow * 4) = -mbv;
    *(LAS float*)(lds + L_MB + 512 + srow * 4) = rden;
    asm volatile("" ::: "memory");
    misc[4 + hw] = 1u;
    float rs = (dot - mbv * sw) * rden;
    sg = pack2(mbv, den);
    asm volatile("" : "+v"(rs), "+v"(sg));
    lim = NBAR;
    const unsigned rb = pack2(rs, rs) & 0xffffu;
    unsigned srt = (rb & 0x8000u) ? (~rb & 0xffffu) : (rb | 0x8000u);
    if (rs != rs) srt = 0u;
    key = (srt << 16) | (unsigned)(0xffff - tp1 * 128 - srow);
    const unsigned mk = wave_maxu(key);
    if (key == mk) __builtin_amdgcn_raw_buffer_store_b128(u32x4{key, tag, sg, tag}, rws, amo + hw * 16, 0, SC1);
    HST(2);
    UNTIL(WB_AT);
    TCH(TMB);
    LOADW(NWC, NWD);
  }
  UNTIL(AM_AT);
#pragma unroll 1
  for (int spins = 0;; ++spins) {
    const u32x4 am = __builtin_amdgcn_raw_buffer_load_b128(rws, WS_AMAX + tq1 * 2048 + ((lane >> 1) & 15) * 128 + (lane & 1) * 16, 0, SC1);
    if (nb != 19) { NEXT(); POLLVM(0); }
    key = am[0]; sg = am[2];
    if (__builtin_expect(__all(((am[1] ^ tag) | (am[3] ^ tag)) == 0) || spins > SPIN_CAP / 64, 1)) break;
    if (nb == 19) JOIN();
  }
  {
    const unsigned mk = wave_maxu(key);
    sg = __builtin_amdgcn_readlane(sg, __builtin_ctzll(__ballot(key == mk)));
    key = mk;
  }
  idx = min(0xffff - (int)(key & 0xffffu), S - 1);
  const unsigned xe = __builtin_amdgcn_raw_buffer_load_b32(rx, (idx * D + (rg & 7) * 128) * 2 + lane * 4, 0, 0);
  if (lane == 0) misc[0] = idx + 1;
  if (b == 0 && lane == 0 && hw == 1) __builtin_amdgcn_raw_buffer_store_b32(tag, rws, WS_EPOCH, 0, SC1);
  unsigned sel[4];
  UNTIL(17);
#pragma unroll
  for (int d = 0; d < 4; ++d) {
    const unsigned src = (lc & 2) ? 0x0302u : 0x0100u;
    sel[d] = (lc >> 2) == d ? ((lc & 1) ? (src << 16 | 0x0c0cu) : (0x0c0c0000u | src)) : 0x0c0c0c0cu;
  }
  const int hc = fg * 112 + hw * 56 + (lq * 7 + min(lc & 7, 6)) * 2;
  int guo = WS_GU + ((lc >> 3) * F + (hc & ~127)) * 8 + ((hc & 6) * 8 + ((hc >> 3) & 15)) * 16;
  PIN4(sel[0], sel[1], sel[2], sel[3]);
  asm volatile("" : "+v"(guo));
  if (NWD < 32 || WD0 || TM18) { UNTIL(18); if (B0SL) __builtin_amdgcn_s_sleep(B0SL); LOADW(NWD, 32); TCH(TM18); LOADD(0, WD0); }
  HST(7);
  UNTIL(19);
  {
    const bool idle = nb == 19;
    if (idle) { if (GV_SL) __builtin_amdgcn_s_sleep(GV_SL); } else { NEXT(); POLLVM(0); }
    const unsigned xs = ln_pair(xe, -lo2f(sg), __builtin_amdgcn_rcpf(hi2f(sg)));
    f32x4 ae = {0.f, 0.f, 0.f, 0.f}, ao = ae;
#define GCUT() do { asm volatile("" : "+v"(ae), "+v"(ao)); if (__builtin_expect(!idle, 0)) NEXT(); } while (0)
    gemv<0, 4>(ae, ao, xs, sel, w); GCUT(); gemv<4, 8>(ae, ao, xs, sel, w); GCUT(); gemv<8, 12>(ae, ao, xs, sel, w); GCUT(); gemv<12, 16>(ae, ao, xs, sel, w);
    if (b < 224) __builtin_amdgcn_raw_buffer_store_b128(u32x4{__float_as_uint(ae[0] + ao[2]), tag, __float_as_uint(ae[1] + ao[3]), tag}, rws,
                                                        WS_GU + ((rg * 2 + hw) * F + cg * 128) * 8 + lane * 16, 0, SC1);
  }
  HST(4);
  if (nb == 19) JOIN();
  {
    const int m0_ = 18 + MB0 + (te & MBS);
#define MB(i) do { if ((MBT >> 8 * (i) & 255) || (MBW >> 4 * (i) & 15) != (MBW >> 4 * (i) + 4 & 15)) { UNTIL(m0_ + MBD * (i)); if (BSL2) __builtin_amdgcn_s_sleep(BSL2); \
      TCH(MBT >> 8 * (i) & 255); LOADD((int)(MBW >> 4 * (i) & 15), (int)(MBW >> 4 * (i) + 4 & 15)); } } while (0)
    MB(0); MB(1); MB(2); MB(3); MB(4); MB(5); MB(6); MB(7);
    if (!hw && lane < (CODE_LINES + 7) / 8) {
      const int co = min(((b >> 3) & 7) * ((CODE_LINES + 7) / 8) + lane, CODE_LINES - 1) * 128;
      asm volatile("global_load_dword %0, %1, off" : "+v"(tch) : "v"(code + co) : "memory");
    }
    UNTIL(m0_ + GGD);
  }
  {
    float s0, s1;
#pragma unroll 1
    for (int spins = 0;; ++spins) {
#pragma unroll
      for (int k = 0; k < 8; ++k) w[k] = __builtin_amdgcn_raw_buffer_load_b128(rws, guo, k * 2 * F * 8, SC1);
      NEXT();
      POLLVM(0);
      unsigned bad = 0; s0 = 0.f; s1 = 0.f;
#pragma unroll
      for (int k = 0; k < 8; ++k) { bad |= (w[k][1] ^ tag) | (w[k][3] ^ tag); s0 += __uint_as_float(w[k][0]); s1 += __uint_as_float(w[k][2]); }
      if (__builtin_expect(__all(bad == 0) || spins > SPIN_CAP / 64, 1)) break;
    }
    asm volatile("" : "+v"(s0), "+v"(s1));
    HST(8);
    NEXT();
    const unsigned gp = pack2(s0, s1);
    const unsigned up = (unsigned)__builtin_amdgcn_mov_dpp((int)gp, 0x128, 0xf, 0xf, true);
    const unsigned sl2 = pack2(silu_fast(lo2f(gp)), silu_fast(hi2f(gp)));
    const unsigned hp = pack2(lo2f(sl2) * lo2f(up), hi2f(sl2) * hi2f(up));
    f32x4 ae = {0.f, 0.f, 0.f, 0.f}, ao = ae;
    gemv<0, 2>(ae, ao, hp, sel, wd);
    asm volatile("" : "+v"(ae), "+v"(ao));
    NEXT();
    gemv<2, 7>(ae, ao, hp, sel, wd);
    f32x2 a = {ae[0] + ao[2], ae[1] + ao[3]};
    HST(5);
    if (hw == 1) {
      *(LAS f32x2*)(lds + L_CMB + lane * 8) = a;
      asm volatile("" ::: "memory");
      misc[10] = 1u;
    } else {
      f32x2 o;
#pragma unroll 1
      for (int spins = 0; spins < SPIN_CAP; ++spins) {
        NEXT();
        const unsigned f = misc[10];
        o = *(volatile LAS f32x2*)(lds + L_CMB + lane * 8);
        if (rfl(f)) break;
      }
      __builtin_amdgcn_raw_buffer_store_b128(u32x4{__float_as_uint(a[0] + o[0]), tag, __float_as_uint(a[1] + o[1]), tag}, rws,
                                             WS_PART + (fg * D + eg * 128) * 8 + lane * 16, 0, SC1);
      HST(6);
    }
  }
  if (hw == 1 && (idx >> 7) == tp1) {
    const unsigned srt = key >> 16;
    const float r = lo2f((srt & 0x8000u) ? (srt & 0x7fffu) : (~srt & 0xffffu));
    const float rsel = rbf(1.0f / (1.0f + __expf(-r)));
    const int cp = lane & 31;
    const int e = (cp >> 4) * 64 + z * 32 + (cp & 15) * 2;
    const int po = WS_PART + ((lane >> 5) * D + te * 128) * 8 + ((e & 6) * 8 + (e >> 3)) * 16;
    float y0, y1;
    UNTIL(FIN_AT);
#pragma unroll 1
    for (int spins = 0;; ++spins) {
#pragma unroll
      for (int k = 0; k < 16; ++k) w[k] = __builtin_amdgcn_raw_buffer_load_b128(rws, po, k * 2 * D * 8, SC1);
      NEXT();
      POLLVM(0);
      unsigned bad = 0;
#pragma unroll
      for (int k = 0; k < 16; ++k) bad |= (w[k][1] ^ tag) | (w[k][3] ^ tag);
      if (__all(bad == 0) || spins > SPIN_CAP / 64) break;
    }
    NEXT();
    y0 = 0.f; y1 = 0.f;
#pragma unroll
    for (int k = 0; k < 16; ++k) { y0 += __uint_as_float(w[k][0]); y1 += __uint_as_float(w[k][2]); }
    asm volatile("" : "+v"(y0), "+v"(y1));
    NEXT();
    { const auto p0 = __builtin_amdgcn_permlane32_swap(__float_as_uint(y0), __float_as_uint(y0), false, false);
      const auto p1 = __builtin_amdgcn_permlane32_swap(__float_as_uint(y1), __float_as_uint(y1), false, false);
      y0 = __uint_as_float(p0[0]) + __uint_as_float(p0[1]); y1 = __uint_as_float(p1[0]) + __uint_as_float(p1[1]); }
    *(LAS f32x2*)(lds + L_UPD + cp * 8) = f32x2{rbf(rbf(y0) * rsel), rbf(rbf(y1) * rsel)};
    asm volatile("" ::: "memory");
    misc[1] = 1u;
    HST(6);
  }
  HST(10);
  POLLVM(0);
  asm volatile("" : "+v"(tch));
  STAMP_OUT();
}

#define KA
__global__ __launch_bounds__(512, 2) void fused(KA const u16* __restrict__ x, KA const u16* __restrict__ hre, KA const u16* __restrict__ him,
                                                KA const u16* __restrict__ wr, KA char* ws, KA const u16* __restrict__ sre, KA const u16* __restrict__ sim,
                                                const u16* __restrict__ wg, const u16* __restrict__ wu, const u16* __restrict__ wdn, u16* out) {
  extern __shared__ __attribute__((aligned(16))) char lds[];
  u64 pc;
  asm volatile("s_getpc_b64 %0" : "=s"(pc));
  const char* code = (const char*)(pc & ~255ull);
  const int wid = __builtin_amdgcn_readfirstlane(threadIdx.x >> 6), lane = threadIdx.x & 63;
  const int b = blockIdx.x, g = b & 7, i = b >> 3;
  const int tp1 = (g & 3) * 4 + (i & 3), tq1 = (g >> 2) * 8 + (i >> 2);
  const int z = tq1 >> 3, te = tq1 & 7;
  asm volatile("" :: "s"(x), "s"(sre), "s"(sim), "s"(hre), "s"(him), "s"(wr), "s"(wg), "s"(wu), "s"(wdn), "s"(ws), "s"(out));
  const rsrc_t rws = mk_rsrc(ws, WS_END);
  const u16* wtile = (z ? him : hre) + (size_t)te * 128 * D;
  if (__builtin_expect(wid == 4 || wid == 5, 1)) {
    const int L = wid - 4;
    loader(L, lane, (LAS char*)lds, L ? wtile : x + (size_t)tp1 * 128 * D, (z ? sim : sre) + (size_t)(tp1 & 7) * 128 * S,
           ws + WS_ABT + (size_t)tq1 * ABT_ST, rws, WS_FLAG1 + tq1 * 2048, b, tp1, tq1 STAMP_PASS);
  } else if (wid < 4) {
    consumer(wid, lane, b, lds, rws, mk_rsrc(out, S * D * 2), code, tp1, tq1 STAMP_PASS);
  } else {
    const int hw = wid - 6;
    helper(hw, lane, b, lds, rws, mk_rsrc(x, S * D * 2), wr, hw ? wu : wg, wdn,
           (z ? sim : sre) + (size_t)(tp1 & 7) * 128 * S, code, tp1, tq1 STAMP_PASS);
  }
}

extern "C" void kernel_launch(void* const* d_in, const int* in_sizes, int n_in, void* d_out, int out_size,
                              void* d_ws, size_t ws_size, hipStream_t stream) {
  (void)hipFuncSetAttribute((const void*)fused, hipFuncAttributeMaxDynamicSharedMemorySize, LDS_BYTES);
  fused<<<256, 512, LDS_BYTES, stream>>>((const u16*)d_in[0], (const u16*)d_in[3], (const u16*)d_in[4], (const u16*)d_in[6], (char*)d_ws,
                                         (const u16*)d_in[1], (const u16*)d_in[2], (const u16*)d_in[7], (const u16*)d_in[8],
                                         (const u16*)d_in[9], (u16*)d_out);
}
```

```cpp
#define CODE_LINES 279
#include <hip/hip_runtime.h>
#include <hip/hip_bf16.h>
#include <stdint.h>

typedef __attribute__((ext_vector_type(8))) short bf16x8;
typedef __attribute__((ext_vector_type(4))) float f32x4;
typedef __attribute__((ext_vector_type(2))) float f32x2;
typedef __attribute__((ext_vector_type(4))) unsigned u32x4;
typedef __attribute__((ext_vector_type(2))) unsigned u32x2;
typedef __attribute__((ext_vector_type(2))) __bf16 bf16x2;
typedef unsigned short u16;
typedef unsigned long long u64;
typedef __amdgpu_buffer_rsrc_t rsrc_t;
#define LAS __attribute__((address_space(3)))

constexpr int S = 2048, D = 1024, F = 3584;
constexpr int UB = 16384;
constexpr int SLOT = 3 * UB;
constexpr int L_MISC = 9 * UB;
constexpr int L_UPD = L_MISC + 64;
constexpr int L_MB = L_MISC + 512;
constexpr int L_CMB = L_MB + 2048;
constexpr int L_RX = L_CMB + 512;
constexpr int LDS_BYTES = L_RX + 11264;
constexpr int NBAR = 17 + 14 + 4;
constexpr int SC1 = 16, NT = 2;

constexpr unsigned WS_EPOCH = 0;
constexpr unsigned WS_XP = 4096;
constexpr unsigned WS_WP = WS_XP + 16 * 8192;
constexpr unsigned WS_AMAX = WS_WP + 16 * 4096;
constexpr unsigned WS_FLAG1 = WS_AMAX + 32768;
constexpr unsigned WS_FLAG2 = WS_FLAG1 + 65536;
constexpr unsigned WS_GU = WS_FLAG2 + 32768;
constexpr unsigned WS_PART = WS_GU + 8 * 2 * F * 8;
constexpr unsigned WS_XCH = WS_PART + 32 * D * 8;
constexpr int XB = 32768 + 22528;
constexpr unsigned ABT_ST = 128 * S * 2;
constexpr unsigned WS_ABT = WS_XCH + 256 * XB;
constexpr unsigned WS_END = WS_ABT + 16 * ABT_ST;

#define STAMP(i)
#define HSTAMP()
#define HPRE()
#define HST(i)
#define STAMP_DECL
#define STAMP_ARG
#define STAMP_PASS
#define STAMP_OUT()

__device__ __forceinline__ float lo2f(unsigned v) { return __uint_as_float(v << 16); }
__device__ __forceinline__ float hi2f(unsigned v) { return __uint_as_float(v & 0xffff0000u); }
__device__ __forceinline__ unsigned pack2(float a, float b) {
  return __builtin_bit_cast(unsigned, __builtin_convertvector(f32x2{a, b}, bf16x2));
}
__device__ __forceinline__ float rbf(float f) { return lo2f(pack2(f, f)); }
__device__ __forceinline__ unsigned rfl(unsigned v) { return __builtin_amdgcn_readfirstlane(v); }
__device__ __forceinline__ rsrc_t mk_rsrc(const void* p, unsigned bytes) {
  return __builtin_amdgcn_make_buffer_rsrc((void*)p, 0, bytes, 0x00020000);
}
#define DPP_ADD(v, ctrl, rm, bc) v += __builtin_bit_cast(float, __builtin_amdgcn_update_dpp(0, __builtin_bit_cast(int, v), ctrl, rm, 0xf, bc))
#define DPP_MAXU(v, ctrl, rm, bc) v = max(v, (unsigned)__builtin_amdgcn_update_dpp(0, (int)v, ctrl, rm, 0xf, bc))
__device__ __forceinline__ unsigned wave_maxu(unsigned v) {
  DPP_MAXU(v, 0x111, 0xf, true); DPP_MAXU(v, 0x112, 0xf, true); DPP_MAXU(v, 0x114, 0xf, true); DPP_MAXU(v, 0x118, 0xf, true);
  DPP_MAXU(v, 0x142, 0xa, false); DPP_MAXU(v, 0x143, 0xc, false);
  return (unsigned)__builtin_amdgcn_readlane((int)v, 63);
}
template <int N> __device__ __forceinline__ void wait_vm() { asm volatile("s_waitcnt vmcnt(%0)" :: "n"(N) : "memory"); }
#define WAIT_LGKM0() asm volatile("s_waitcnt lgkmcnt(0)" ::: "memory")
#define BAR() do { asm volatile("" ::: "memory"); __builtin_amdgcn_s_barrier(); asm volatile("" ::: "memory"); } while (0)
#define MFMA(a, b, c) __builtin_amdgcn_mfma_f32_16x16x32_bf16(a, b, c, 0, 0, 0)
#define BF8(v) __builtin_bit_cast(bf16x8, v)
constexpr int SPIN_CAP = 1 << 20;
#define UP_T 0x0507060508070605ull
#define T1_T 0x090a09090a0a0909ull
#define GAD 0
#define NWA1 16
#define NWA 16
#define NWC 28
#define TM0 0
#define TM1 0
#define TMB 3
#define WB_AT 12
#define AM_AT 15
#define GV_SL 0
#define MB0 2
#define MBS 0
#define MBD 2
#define MBT 0x80402018ull
#define MBW 0xeeeeea500ull
#define BSL2 13
#define GGD 7
#define FIN_AT 35
#define EG0 1
#define EGD 1
#define PEARLY 0
#define T7 2
#define DTM 0
#define KEEP 1
#define FCHK 0
#define FBLK 1
#define WPOL NT
#define OPOL (SC1 | NT)
#define KM 6
#define QR 0
#define PI 0x2e32363c
#define SWT 0x100d0a070400ull
#define SWE (int)(SWT >> 56 ? SWT >> 56 : SWT >> 48 ? SWT >> 48 : SWT >> 40)
#define Q2E 1
#define FA_ 1
#define FB_ 7
#define AM_P 17
#define GX1 7
#define BDEF 0
#define XEJ 1
#define QS 0
#define WBS 0x11001100
__device__ __forceinline__ int sched(u64 tab, int te) { return (int)(tab >> (te * 8)) & 255; }
#define PIN4(a, b, c, d) asm volatile("" : "+v"(a), "+v"(b), "+v"(c), "+v"(d))
__device__ __forceinline__ unsigned vm_now() { const unsigned r = __builtin_amdgcn_s_getreg(7 | (31 << 11)); return (r & 15u) | ((r >> 18) & 0x30u); }
__device__ __forceinline__ unsigned mk_tag(unsigned epoch) { return rfl(epoch) * 0x9E3779B1u + 0x7F4A7C15u; }

template <int LD, int AUX, int MODE, int NP = 16, int J0 = 0>
__device__ __forceinline__ void issue(rsrc_t r, int voff, int koff, LAS char* dst) {
#pragma unroll
  for (int j = J0; j < NP; ++j) {
    const int so = MODE == 2 ? j * 1024 : MODE == 1 ? (32 * (j >> 3) + 64 * ((j >> 2) & 1) + 16 * (j & 1) + 4 * ((j >> 1) & 1)) * LD : j * 8 * LD;
    __builtin_amdgcn_raw_ptr_buffer_load_lds(r, (LAS void*)(dst + j * 1024), 16, voff, koff + so, 0, AUX);
  }
}
__device__ __forceinline__ unsigned ktab(int tp1, int te) {
  const int j = tp1 & 3;
  const unsigned t = (tp1 * 0x01010101u ^ 0x03020100u) | (((tp1 & 12) ^ 4) * 0x10101010u ^ (j == 1 ? 0x30002010u : j == 2 ? 0x30100020u : 0x30201000u));
  const unsigned m = t >> 4 & 0xffffffu, r = (QR >> 4 * te & 7) * 4;
  return (t & 0xf000000fu) | ((m >> r | m << (24 - r)) & 0xffffffu) << 4;
}
__device__ __forceinline__ int rot1(int tp1, int tq1) { return KM == 4 ? 4 * (tp1 + tq1) & 15 : (8 * (tp1 + tq1) + 2 * (((tp1 >> 2) + 2 * (tq1 >> 3)) & 3)) & 15; }
__device__ __forceinline__ int kstep0(unsigned kt, int n) { return (int)(kt >> (n >> 1) * 4 & 15) * 2 + (n & 1); }
__device__ __forceinline__ int kstep(unsigned kt, int n) { return kstep0(kt, n == 3 ? 4 : n == 4 ? 3 : n); }
constexpr int QROWS = 1024 + 128;

__device__ __forceinline__ void loader(int L, int lane, LAS char* lds, const u16* g1, const u16* gq, const char* gp, rsrc_t rws,
                                       int flag_off, int b, int tp1, int tq1 STAMP_ARG) {
  const int te = tq1 & 7;
  STAMP_DECL;
  __builtin_amdgcn_s_setprio(2);
  const int rsub = lane >> 3, ch = ((lane & 7) ^ rsub) * 16;
  const rsrc_t rq = mk_rsrc(gq, QROWS * S * 2);
  const int h = tp1 >> 3;
  unsigned kt = ktab(tp1, te);
  int vq = rsub * (S * 2) + ch, vp = lane * 16;
  int qa = h * 1024 * (S * 2), qb = ((1 - h) * 1024 + L * 64) * (S * 2);
  int q_ = (L ? qa : qb) + tp1 * 256;
  LAS char* d_ = lds + (1 - L) * UB;
  volatile LAS unsigned* misc = (volatile LAS unsigned*)(lds + L_MISC);
  unsigned fl = 0, tag;
  {
    const rsrc_t r1 = mk_rsrc(g1, 128 * D * 2);
    const int v1 = (8 * (rsub >> 2) + (rsub & 3)) * (D * 2) + ch;
    const unsigned utab = L ? 0x8541u : 0x7230u;
    STAMP(1);
    int ko = rot1(tp1, tq1) * 128;
#pragma unroll 1
    for (int t = 0; t < 4; ++t) {
      LAS char* dst = lds + ((utab >> (t * 4)) & 15) * UB;
      if (t >= 2) { wait_vm<16>(); BAR(); }
      issue<D * 2, 0, 1>(r1, v1, ko, dst);
      ko = (ko + 128) & 2047;
    }
    LAS char* dt[4];
#pragma unroll
    for (int u = 0; u < 4; ++u) dt[u] = lds + ((utab >> (u * 4)) & 15) * UB;
    asm volatile("" : "+s"(kt), "+s"(qa), "+s"(qb), "+s"(q_), "+s"(d_) : "s"(rq));
    asm volatile("" : "+v"(vq), "+v"(vp));
#pragma unroll 1
    for (int t = 0; t < 3; ++t) {
#pragma unroll
      for (int u = 0; u < 4; ++u) {
        issue<D * 2, 0, 1, 8>(r1, v1, ko, dt[u]);
        int kn = (ko + 128) & 2047;
        asm volatile("" : "+s"(kn), "+s"(dt[(u + 1) & 3]));
        if (u == 1) asm volatile("ds_read_b32 %0, %1" : "=v"(tag) : "v"(L_MISC + 8) : "memory");
        issue<D * 2, 0, 1, 16, 8>(r1, v1, ko, dt[u]);
        ko = kn;
        wait_vm<32>();
        BAR();
      }
    }
    asm volatile("s_waitcnt lgkmcnt(0)" : "+v"(tag) :: "memory");
  }
#define POLL(o) asm volatile("buffer_load_dword %0, %1, %2, 0 offen sc1" : "+v"(fl) : "v"(o), "s"(rws) : "memory")
#define POLLW(o) asm volatile("buffer_load_dword %0, %1, %2, 0 offen sc1\n\ts_waitcnt vmcnt(0)" : "+v"(fl) : "v"(o), "s"(rws) : "memory")
  {
    issue<S * 2, 0, 0>(rq, vq, q_, d_);
    const int q2 = qa + L * 64 * (S * 2) + kstep(kt, 2) * 128;
    wait_vm<32>();
    BAR();
    if (Q2E == 1) issue<S * 2, 0, 0, 8>(rq, vq, q2, lds + 2 * SLOT + L * 8192);
    wait_vm<Q2E == 1 ? 24 : 16>();
    BAR();
    issue<S * 2, 0, 0>(rq, vq, q_ + 128, d_ + SLOT);
    tag = rfl(tag);
    BAR();
    if (Q2E == 0) { issue<S * 2, 0, 0, 8>(rq, vq, q2, lds + 2 * SLOT + L * 8192); issue<S * 2, 0, 0, 8>(rq, vq, qb + kstep(kt, 2) * 128, lds + 2 * SLOT + UB + L * 8192); }
    wait_vm<Q2E == 0 ? 32 : 16>();
    BAR();
    if (Q2E == 2) issue<S * 2, 0, 0, 8>(rq, vq, q2, lds + 2 * SLOT + L * 8192);
    if (Q2E) issue<S * 2, 0, 0, 8>(rq, vq, qb + kstep(kt, 2) * 128, lds + 2 * SLOT + UB + L * 8192);
  }
  if (L) {
    int k_ = kstep(kt, 3) * 128, ka = qa + k_;
    LAS char* sl = lds;
    wait_vm<Q2E == 1 ? 8 : 16>();
    BAR();
    if (QS) __builtin_amdgcn_s_sleep(QS);
#pragma unroll 1
    for (int n = 4; n < 16; ++n) {
      issue<S * 2, 0, 0>(rq, vq, ka, sl);
      const int kb = qb + k_;
      LAS char* const sb = sl + UB + 8192;
      k_ = kstep(kt, n) * 128; ka = qa + k_;
      sl = sl == lds + 2 * SLOT ? lds : sl + SLOT;
      asm volatile("" : "+s"(k_), "+s"(ka), "+s"(sl));
      issue<S * 2, 0, 0, 8>(rq, vq, kb, sb);
      wait_vm<24>();
      BAR();
    }
    issue<S * 2, 0, 0>(rq, vq, qa + (k_ ^ 2048), lds);
    const int f2o = WS_FLAG2 + (b ^ 4) * 128 + 16 + (lane & 3) * 4;
    wait_vm<16>();
    BAR();
    wait_vm<0>();
    BAR();
    issue<S * 2, 0, 0, 8>(rq, vq, qa + 64 * (S * 2) + k_, lds + 4 * UB + 8192);
    wait_vm<0>();
    BAR();
    BAR();
#pragma unroll 1
    for (int spins = 0; spins < SPIN_CAP; ++spins) {
      POLLW(f2o);
      if (__all(fl == tag)) break;
      __builtin_amdgcn_s_sleep(1);
    }
    STAMP(4);
    issue<0, SC1, 2, 11>(rws, vp, WS_XCH + (b ^ 4) * XB + 32768, lds + 7 * UB);
    issue<0, SC1, 2, 11>(rws, vp, WS_XCH + (b ^ 4) * XB + 32768 + 11264, lds + L_RX);
    wait_vm<0>();
    misc[12] = 1u;
    STAMP(5);
  } else {
#define QHALF(n, sl) issue<S * 2, 0, 0, 8>(rq, vq, qb + kstep(kt, n) * 128, dqb + (sl))
    LAS char* const dqb = lds + UB;
    const rsrc_t rp = mk_rsrc(gp, 128 * S * 2);
    unsigned mask = 0;
    const int fo = flag_off + 32768 + (((lane >> 2) & 7) == (tp1 & 7) ? (kt >> 28) ^ 8 : h * 8 + ((lane >> 2) & 7)) * 128 + (lane & 3) * 4;
#define KM_(k) (15u << 2 * ((k) & 14))
    const int fo1 = flag_off + (kt >> (lane & 4 ? 8 : 4) & 15) * 128 + (lane & 3) * 4;
    unsigned m1 = 0;
    if (PEARLY) {
      POLLW(fo1);
      m1 = (unsigned)__ballot(fl == tag);
      if ((m1 & 15) == 15) issue<0, SC1, 2>(rp, vp, kstep(kt, 2) * UB, lds + 2 * SLOT + 2 * UB);
    } else wait_vm<Q2E == 1 ? 8 : 16>();
    BAR();
    STAMP(2);
    {
      const bool p2 = (m1 & 15) == 15;
      unsigned f1, f2;
      asm volatile("buffer_load_dword %0, %1, %2, 0 offen sc1" : "=&v"(f1) : "v"(fo1), "s"(rws) : "memory");
      QHALF(3, 0);
      asm volatile("buffer_load_dword %0, %2, %3, 0 offen sc1\n\ts_waitcnt vmcnt(9)" : "=&v"(f2), "+v"(f1) : "v"(fo1), "s"(rws) : "memory");
      m1 |= (unsigned)__ballot(f1 == tag);
      if (!p2) {
#pragma unroll 1
        for (int spins = 0; (m1 & 15) != 15 && spins < SPIN_CAP; ++spins) {
          asm volatile("s_sleep 2\n\tbuffer_load_dword %0, %2, %3, 0 offen sc1\n\ts_waitcnt vmcnt(1)" : "+v"(f1), "+v"(f2) : "v"(fo1), "s"(rws) : "memory");
          m1 |= (unsigned)__ballot(f2 == tag);
          if ((m1 & 15) == 15) break;
          asm volatile("s_sleep 2\n\tbuffer_load_dword %0, %2, %3, 0 offen sc1\n\ts_waitcnt vmcnt(1)" : "+v"(f2), "+v"(f1) : "v"(fo1), "s"(rws) : "memory");
          m1 |= (unsigned)__ballot(f1 == tag);
        }
        STAMP(3);
        issue<0, SC1, 2>(rp, vp, kstep(kt, 2) * UB, lds + 2 * SLOT + 2 * UB);
        asm volatile("s_waitcnt vmcnt(16)" : "+v"(f1), "+v"(f2) :: "memory");
      } else asm volatile("s_waitcnt vmcnt(0)" : "+v"(f1), "+v"(f2) :: "memory");
      m1 |= (unsigned)(__ballot(f1 == tag) | __ballot(f2 == tag));
      const bool p3 = (m1 & 0xf0) == 0xf0;
      if (!p3) {
        POLL(fo1);
        wait_vm<1>();
        BAR();
        asm volatile("s_waitcnt vmcnt(0)" : "+v"(fl) :: "memory");
#pragma unroll 1
        for (int spins = 0; ((unsigned)__ballot(fl == tag) & 0xf0) != 0xf0 && spins < SPIN_CAP; ++spins) POLLW(fo1);
      }
      POLL(fo);
      issue<0, SC1, 2>(rp, vp, kstep(kt, 3) * UB, lds + 2 * UB);
      if (p3) { wait_vm<17>(); BAR(); }
      asm volatile("s_waitcnt vmcnt(16)" : "+v"(fl) :: "memory");
    }
#define NEED(need) do { if (__builtin_expect(mask != ~0u, 0)) { asm volatile("" : "+v"(fl) :: "memory"); mask |= (unsigned)__ballot(fl == tag); \
      _Pragma("unroll 1") for (int spins = 0; (mask & (need)) != (need) && spins < SPIN_CAP; ++spins) { POLLW(fo); mask |= (unsigned)__ballot(fl == tag); } \
      if (mask != ~0u) POLL(fo); } } while (0)
    int k_ = kstep(kt, 4), kp = k_ * UB;
    LAS char* sl = lds + SLOT;
#pragma unroll 1
    for (int n = 5; n < 16; ++n) {
      NEED(KM_(k_));
      issue<0, SC1, 2>(rp, vp, kp, sl + 2 * UB);
      const int kb = qb + k_ * 128;
      LAS char* const sb = sl + UB;
      k_ = kstep0(kt, n); kp = k_ * UB;
      sl = sl == lds + 2 * SLOT ? lds : sl + SLOT;
      asm volatile("" : "+s"(k_), "+s"(kp), "+s"(sl));
      issue<S * 2, 0, 0, 8>(rq, vq, kb, sb);
      wait_vm<24>();
      BAR();
    }
    NEED(~0u);
    issue<0, SC1, 2>(rp, vp, (k_ ^ 16) * UB, lds + 2 * UB);
    wait_vm<16>();
    BAR();
    issue<0, SC1, 2>(rp, vp, k_ * UB, lds + UB);
    wait_vm<16>();
    BAR();
    issue<S * 2, 0, 0, 8>(rq, vq, qa + k_ * 128, lds + 4 * UB);
    const int f2o = WS_FLAG2 + (b ^ 2 ^ (lane & 4)) * 128 + (lane & 3) * 4;
    wait_vm<0>();
    BAR();
    BAR();
    unsigned got = 0;
#pragma unroll 1
    for (int spins = 0; spins < SPIN_CAP && got != 3; ++spins) {
      POLLW(f2o);
      const unsigned m_ = (unsigned)__ballot(fl == tag);
      const unsigned g_ = (((m_ & 15u) == 15u) | ((m_ & 0xf0u) == 0xf0u) << 1) & ~got;
#pragma unroll 1
      for (int i = 0; i < 2; ++i)
        if (g_ >> i & 1) issue<0, SC1, 2>(rws, vp, WS_XCH + (b ^ 2 ^ 4 * i) * XB + i * 16384, lds + (1 + 3 * i) * UB);
      got |= g_;
      if (!g_) __builtin_amdgcn_s_sleep(1);
    }
    STAMP(4);
    wait_vm<0>();
    misc[13] = 1u;
    STAMP(5);
  }
  STAMP_OUT();
}

#define LDF(off) (*reinterpret_cast<const bf16x8*>(lds + (off)))
struct Frag { bf16x8 p[4], q[4]; };
#define SGB(m, n) __builtin_amdgcn_sched_group_barrier(m, n, 0)
__device__ __forceinline__ float silu_fast(float z) { return z * __builtin_amdgcn_rcpf(1.0f + __expf(-z)); }
__device__ __forceinline__ u32x4 pack8(const f32x4& a, const f32x4& c) {
  return u32x4{pack2(a[0], a[1]), pack2(a[2], a[3]), pack2(c[0], c[1]), pack2(c[2], c[3])};
}
__device__ __forceinline__ unsigned ln_pair(unsigned x, float nmb, float rden) {
  const unsigned d = pack2(lo2f(x) + nmb, hi2f(x) + nmb);
  return pack2(lo2f(d) * rden, hi2f(d) * rden);
}

template <bool BARRIER, bool LAST, bool ZERO, class X>
__device__ __forceinline__ void big_half(f32x4 (&a0)[4][4], f32x4 (&a1)[4][4], const bf16x8 (&pc)[4], bf16x8 (&pn)[4], bf16x8 (&q)[8],
                                         const char* lds, int qc, int pa, int qn, int qn2, X extra) {
  const f32x4 zero = {0.f, 0.f, 0.f, 0.f};
#pragma unroll
  for (int g = 0; g < 8; ++g) {
    if (g == 0) { q[5] = LDF(qc + UB + 2048); q[6] = LDF(qc + UB + 4096); }
    if (g == 1) q[7] = LDF(qc + UB + 6144);
    if (g == 3) { pn[0] = LDF(pa); pn[1] = LDF(pa + 2048); }
    if (g == 4) { pn[2] = LDF(pa + 4096); pn[3] = LDF(pa + 6144); }
    if (g == 5) { q[0] = LDF(qn); q[1] = LDF(qn + 2048); }
    if (g == 6) { q[2] = LDF(qn + 4096); q[3] = LDF(qn + 6144); }
    if (g == 7 && !LAST) q[4] = LDF(qn2);
#pragma unroll
    for (int i = 0; i < 4; ++i) {
      const int ii = (g & 1) ? 3 - i : i;
      if (g < 4) a0[ii][g] = MFMA(pc[ii], q[g], ZERO ? zero : a0[ii][g]); else a1[ii][g - 4] = MFMA(pc[ii], q[g], ZERO ? zero : a1[ii][g - 4]);
    }
    SGB(0x8, 1); SGB(0x100, 1); SGB(0x8, 1); SGB(0x100, 1); SGB(0x8, 2);
    __builtin_amdgcn_sched_barrier(0);
    if (g == 2 && BARRIER) { WAIT_LGKM0(); BAR(); }
    extra(g);
  }
}

template <bool JOBNEXT>
__device__ __forceinline__ void small_step(f32x4 (&a)[4][4], bf16x8 (&p0)[4], bf16x8 (&p1)[4], bf16x8 (&q)[8], const char* lds,
                                           int ph, int qh, int pn, int qn, int qn2) {
#pragma unroll
  for (int g = 0; g < 8; ++g) {
    if (g == 0) { q[4] = LDF(qh); p1[0] = LDF(ph); }
    if (g == 1) { p1[1] = LDF(ph + 2048); p1[2] = LDF(ph + 4096); }
    if (g == 2) { p1[3] = LDF(ph + 6144); q[5] = LDF(qh + 2048); }
    if (g == 3) { q[6] = LDF(qh + 4096); q[7] = LDF(qh + 6144); }
    if (g == 4) { p0[0] = LDF(pn); q[0] = LDF(qn); }
    if (g == 5) { p0[1] = LDF(pn + 2048); p0[2] = LDF(pn + (JOBNEXT ? 1024 : 4096)); if (JOBNEXT) q[1] = LDF(qn + 2048); }
    if (g == 6) { p0[3] = LDF(pn + (JOBNEXT ? 3072 : 6144)); if (JOBNEXT) { q[2] = LDF(qn + 4096); q[3] = LDF(qn + 6144); } else q[1] = LDF(qn + 2048); }
    if (g == 7) { if (JOBNEXT) { q[4] = LDF(qn2); q[5] = LDF(qn2 + 2048); } else { q[2] = LDF(qn + 4096); q[3] = LDF(qn + 6144); } }
#pragma unroll
    for (int i = 0; i < 4; ++i) {
      const int ii = (g & 1) ? 3 - i : i;
      a[ii][g & 3] = MFMA(g < 4 ? p0[ii] : p1[ii], q[g], a[ii][g & 3]);
    }
    SGB(0x8, 1); SGB(0x100, 1); SGB(0x8, 1); SGB(0x100, 1); SGB(0x8, 1); SGB(0x100, 1); SGB(0x8, 1);
    __builtin_amdgcn_sched_barrier(0);
    if (g == 3) BAR();
  }
}

__device__ __forceinline__ void consumer(int wid, int lane, int b, char* lds, rsrc_t rws, rsrc_t rout,
                                         const char* code, int tp1, int tq1 STAMP_ARG) {
  STAMP_DECL;
  const int z = tq1 >> 3, te = tq1 & 7;
  const int wp = wid >> 1, wq = wid & 1, fr = lane & 15, fq = lane >> 4;
  const int c0 = (fq ^ (fr & 7)) * 16, c1 = ((fq + 4) ^ (fr & 7)) * 16;
  int q0 = (wq * 64 + fr) * 128 + c0, q1 = (wq * 64 + fr) * 128 + c1;
  volatile LAS unsigned* misc = (volatile LAS unsigned*)(lds + L_MISC);
  f32x4 acc[4][4];
#pragma unroll
  for (int i = 0; i < 4; ++i)
#pragma unroll
    for (int j = 0; j < 4; ++j) acc[i][j] = f32x4{0.f, 0.f, 0.f, 0.f};
  unsigned tag;
  asm volatile("" :: "s"(rout));
  u32x4 ob[4]; int bo[2];
  u32x4 xv[4];
  {
    Frag f0, f1;
    const int xrow = wq * 32 + 16 * ((fr >> 2) & 1) + 4 * (fr >> 3) + (fr & 3);
    const int xr = xrow * 128 + (((wp * 4 + fq) ^ (xrow & 7)) * 16);
#define XV(u) do { _Pragma("unroll") for (int k = 0; k < 4; ++k) \
      xv[k] = *reinterpret_cast<const u32x4*>(lds + (u) * UB + xr + (k >> 1) * 8192 + (k & 1) * 1024); } while (0)
    const int p0 = (wp * 64 + fr) * 128 + c0, p1 = (wp * 64 + fr) * 128 + c1;
    unsigned tch = 0;
    if (wid == 3 && lane < (CODE_LINES + 7) / 8) {
      __builtin_amdgcn_s_sleep(8);
      const int co = min(((b >> 3) & 7) * ((CODE_LINES + 7) / 8) + lane, CODE_LINES - 1) * 128;
      asm volatile("global_load_dword %0, %1, off" : "+v"(tch) : "v"(code + co) : "memory");
    }
    const int xt = (2 * te + z - rot1(tp1, tq1)) & 15;
#define G1H(fc, fn, po, qo, EXTRA) do { _Pragma("unroll") for (int g = 0; g < 8; ++g) { \
      EXTRA(g); { if (g == 0) { fn.q[0] = LDF(qo); fn.p[0] = LDF(po); } else if (g == 1) { fn.p[1] = LDF((po) + 2048); fn.p[2] = LDF((po) + 4096); } \
        else if (g == 2) { fn.p[3] = LDF((po) + 6144); fn.q[1] = LDF((qo) + 2048); } else if (g == 3) { fn.q[2] = LDF((qo) + 4096); fn.q[3] = LDF((qo) + 6144); } } \
      _Pragma("unroll") for (int e = 0; e < 2; ++e) { const int i0 = (g & 1) * 2 + e, ii = (g & 2) ? 3 - i0 : i0; \
        acc[ii][g >> 1] = MFMA(fc.p[ii], fc.q[g >> 1], acc[ii][g >> 1]); } \
      SGB(0x8, 1); SGB(0x100, 1); SGB(0x8, 1); SGB(0x100, 2); __builtin_amdgcn_sched_barrier(0); } } while (0)
#define X_NO(g)
#define XI(u) ((u) == 0 ? 0 : (u) == 1 ? 3 : (u) == 2 ? 2 : 7)
#define WI(u) ((u) == 0 ? 1 : (u) == 1 ? 4 : (u) == 2 ? 5 : 8)
#define XVC(t, u) do { if (__builtin_expect((t) == xt, 0)) { XV(XI(u)); PIN4(xv[0], xv[1], xv[2], xv[3]); } __builtin_amdgcn_sched_barrier(0); } while (0)
    int pA[2] = {p0, p1}, pB[2] = {p0 + 7 * UB, p1 + 7 * UB}, qA[2] = {q0 + UB, q1 + UB}, qB[2] = {q0 + 5 * UB, q1 + 5 * UB};
    PIN4(pA[0], pA[1], pB[0], pB[1]); PIN4(qA[0], qA[1], qB[0], qB[1]);
#define XA(u, h) ((u) == 3 ? pB[h] : pA[h] + XI(u) * UB)
#define WA(u, h) ((u) < 2 ? qA[h] + (u) * 3 * UB : qB[h] + ((u) - 2) * 3 * UB)
    int ta = L_MB + (wp * 32 + fq * 8) * 4, tc = L_MB + 1024 + (wq * 32 + 8 * (fr >> 2) + (fr & 3)) * 4;
    const int flip = z ? 0 : 4096;
    int ao[2], ow[2];
#pragma unroll
    for (int h = 0; h < 2; ++h) {
      ao[h] = WS_ABT + tq1 * ABT_ST + tp1 * 2 * UB + wq * 8192 + wp * 1024 + lane * 16 + (h * 4096 ^ flip);
      ow[h] = 2 * UB + wq * 8192 + wp * 1024 + lane * 16 + (h * 4096 ^ flip);
    }
    PIN4(ao[0], ao[1], ow[0], ow[1]);
    asm volatile("" : "+v"(ta), "+v"(tc));
    STAMP(1);
    BAR();
    XVC(0, 0);
    f0.q[0] = LDF(WA(0, 0));
#pragma unroll
    for (int i = 0; i < 4; ++i) f0.p[i] = LDF(XA(0, 0) + i * 2048);
#pragma unroll
    for (int i = 1; i < 4; ++i) f0.q[i] = LDF(WA(0, 0) + i * 2048);
    __builtin_amdgcn_sched_barrier(0);
#pragma unroll 1
    for (int t = 0; t < 12; t += 4) {
#pragma unroll
      for (int u = 0; u < 4; ++u) {
        G1H(f0, f1, XA(u, 1), WA(u, 1), X_NO);
        WAIT_LGKM0();
        BAR();
        XVC(t + u + 1, (u + 1) & 3);
        G1H(f1, f0, XA((u + 1) & 3, 0), WA((u + 1) & 3, 0), X_NO);
      }
    }
    u32x2 rdy;
#define X_RDY(g) if ((g) == 4) rdy = *(const volatile LAS u32x2*)(lds + L_MISC + 16)
    G1H(f0, f1, XA(0, 1), WA(0, 1), X_RDY);
    asm volatile("s_waitcnt vmcnt(0) lgkmcnt(0)" : "+v"(tch) :: "memory");
    BAR();
    XVC(13, 1);
    if (__builtin_expect(!rfl(rdy[0] & rdy[1]), 0)) {
#pragma unroll 1
      for (int spins = 0; spins < SPIN_CAP && !rfl(misc[4] & misc[5]); ++spins) __builtin_amdgcn_s_sleep(1);
    }
    asm volatile("" ::: "memory");
    f32x4 mb[2][2], rd[2][2]; float cs[4];
    unsigned tagv;
#define TB(o) (*reinterpret_cast<const f32x4*>(lds + ta + (o)))
#define X_T1(g) if ((g) >= 4) cs[(g) & 3] = *reinterpret_cast<const float*>(lds + tc + (64 * (((g) & 3) >> 1) + 4 * ((g) & 1)) * 4)
#define X_T2(g) if ((g) == 4) { mb[1][0] = TB(256); mb[1][1] = TB(272); } else if ((g) == 5) tagv = *(const LAS unsigned*)(lds + L_MISC + 8)
    G1H(f1, f0, XA(1, 0), WA(1, 0), X_T1);
    G1H(f0, f1, XA(1, 1), WA(1, 1), X_T2);
    WAIT_LGKM0();
    BAR();
    XVC(14, 2);
    Frag F[4];
#define SB() __builtin_amdgcn_sched_barrier(0)
#define FQ(k, i) F[k].q[i] = LDF(WA(2 + ((k) >> 1), (k) & 1) + (i) * 2048)
#define FP(k, i) F[k].p[i] = LDF(XA(2 + ((k) >> 1), (k) & 1) + (i) * 2048)
#define FA(k, r) do { if ((r) == 0) FQ(k, 0); else if ((r) < 3) FP(k, (r) - 1); else FQ(k, (r) - 2); } while (0)
#pragma unroll
    for (int n = 0; n < 16; ++n) {
      const int j = n >> 2, ii = (j & 1) ? 3 - (n & 3) : n & 3;
      acc[ii][j] = MFMA(f1.p[ii], f1.q[j], acc[ii][j]);
      SB();
      if ((n & 3) != 3) { const int m = n - (n >> 2); FA(m / 6, m % 6); }
      SB();
    }
    BAR();
    XVC(15, 3);
#define VFMA(d, a, c) asm volatile("v_fma_f32 %0, %1, %2, %0" : "+v"(d) : "v"(a), "v"(c))
#define VMUL(d, a) asm volatile("v_mul_f32 %0, %1, %0" : "+v"(d) : "v"(a))
    float cb[2][4][4];
#pragma unroll
    for (int n = 0; n < 32; ++n) {
      const int k = n >> 3, j = (n >> 1) & 3, ii = (n ^ n >> 1) & 1;
      acc[ii][j] = MFMA(F[k].p[ii], F[k].q[j], acc[ii][j]);
      SB();
      if ((n & 3) != 3) { const int m = n - (n >> 2); if (m < 12) FA(2 + m / 6, m % 6); else if (m < 20) FP((m - 12) >> 1, 2 + (m & 1)); }
      if (n == 19) mb[0][0] = TB(0); if (n == 23) mb[0][1] = TB(16); if (n == 26) rd[0][0] = TB(1536); if (n == 27) rd[0][1] = TB(1552);
      {
        const int jj = n >> 3, h = (n >> 2) & 1, r = n & 3;
        cb[h][jj][r] = acc[2 + h][jj][r];
        VFMA(cb[h][jj][r], mb[1][h][r], cs[jj]);
        acc[2 + h][jj][r] = cb[h][jj][r];
      }
      SB();
    }
    WAIT_LGKM0();
    BAR();
    tag = rfl(tagv);
    u32x4 o;
#pragma unroll
    for (int n = 0; n < 32; ++n) {
      const int k = n >> 3, j = (n >> 1) & 3, ii = 2 + ((n ^ n >> 1) & 1);
      acc[ii][j] = MFMA(F[k].p[ii], F[k].q[j], acc[ii][j]);
      SB();
      {
        const int h = (n >> 2) & 1, r = n & 3;
        cb[h][k][r] = acc[h][k][r];
        VFMA(cb[h][k][r], mb[0][h][r], cs[k]);
        if (n) { const int n1 = n - 1; VMUL(cb[(n1 >> 2) & 1][n1 >> 3][n1 & 3], rd[0][(n1 >> 2) & 1][n1 & 3]); }
        if (n > 2 && (n & 1)) { const int n2 = n - 3;
          unsigned o_ = pack2(cb[(n2 >> 2) & 1][n2 >> 3][n2 & 3], cb[(n2 >> 2) & 1][n2 >> 3][(n2 & 3) + 1]); asm volatile("" : "+v"(o_)); o[(n2 >> 1) & 3] = o_; }
        if (n == 20) rd[1][0] = TB(1792); if (n == 22) rd[1][1] = TB(1808);
        if (n > 8 && (n & 7) == 2) {
          __builtin_amdgcn_raw_buffer_store_b128(o, rws, ao[(k - 1) >> 1] + ((k - 1) & 1) * 2048, 0, SC1);
          *reinterpret_cast<u32x4*>(lds + ow[(k - 1) >> 1] + ((k - 1) & 1) * 2048) = o;
        }
      }
      SB();
    }
    STAMP(2);
    VMUL(cb[1][3][3], rd[0][1][3]);
    o[3] = pack2(cb[1][3][2], cb[1][3][3]);
    __builtin_amdgcn_raw_buffer_store_b128(o, rws, ao[1] + 2048, 0, SC1);
    *reinterpret_cast<u32x4*>(lds + ow[1] + 2048) = o;
#pragma unroll
    for (int j = 0; j < 4; ++j) {
      ob[j] = pack8(acc[2][j] * rd[1][0], acc[3][j] * rd[1][1]);
      if (!BDEF) __builtin_amdgcn_raw_buffer_store_b128(ob[j], rws, ao[j >> 1] + UB + (j & 1) * 2048, 0, SC1);
      *reinterpret_cast<u32x4*>(lds + ow[j >> 1] + SLOT + (j & 1) * 2048) = ob[j];
    }
    bo[0] = ao[0] + UB; bo[1] = ao[1] + UB;
    STAMP(3);
  }
  f32x4 acb[4][4];
  bf16x8 pa[4], pb[4], q[8];
  int pp = wp * 8192 + lane * 16;
  bool sent = false;
#define FLAG(off, n) do { if (!sent && vm_now() <= (n)) { sent = true; wait_vm<n>(); \
    if (lane == 0) __builtin_amdgcn_raw_buffer_store_b32(tag, rws, off, 0, SC1); STAMP(FST); } } while (0)
#define FST 4
  const int f1o = WS_FLAG1 + (tq1 * 16 + tp1) * 128 + wid * 4;
#define FLAG1(i, n) do { wait_vm<n>(); __builtin_amdgcn_raw_buffer_store_b32(tag, rws, lane ? -1 : f1o + (i) * 32768, 0, SC1); if (i) STAMP(4); __builtin_amdgcn_sched_barrier(0); } while (0)
  auto fl0 = [&](int g) {
    if (g == FA_) FLAG1(0, BDEF ? 0 : 4);
    if (BDEF && g >= FA_ && g < FA_ + 2) {
      __builtin_amdgcn_raw_buffer_store_b128(ob[2 * (g - FA_) & 3], rws, bo[(g - FA_) & 1], 0, SC1);
      __builtin_amdgcn_raw_buffer_store_b128(ob[2 * (g - FA_) + 1 & 3], rws, bo[(g - FA_) & 1] + 2048, 0, SC1);
      __builtin_amdgcn_sched_barrier(0);
    }
    if (g == FB_) FLAG1(1, BDEF ? 0 : 1); };
  WAIT_LGKM0();
  BAR();
  STAMP(5);
  pa[0] = LDF(2 * UB + pp); q[0] = LDF(q0); pa[1] = LDF(2 * UB + pp + 2048); pa[2] = LDF(2 * UB + pp + 4096); pa[3] = LDF(2 * UB + pp + 6144);
  q[1] = LDF(q0 + 2048); q[2] = LDF(q0 + 4096); q[3] = LDF(q0 + 6144); q[4] = LDF(q0 + UB);
  __builtin_amdgcn_sched_barrier(0);
  auto none = [](int) {};
  big_half<false, false, true>(acc, acb, pa, pb, q, lds, q0, 2 * UB + pp + 1024, q1, q1 + UB, fl0);
  PIN4(q0, q1, pp, lane);
  {
    int sl = 0;
#pragma unroll 1
    for (int n = 0; n < 12; ++n) {
      const int sn = sl == 2 * SLOT ? 0 : sl + SLOT;
      auto fl1 = [&](int g) { if (g + 8 == FB_ && __builtin_expect(n == 0, 0)) FLAG1(1, BDEF ? 0 : 1); };
      auto fl2 = [&](int g) { if (g + 16 == FB_ && __builtin_expect(n == 0, 0)) FLAG1(1, BDEF ? 0 : 1); };
      big_half<true, false, false>(acc, acb, pb, pa, q, lds, sl + q1, sn + 2 * UB + pp, sn + q0, sn + q0 + UB, fl1);
      big_half<false, false, false>(acc, acb, pa, pb, q, lds, sn + q0, sn + 2 * UB + pp + 1024, sn + q1, sn + q1 + UB, fl2);
      sl = sn;
    }
  }
  int q0h = q0 + 4 * UB, q1h = q1 + 4 * UB, pph = pp + 5 * UB;
  asm volatile("" : "+v"(q0h), "+v"(q1h), "+v"(pph));
  big_half<true, true, false>(acc, acb, pb, pa, q, lds, q1, pph, q0h, 0, none);
  small_step<false>(acb, pa, pb, q, lds, pph + 1024, q1h, pph + 3 * UB, q0h + 3 * UB, 0);
  small_step<true>(acb, pa, pb, q, lds, pph + 3 * UB + 1024, q1h + 3 * UB, pph, 3 * UB + q0, 3 * UB + q1);
  int xw = WS_XCH + b * XB + wid * 4096 + lane * 16;
  const int xl = WS_XCH + 32768 + wid * 5632 + lane * 16;
  asm volatile("" : "+v"(xw));
#define SEND_E(j) __builtin_amdgcn_raw_buffer_store_b128(pack8(acb[(j) < 4 ? 2 : 0][(j) & 3], acb[(j) < 4 ? 3 : 1][(j) & 3]), rws, xw + ((j) >> 2) * 16384 + ((j) & 3) * 1024, 0, SC1)
  STAMP(10);
  const int f2o = WS_FLAG2 + b * 128 + wid * 4;
#define PA(k) ((k) == 0 ? pph : (k) == 1 ? pph + 3 * UB : (k) == 2 ? pp + 2 * UB : pp + UB)
#define QA0(k) ((k) == 0 ? q0 + 3 * UB : (k) == 1 ? q0h + 2 * UB : (k) == 2 ? q0 : q0h)
#define QA1(k) ((k) == 0 ? q1 + 3 * UB : (k) == 1 ? q1h + 2 * UB : (k) == 2 ? q1 : q1h)
#define JOB(K, I0, pc, qc, LC, pn, KN, IN, qx, LN, EXTRA) do { _Pragma("unroll") for (int g = 0; g < 8; ++g) { \
    if (g < 2 && (LC)) qc[g + 6] = LDF(QA1(K) + (g + 2) * 2048); \
    if ((KN) >= 0) { \
      if (g >= 2 && (LN)) qx[g - 2] = LDF(g < 6 ? QA0(KN) + (g - 2) * 2048 : QA1(KN) + (g - 6) * 2048); \
      if (g < 4) pn[g] = LDF(PA(KN) + ((IN) + (g & 1)) * 2048 + (g >> 1) * 1024); } \
    acc[I0][g & 3] = MFMA(pc[(g >> 2) * 2], qc[g], acc[I0][g & 3]); \
    acc[(I0) + 1][g & 3] = MFMA(pc[(g >> 2) * 2 + 1], qc[g], acc[(I0) + 1][g & 3]); \
    EXTRA(g); \
    SGB(0x8, 1); SGB(0x100, 1); SGB(0x2, 2); SGB(0x8, 1); SGB(0x100, 1); SGB(0x2, 2); \
    __builtin_amdgcn_sched_barrier(0); } \
    if ((KN) < 0) { PIN4(acc[2][0], acc[2][1], acc[2][2], acc[2][3]); PIN4(acc[3][0], acc[3][1], acc[3][2], acc[3][3]); } } while (0)
#define X_NONE(g)
  u32x4 e1[4], e2[4], ot[5]; u32x2 o5;
#define LT(i) pack2(acc[((i) >> 1) & 1][((i) >> 2) & 3][((i) & 1) * 2], acc[((i) >> 1) & 1][((i) >> 2) & 3][((i) & 1) * 2 + 1])
#define X_LATE(g) if ((g) < 5) __builtin_amdgcn_raw_buffer_store_b128(u32x4{LT(3 * (g)), LT(3 * (g) + 1), LT(3 * (g) + 2), tag}, rws, xl + b * XB + (g) * 1024, 0, SC1); \
  else if ((g) == 5) __builtin_amdgcn_raw_buffer_store_b64(u32x2{LT(15), tag}, rws, xl + b * XB + 5120 - lane * 8, 0, SC1); \
  else if ((g) == 6) __builtin_amdgcn_raw_buffer_store_b32(tag, rws, lane ? -1 : f2o + 16, 0, SC1)
#undef FST
#define FST 9
  sent = false;
#define X_E(n, g) if ((n) * 8 + (g) >= EG0 && ((n) * 8 + (g) - EG0) % EGD == 0 && ((n) * 8 + (g) - EG0) / EGD < 8) SEND_E(((n) * 8 + (g) - EG0) / EGD & 7)
#define X_E0(g) X_E(0, g)
#define X_E1(g) X_E(1, g)
  bf16x8 qb[8], qc_[8];
  JOB(0, 0, pa, q, 1, pb, 1, 0, q, 1, X_E0);
  JOB(1, 0, pb, q, 1, pa, 2, 0, qb, 1, X_E1);
  BAR();
  JOB(2, 0, pa, qb, 1, pb, 3, 0, qc_, 1, X_NONE);
#define FL(i, n) do { if ((i) < FBLK) { if (FCHK >> (i) & 1) FLAG(f2o, n); } else if ((i) == FBLK && !sent) { sent = true; wait_vm<n>(); \
    __builtin_amdgcn_raw_buffer_store_b32(tag, rws, lane ? -1 : f2o, 0, SC1); STAMP(FST); } } while (0)
  FL(0, 0);
  JOB(3, 0, pb, qc_, 1, pa, 3, 2, q, 0, X_NONE);
  FL(1, 0);
  JOB(3, 2, pa, qc_, 0, pb, 2, 2, qb, !KEEP, X_LATE);
  BAR();
  FL(2, 7);
  JOB(2, 2, pb, qb, !KEEP, pa, 1, 2, q, !KEEP, X_NONE);
  FL(3, 7);
  JOB(1, 2, pa, q, !KEEP, pb, 0, 2, qb, 1, X_NONE);
  FL(4, 7);
  float tm[4], tr[4]; unsigned selv;
  const char* tb = lds + L_MB + (wq * 64 + (lane & 15)) * 4;
#define X_TAB(g) if ((g) >= 2 && (g) < 6) { tm[((g) - 2) & 3] = *reinterpret_cast<const float*>(tb + ((g) - 2) * 64); tr[((g) - 2) & 3] = *reinterpret_cast<const float*>(tb + 512 + ((g) - 2) * 64); } \
  else if ((g) == 6) selv = misc[0]
  JOB(0, 2, pb, qb, 1, pa, -1, 0, q, 0, X_TAB);
#define FLAGU() do { if (__builtin_expect(!sent, 0)) FLAG(f2o, 7); } while (0)
  FLAGU();
  STAMP(6);
#define RECV(v, n, off) do { _Pragma("unroll") for (int k_ = 0; k_ < (n); ++k_) \
    v[k_] = __builtin_amdgcn_raw_buffer_load_b128(rws, (off) + (k_ & 3) * 1024, (k_ >> 2) * 4096, SC1); asm volatile("" ::: "memory"); } while (0)
#define RECV_LATE() do { o5 = __builtin_amdgcn_raw_buffer_load_b64(rws, xl + (b ^ 4) * XB + 5120 - lane * 8, 0, SC1); RECV(ot, 5, xl + (b ^ 4) * XB); } while (0)
  PIN4(xv[0], xv[1], xv[2], xv[3]);
  f32x2 gf[4][4];
#pragma unroll
  for (int k = 0; k < 4; ++k) {
    const float tn = tm[k] * tr[k];
#pragma unroll
    for (int c = 0; c < 4; ++c) {
      const f32x2 n = f32x2{lo2f(xv[k][c]), hi2f(xv[k][c])} * tr[k] + tn, a = n * -1.44269504f;
      const f32x2 d = f32x2{__builtin_amdgcn_exp2f(a[0]), __builtin_amdgcn_exp2f(a[1])} + 1.0f;
      gf[k][c] = n * f32x2{__builtin_amdgcn_rcpf(d[0]), __builtin_amdgcn_rcpf(d[1])};
    }
    PIN4(gf[k][0], gf[k][1], gf[k][2], gf[k][3]);
    if (k < 3) FLAGU();
  }
  if (__builtin_expect(!sent, 0)) { wait_vm<7>(); FLAG(f2o, 7); }
#pragma unroll 1
  for (int spins = 0; spins < SPIN_CAP && !rfl(misc[12]); ++spins) ;
  asm volatile("" ::: "memory");
  STAMP(7);
  {
    const char* ll = lds + (wp ? L_RX : 7 * UB) + wq * 5632 + lane * 16;
#pragma unroll
    for (int j = 0; j < 5; ++j) ot[j] = *reinterpret_cast<const u32x4*>(ll + j * 1024);
    o5 = *reinterpret_cast<const u32x2*>(ll + 5120 - lane * 8);
  }
#define LBAD() (!__all(((ot[0][3] ^ tag) | (ot[1][3] ^ tag) | (ot[2][3] ^ tag) | (ot[3][3] ^ tag) | (ot[4][3] ^ tag) | (o5[1] ^ tag)) == 0))
  if (__builtin_expect(LBAD(), 0)) {
#pragma unroll 1
    for (int spins = 0; spins < SPIN_CAP / 16; ++spins) {
      asm volatile("" ::: "memory");
      RECV_LATE();
      if (!LBAD()) break;
    }
    asm volatile("" : "+v"(ot[0]), "+v"(ot[1]), "+v"(ot[2]), "+v"(ot[3]), "+v"(ot[4]), "+v"(o5));
  }
  f32x2 sv[4][4], xf[4][4];
#pragma unroll
  for (int k = 0; k < 4; ++k) {
#pragma unroll
    for (int c = 0; c < 4; ++c) {
      const unsigned a3 = 4 * k + c < 15 ? ot[(4 * k + c) / 3 % 5][(4 * k + c) % 3] : o5[0];
      sv[k][c] = f32x2{acc[2 + (c >> 1)][k][(c & 1) * 2], acc[2 + (c >> 1)][k][(c & 1) * 2 + 1]} + f32x2{lo2f(a3), hi2f(a3)};
      xf[k][c] = f32x2{lo2f(xv[k][c]), hi2f(xv[k][c])};
    }
    PIN4(sv[k][0], sv[k][1], sv[k][2], sv[k][3]);
    PIN4(xf[k][0], xf[k][1], xf[k][2], xf[k][3]);
  }
  {
    unsigned sel = rfl(selv);
    if (__builtin_expect(!sel || (int)(sel - 1 >> 6) == tp1 * 2 + wq, 0)) {
#pragma unroll 1
      for (int spins = 0; spins < SPIN_CAP && !sel; ++spins) { sel = rfl(misc[0]); __builtin_amdgcn_s_sleep(2); }
      const int idx = (int)sel - 1;
      if ((idx >> 6) == tp1 * 2 + wq) {
        const int ks = (idx >> 4) & 3;
        const u32x4 xs = ks == 0 ? xv[0] : ks == 1 ? xv[1] : ks == 2 ? xv[2] : xv[3];
        const float ms = ks == 0 ? tm[0] : ks == 1 ? tm[1] : ks == 2 ? tm[2] : tm[3], rs = ks == 0 ? tr[0] : ks == 1 ? tr[1] : ks == 2 ? tr[2] : tr[3];
        u32x4 n;
#pragma unroll
        for (int c = 0; c < 4; ++c) n[c] = ln_pair(xs[c], ms, rs);
        asm volatile("" : "+v"(n));
#pragma unroll 1
        for (int spins = 0; spins < SPIN_CAP && !rfl(misc[1]); ++spins) __builtin_amdgcn_s_sleep(1);
        asm volatile("" ::: "memory");
        const LAS float* upd = (const LAS float*)(lds + L_UPD) + wp * 32 + (lane >> 4) * 8;
#pragma unroll
        for (int c = 0; c < 4; ++c) {
          const unsigned u = pack2(lo2f(n[c]) + upd[2 * c], hi2f(n[c]) + upd[2 * c + 1]);
          const f32x2 gn = {silu_fast(lo2f(u)), silu_fast(hi2f(u))};
#pragma unroll
          for (int k = 0; k < 4; ++k) if (ks == k && (idx & 15) == (lane & 15)) gf[k][c] = gn;
        }
      }
    }
  }
  int xo = ((tp1 * 128 + wq * 64 + (lane & 15)) * D + te * 128 + z * 64 + wp * 32 + (lane >> 4) * 8) * 2, le = UB + wid * 4096 + lane * 16;
  asm volatile("" : "+v"(xo), "+v"(le));
#pragma unroll 1
  for (int spins = 0; spins < SPIN_CAP && !rfl(misc[13]); ++spins) ;
  asm volatile("" ::: "memory");
  STAMP(11);
#pragma unroll
  for (int k = 0; k < 4; ++k) { e1[k] = *reinterpret_cast<const u32x4*>(lds + le + k * 1024); e2[k] = *reinterpret_cast<const u32x4*>(lds + le + 3 * UB + k * 1024); }
#pragma unroll
  for (int k = 0; k < 4; ++k) {
    u32x4 ov;
#pragma unroll
    for (int c = 0; c < 4; ++c) {
      const f32x2 r = ((sv[k][c] + f32x2{lo2f(e1[k][c]), hi2f(e1[k][c])}) + f32x2{lo2f(e2[k][c]), hi2f(e2[k][c])}) * gf[k][c] + xf[k][c];
      ov[c] = pack2(r[0], r[1]);
    }
    __builtin_amdgcn_raw_buffer_store_b128(ov, rout, xo + k * 16 * D * 2, 0, OPOL);
  }
  STAMP(8);
  STAMP_OUT();
}

#define BCAST(v, k) ((unsigned)__builtin_amdgcn_mov_dpp((int)(v), 0x150 + (k), 0xf, 0xf, true))
template <int K0, int K1>
__device__ __forceinline__ void expand(u32x4 (&dg)[16], unsigned xs, const unsigned (&sel)[4]) {
  if constexpr (K0 < K1) {
    const unsigned v = BCAST(xs, K0);
    dg[K0] = u32x4{__builtin_amdgcn_perm(0u, v, sel[0]), __builtin_amdgcn_perm(0u, v, sel[1]), __builtin_amdgcn_perm(0u, v, sel[2]), __builtin_amdgcn_perm(0u, v, sel[3])};
    asm volatile("" : "+v"(dg[K0]));
    expand<K0 + 1, K1>(dg, xs, sel);
  }
}
template <int K0, int K1, int N>
__device__ __forceinline__ void gemv(f32x4& ae, f32x4& ao, unsigned xs, const unsigned (&sel)[4], const u32x4 (&w)[N]) {
  if constexpr (K0 < K1) {
    const unsigned v = BCAST(xs, K0);
    const bf16x8 dg = BF8((u32x4{__builtin_amdgcn_perm(0u, v, sel[0]), __builtin_amdgcn_perm(0u, v, sel[1]),
                                 __builtin_amdgcn_perm(0u, v, sel[2]), __builtin_amdgcn_perm(0u, v, sel[3])}));
    ae = MFMA(dg, BF8(w[2 * K0]), ae); ao = MFMA(dg, BF8(w[2 * K0 + 1]), ao);
    gemv<K0 + 1, K1, N>(ae, ao, xs, sel, w);
  }
}

__device__ __forceinline__ void helper(int hw, int lane, int b, char* lds, rsrc_t rws, rsrc_t rx, const u16* wr,
                                       const u16* wgu, const u16* wdn, const u16* gq, const char* code, int tp1, int tq1 STAMP_ARG) {
  STAMP_DECL;
  int nb = 0, lim = 14;
  unsigned tch = 0;
  volatile LAS unsigned* misc = (volatile LAS unsigned*)(lds + L_MISC);
  __builtin_amdgcn_s_setprio(3);
  const int z = tq1 >> 3, te = tq1 & 7;
  const int lc = lane & 15, lq = lane >> 4;
  const int r1 = rot1(tp1, tq1), kb = r1 >> 2, amo = WS_AMAX + (tq1 * 16 + tp1) * 128;
#define JOIN() do { HPRE(); BAR(); HSTAMP(); ++nb; } while (0)
#define NEXT() do { if (nb < lim) JOIN(); } while (0)
#define UNTIL(m) do { _Pragma("unroll 1") while (nb < (m)) JOIN(); } while (0)
#define POLLVM(m) do { _Pragma("unroll 1") while (vm_now() > (m) && nb < lim) JOIN(); wait_vm<m>(); } while (0)
  unsigned tag, key, sg; int idx;
  float sw;
  const int rg = b / 28, cg = b - rg * 28;
  const int fg = b >> 3, eg = b & 7;
  u32x4 w[32], wd[14];
  const rsrc_t rgu = mk_rsrc(wgu + (size_t)(rg & 7) * 128 * F + cg * 128, b < 224 ? 128 * F * 2 : 0);
  const rsrc_t rd = mk_rsrc(wdn + (size_t)(fg * 112 + hw * 56) * D + eg * 128, 56 * D * 2);
  const int wvo = lq * 32 * F * 2 + lc * 16, dvo = lq * 14 * D * 2 + lc * 16;
#define LOADW(k0, k1) do { _Pragma("unroll") for (int k = k0; k < (k1); ++k) \
    w[k] = __builtin_amdgcn_raw_buffer_load_b128(rgu, wvo, k * F * 2, WPOL); asm volatile("" ::: "memory"); } while (0)
#define LOADD(k0, k1) do { _Pragma("unroll") for (int k = k0; k < (k1); ++k) \
    wd[k] = __builtin_amdgcn_raw_buffer_load_b128(rd, dvo, k * D * 2, WPOL); asm volatile("" ::: "memory"); } while (0)
  {
    const rsrc_t rr = mk_rsrc(wr, D * 2);
    u32x4 wf[8];
#pragma unroll
    for (int i = 0; i < 8; ++i) wf[i] = __builtin_amdgcn_raw_buffer_load_b128(rr, lq * 16 + (i & 3) * 64, ((r1 + (i >> 2) * 2) & 15) * 128, 0);
    const unsigned epoch = __builtin_amdgcn_raw_buffer_load_b32(rws, lane == 1 ? (hw ? amo : WS_FLAG2 + b * 128) : lane == 2 ? amo + (WS_FLAG1 - WS_AMAX) + hw * 32768 : WS_EPOCH, 0, SC1);
    const u32x4 ra = __builtin_amdgcn_raw_buffer_load_b128(rr, lane * 16, 0, 0), rb2 = __builtin_amdgcn_raw_buffer_load_b128(rr, lane * 16 + 1024, 0, 0);
    misc[lc] = 0u;
    u32x4 ones = {0x3f803f80u, 0x3f803f80u, 0x3f803f80u, 0x3f803f80u};
    const int sx = (KM == 4 ? (te >> 2) + 2 * z : te >> 1) * 32 + hw * 16, sv = (KM == 4 ? tp1 >> 2 : tp1 >> 1 & 3) * 32 + hw * 16;
    int xa[2], wa[2];
    xa[0] = (sx + lc) * 128 + (lq ^ (lc & 7)) * 16; wa[0] = xa[0] + (sv - sx) * 128 + UB;
    xa[1] = xa[0] ^ 64; wa[1] = wa[0] ^ 64;
    asm volatile("" : "+v"(ones));
    f32x4 d1 = {0.f, 0.f, 0.f, 0.f}, d2 = d1, d3 = d1, d4 = d1;
    HST(9);
#pragma unroll
    for (int u = 0; u < 4; ++u) {
      if (u == 2) { wa[0] += 4 * UB; wa[1] += 4 * UB; }
      if (u == 3) { xa[0] += 7 * UB; xa[1] += 7 * UB; }
      PIN4(xa[0], xa[1], wa[0], wa[1]);
      WAIT_LGKM0();
      JOIN();
      const unsigned tj = (unsigned)__builtin_amdgcn_s_memrealtime();
      bf16x8 xf[2], vf[2];
#pragma unroll
      for (int h = 0; h < 2; ++h) { xf[h] = LDF(xa[h] + (u == 3 ? 0 : XI(u) * UB)); vf[h] = LDF(wa[h] + (u & 1) * 3 * UB); }
      __builtin_amdgcn_sched_barrier(0);
      if (u) LOADW((int)(SWT >> 8 * u & 63), (int)(SWT >> 8 * u + 8 & 63));
#pragma unroll
      for (int h = 0; h < 2; ++h) {
        d2 = MFMA(xf[h], xf[h], d2); d4 = MFMA(BF8(ones), xf[h], d4); d3 = MFMA(BF8(ones), vf[h], d3); d1 = MFMA(BF8(wf[2 * u + h]), xf[h], d1);
      }
      PIN4(d1, d2, d3, d4);
      if (u == 0) {
        tag = mk_tag(epoch);
        asm volatile("" : "+s"(tag));
        misc[2] = tag;
        const f32x4 d5 = MFMA(BF8(rb2), BF8(ones), MFMA(BF8(ra), BF8(ones), (f32x4{0.f, 0.f, 0.f, 0.f})));
        const int s4 = __builtin_bit_cast(int, (d5[0] + d5[1]) + (d5[2] + d5[3]));
#define RL(l) __builtin_bit_cast(float, __builtin_amdgcn_readlane(s4, l))
        sw = (RL(0) + RL(16)) + (RL(32) + RL(48));
        asm volatile("" : "+v"(sw));
        LOADW(0, (int)(SWT >> 8 & 63));
      }
      if (PI >> 8 * u & 255) {
#pragma unroll 1
        while ((unsigned)__builtin_amdgcn_s_memrealtime() - tj < (PI >> 8 * u & 255)) ;
      }
      __builtin_amdgcn_sched_barrier(0);
    }
    JOIN();
    const float sq = (lc & 2) ? ((lc & 1) ? d2[3] : d2[2]) : ((lc & 1) ? d2[1] : d2[0]);
    __builtin_amdgcn_raw_buffer_store_b128(u32x4{__float_as_uint(d4[0]), __float_as_uint(sq), __float_as_uint(d1[0]), tag}, rws,
                                           lq == lc >> 2 ? WS_XP + (tp1 * 4 + kb) * 2048 + (sx + lc) * 16 : -1, 0, SC1);
    __builtin_amdgcn_raw_buffer_store_b64(u32x2{__float_as_uint(d3[0]), tag}, rws, lane < 16 ? WS_WP + (tq1 * 4 + kb) * 1024 + (sv + lc) * 8 : -1, 0, SC1);
    __builtin_amdgcn_sched_barrier(0);
    LOADW((int)(SWT >> 32 & 63), (int)(SWT >> 40 & 63));
    if (SWT >> 48) { JOIN(); LOADW((int)(SWT >> 40 & 63), (int)(SWT >> 48 & 63)); }
    if (SWT >> 56) { JOIN(); LOADW((int)(SWT >> 48 & 63), (int)(SWT >> 56 & 63)); }
  }
  const rsrc_t rq = mk_rsrc(gq, QROWS * S * 2);
  const unsigned kt = ktab(tp1, te);
  const int trow = (te >> 2) * 1024 + (te & 3) * 32 + hw * 16;
  const int t1o = (trow + ((lane >> 1) & 15)) * (S * 2) + (lane & 1) * 128;
#define QTOUCH0(o, so) asm volatile("buffer_load_dword %0, %1, %2, %3 offen" : "+v"(tch) : "v"(o), "s"(rq), "s"(so) : "memory")
#define QTOUCH1(o, so) asm volatile("buffer_load_dword %0, %1, %2, %3 offen nt\n\tbuffer_load_dword %0, %1, %2, %3 offen" : "+v"(tch) : "v"(o), "s"(rq), "s"(so) : "memory")
#define QTOUCH(o, so) do { if (DTM >> i_ & 1) QTOUCH1(o, so); else QTOUCH0(o, so); } while (0)
#define TCH(m) do { _Pragma("unroll") for (int i_ = 0; i_ < 8; ++i_) if ((m) >> i_ & 1) { \
      if (i_ < 7) QTOUCH(t1o, kstep0(kt, 2 * i_) * 128); \
      else QTOUCH(lane & 16 ? ((tp1 >> 3) * 1024 + te * 16 + lc) * (S * 2) + (kstep(kt, 15) ^ hw * 16) * 128 : (trow + lc) * (S * 2) + kstep(kt, 14) * 128, 0); } } while (0)
  const int srow = hw * 32 + (lane & 3) + 2 * (lane & 44) + ((lane >> 2) & 4);
  {
    const int gxo = WS_XP + tp1 * 8192 + (hw * 64 + lane) * 16, gwo = WS_WP + tq1 * 4096 + (hw * 64 + lane) * 8;
    u32x4 gx[4]; u32x2 gw[4];
#define GATHER() asm volatile("buffer_load_dwordx4 %0, %8, %10, 0 offen sc1\n\tbuffer_load_dwordx4 %1, %8, %10, 0 offen offset:2048 sc1\n\t" \
                 "buffer_load_dwordx4 %2, %8, %10, %11 offen sc1\n\tbuffer_load_dwordx4 %3, %8, %10, %11 offen offset:2048 sc1\n\t" \
                 "buffer_load_dwordx2 %4, %9, %10, 0 offen sc1\n\tbuffer_load_dwordx2 %5, %9, %10, 0 offen offset:1024 sc1\n\t" \
                 "buffer_load_dwordx2 %6, %9, %10, 0 offen offset:2048 sc1\n\tbuffer_load_dwordx2 %7, %9, %10, 0 offen offset:3072 sc1" \
                 : "=&v"(gx[0]), "=&v"(gx[1]), "=&v"(gx[2]), "=&v"(gx[3]), "=&v"(gw[0]), "=&v"(gw[1]), "=&v"(gw[2]), "=&v"(gw[3]) \
                 : "v"(gxo), "v"(gwo), "s"(rws), "s"(4096) : "memory")
    UNTIL(sched(UP_T, te));
#define TCH7(i) do { if (T7 >= 0 && te == 7) { const int i_ = T7 & 7, o_ = (lane >> 1) * (S * 2) + (lane & 1) * 128, so_ = ((i) * 1024 + hw * 64) * (S * 2) + kstep0(kt, 2 * T7 & 15) * 128; \
      QTOUCH(o_, so_); QTOUCH(o_, so_ + 32 * S * 2); } } while (0)
    TCH(TM0);
    TCH7(0);
    LOADW(SWE, NWA1);
    if (NWA > NWA1) { JOIN(); LOADW(NWA1, NWA); }
    UNTIL(sched(T1_T, te) - GAD);
    GATHER();
    UNTIL(sched(T1_T, te));
    TCH(TM1);
    LOADW(NWA, NWC);
    wait_vm<NWC - NWA + __builtin_popcount(TM1) + __builtin_popcount(TM1 & DTM)>();
    TCH7(1);
#pragma unroll 1
    for (int spins = 0;; ++spins) {
      asm volatile("" : "+v"(gx[0]), "+v"(gx[1]), "+v"(gx[2]), "+v"(gx[3]), "+v"(gw[0]), "+v"(gw[1]), "+v"(gw[2]), "+v"(gw[3]) :: "memory");
      unsigned bad = 0;
#pragma unroll
      for (int q = 0; q < 4; ++q) bad |= (gx[q][3] ^ tag) | (gw[q][1] ^ tag);
      if (__builtin_expect(__all(bad == 0) || spins > SPIN_CAP / 64, 1)) break;
      GATHER();
      NEXT();
      POLLVM(0);
    }
    HST(3);
#define SUM4(v, c) ((__uint_as_float(v[0][c]) + __uint_as_float(v[1][c])) + (__uint_as_float(v[2][c]) + __uint_as_float(v[3][c])))
    float sum = SUM4(gx, 0), sq = SUM4(gx, 1), dot = SUM4(gx, 2);
    *(LAS float*)(lds + L_MB + 1024 + srow * 4) = SUM4(gw, 0);
    asm volatile("" : "+v"(sum), "+v"(sq), "+v"(dot));
    const float mn = sum * (1.0f / D);
    const float var = fmaxf(fmaf(-mn, mn, sq * (1.0f / D)), 0.f);
    const float mbv = rbf(mn), den = rbf(__builtin_amdgcn_sqrtf(rbf(rbf(var) + 1e-5f))), rden = __builtin_amdgcn_rcpf(den);
    *(LAS float*)(lds + L_MB + srow * 4) = -mbv;
    *(LAS float*)(lds + L_MB + 512 + srow * 4) = rden;
    *(LAS float*)(lds + L_MB + 1536 + srow * 4) = z ? -rden : rden;
    asm volatile("" ::: "memory");
    misc[4 + hw] = 1u;
    float rs = (dot - mbv * sw) * rden;
    sg = pack2(mbv, den);
    asm volatile("" : "+v"(rs), "+v"(sg));
    lim = NBAR;
    const unsigned rb = pack2(rs, rs) & 0xffffu;
    unsigned srt = (rb & 0x8000u) ? (~rb & 0xffffu) : (rb | 0x8000u);
    if (rs != rs) srt = 0u;
    key = (srt << 16) | (unsigned)(0xffff - tp1 * 128 - srow);
    const unsigned mk = wave_maxu(key);
    if (key == mk) __builtin_amdgcn_raw_buffer_store_b128(u32x4{key, tag, sg, tag}, rws, amo + hw * 16, 0, SC1);
    HST(2);
    UNTIL(WB_AT + (WBS >> 4 * te & 3));
    TCH(TMB);
    LOADW(NWC, 32);
  }
  unsigned sel[4];
#pragma unroll
  for (int d = 0; d < 4; ++d) {
    const unsigned src = (lc & 2) ? 0x0302u : 0x0100u;
    sel[d] = (lc >> 2) == d ? ((lc & 1) ? (src << 16 | 0x0c0cu) : (0x0c0c0000u | src)) : 0x0c0c0c0cu;
  }
  PIN4(sel[0], sel[1], sel[2], sel[3]);
  JOIN();
  const int hc = fg * 112 + hw * 56 + (lq * 7 + min(lc & 7, 6)) * 2;
  int guo = WS_GU + ((lc >> 3) * F + (hc & ~127)) * 8 + ((hc & 6) * 8 + ((hc >> 3) & 15)) * 16;
  int gao = WS_AMAX + tq1 * 2048 + ((lane >> 1) & 15) * 128 + (lane & 1) * 16, gso = b < 224 ? WS_GU + ((rg * 2 + hw) * F + cg * 128) * 8 + lane * 16 : -1;
  asm volatile("" : "+v"(guo), "+v"(gao), "+v"(gso));
  UNTIL(AM_AT);
#pragma unroll 1
  for (int spins = 0;; ++spins) {
    const u32x4 am = __builtin_amdgcn_raw_buffer_load_b128(rws, gao, 0, SC1);
    NEXT();
    UNTIL(AM_P);
#pragma unroll 1
    while (vm_now() > 0 && nb < lim && nb != 19 && nb != 17) JOIN();
    key = am[0]; sg = am[2];
    if (__builtin_expect(__all(((am[1] ^ tag) | (am[3] ^ tag)) == 0) || spins > SPIN_CAP / 64, 1)) break;
  }
  {
    const unsigned mk = wave_maxu(key);
    sg = __builtin_amdgcn_readlane(sg, __builtin_ctzll(__ballot(key == mk)));
    key = mk;
  }
  idx = min(0xffff - (int)(key & 0xffffu), S - 1);
  const unsigned xe = __builtin_amdgcn_raw_buffer_load_b32(rx, (idx * D + (rg & 7) * 128) * 2 + lane * 4, 0, 0);
  misc[0] = idx + 1;
  HST(7);
  if (nb != 19) NEXT();
#pragma unroll 1
  while (vm_now() > 0 && nb < lim && nb != 19 && nb != 18 + XEJ) JOIN();
  {
    const unsigned xs = ln_pair(xe, -lo2f(sg), __builtin_amdgcn_rcpf(hi2f(sg)));
    if (b == 0 && lane == 0 && hw == 1) __builtin_amdgcn_raw_buffer_store_b32(tag, rws, WS_EPOCH, 0, SC1);
    u32x4 dg[16];
    expand<0, GX1>(dg, xs, sel);
    if (nb != 19) NEXT();
    expand<GX1, 16>(dg, xs, sel);
    UNTIL(19);
    const bool idle = nb == 19;
    if (idle) { if (GV_SL) __builtin_amdgcn_s_sleep(GV_SL); } else NEXT();
    f32x4 ae = {0.f, 0.f, 0.f, 0.f}, ao = ae;
#pragma unroll
    for (int k = 0; k < 16; ++k) {
      ae = MFMA(BF8(dg[k]), BF8(w[2 * k]), ae); ao = MFMA(BF8(dg[k]), BF8(w[2 * k + 1]), ao);
      if (k == 7) { asm volatile("" : "+v"(ae), "+v"(ao)); if (__builtin_expect(!idle, 0)) NEXT(); }
    }
    __builtin_amdgcn_raw_buffer_store_b128(u32x4{__float_as_uint(ae[0] + ao[2]), tag, __float_as_uint(ae[1] + ao[3]), tag}, rws, gso, 0, SC1);
  }
  HST(4);
  if (nb == 19) JOIN();
  {
    const int m0_ = 18 + MB0 + (te & MBS);
#define MB(i) do { if ((MBT >> 8 * (i) & 255) || (MBW >> 4 * (i) & 15) != (MBW >> 4 * (i) + 4 & 15)) { UNTIL(m0_ + MBD * (i)); if (BSL2) __builtin_amdgcn_s_sleep(BSL2); \
      TCH(MBT >> 8 * (i) & 255); LOADD((int)(MBW >> 4 * (i) & 15), (int)(MBW >> 4 * (i) + 4 & 15)); } } while (0)
    MB(0); MB(1); MB(2); MB(3); MB(4); MB(5); MB(6); MB(7);
    if (!hw && lane < (CODE_LINES + 7) / 8) {
      const int co = min(((b >> 3) & 7) * ((CODE_LINES + 7) / 8) + lane, CODE_LINES - 1) * 128;
      asm volatile("global_load_dword %0, %1, off" : "+v"(tch) : "v"(code + co) : "memory");
    }
    UNTIL(m0_ + GGD);
  }
  {
    float s0, s1;
#pragma unroll 1
    for (int spins = 0;; ++spins) {
#pragma unroll
      for (int k = 0; k < 8; ++k) w[k] = __builtin_amdgcn_raw_buffer_load_b128(rws, guo, k * 2 * F * 8, SC1);
      NEXT();
      POLLVM(0);
      unsigned bad = 0; s0 = 0.f; s1 = 0.f;
#pragma unroll
      for (int k = 0; k < 8; ++k) { bad |= (w[k][1] ^ tag) | (w[k][3] ^ tag); s0 += __uint_as_float(w[k][0]); s1 += __uint_as_float(w[k][2]); }
      if (__builtin_expect(__all(bad == 0) || spins > SPIN_CAP / 64, 1)) break;
    }
    asm volatile("" : "+v"(s0), "+v"(s1));
    HST(8);
    NEXT();
    const unsigned gp = pack2(s0, s1);
    const unsigned up = (unsigned)__builtin_amdgcn_mov_dpp((int)gp, 0x128, 0xf, 0xf, true);
    const unsigned sl2 = pack2(silu_fast(lo2f(gp)), silu_fast(hi2f(gp)));
    const unsigned hp = pack2(lo2f(sl2) * lo2f(up), hi2f(sl2) * hi2f(up));
    f32x4 ae = {0.f, 0.f, 0.f, 0.f}, ao = ae;
    gemv<0, 2>(ae, ao, hp, sel, wd);
    asm volatile("" : "+v"(ae), "+v"(ao));
    NEXT();
    gemv<2, 7>(ae, ao, hp, sel, wd);
    f32x2 a = {ae[0] + ao[2], ae[1] + ao[3]};
    HST(5);
    if (hw == 1) {
      *(LAS f32x2*)(lds + L_CMB + lane * 8) = a;
      asm volatile("" ::: "memory");
      misc[10] = 1u;
    } else {
      f32x2 o;
#pragma unroll 1
      for (int spins = 0; spins < SPIN_CAP; ++spins) {
        NEXT();
        const unsigned f = misc[10];
        o = *(volatile LAS f32x2*)(lds + L_CMB + lane * 8);
        if (rfl(f)) break;
      }
      __builtin_amdgcn_raw_buffer_store_b128(u32x4{__float_as_uint(a[0] + o[0]), tag, __float_as_uint(a[1] + o[1]), tag}, rws,
                                             WS_PART + (fg * D + eg * 128) * 8 + lane * 16, 0, SC1);
      HST(6);
    }
  }
  if (hw == 1 && (idx >> 7) == tp1) {
    const unsigned srt = key >> 16;
    const float r = lo2f((srt & 0x8000u) ? (srt & 0x7fffu) : (~srt & 0xffffu));
    const float rsel = rbf(1.0f / (1.0f + __expf(-r)));
    const int cp = lane & 31;
    const int e = z * 64 + cp * 2;
    const int po = WS_PART + ((lane >> 5) * D + te * 128) * 8 + ((e & 6) * 8 + (e >> 3)) * 16;
    float y0, y1;
    UNTIL(FIN_AT);
#pragma unroll 1
    for (int spins = 0;; ++spins) {
#pragma unroll
      for (int k = 0; k < 16; ++k) w[k] = __builtin_amdgcn_raw_buffer_load_b128(rws, po, k * 2 * D * 8, SC1);
      NEXT();
      POLLVM(0);
      unsigned bad = 0;
#pragma unroll
      for (int k = 0; k < 16; ++k) bad |= (w[k][1] ^ tag) | (w[k][3] ^ tag);
      if (__all(bad == 0) || spins > SPIN_CAP / 64) break;
    }
    NEXT();
    y0 = 0.f; y1 = 0.f;
#pragma unroll
    for (int k = 0; k < 16; ++k) { y0 += __uint_as_float(w[k][0]); y1 += __uint_as_float(w[k][2]); }
    asm volatile("" : "+v"(y0), "+v"(y1));
    NEXT();
    { const auto p0 = __builtin_amdgcn_permlane32_swap(__float_as_uint(y0), __float_as_uint(y0), false, false);
      const auto p1 = __builtin_amdgcn_permlane32_swap(__float_as_uint(y1), __float_as_uint(y1), false, false);
      y0 = __uint_as_float(p0[0]) + __uint_as_float(p0[1]); y1 = __uint_as_float(p1[0]) + __uint_as_float(p1[1]); }
    *(LAS f32x2*)(lds + L_UPD + cp * 8) = f32x2{rbf(rbf(y0) * rsel), rbf(rbf(y1) * rsel)};
    asm volatile("" ::: "memory");
    misc[1] = 1u;
    HST(6);
  }
  HST(10);
  POLLVM(0);
  asm volatile("" : "+v"(tch));
  STAMP_OUT();
}

#define KA
__global__ __launch_bounds__(512, 2) void fused(KA const u16* __restrict__ x, KA const u16* __restrict__ hre, KA const u16* __restrict__ him,
                                                KA const u16* __restrict__ wr, KA char* ws, KA const u16* __restrict__ sre, KA const u16* __restrict__ sim,
                                                const u16* __restrict__ wg, const u16* __restrict__ wu, const u16* __restrict__ wdn, u16* out) {
  extern __shared__ __attribute__((aligned(16))) char lds[];
  u64 pc;
  asm volatile("s_getpc_b64 %0" : "=s"(pc));
  const char* code = (const char*)(pc & ~255ull);
  const int wid = __builtin_amdgcn_readfirstlane(threadIdx.x >> 6), lane = threadIdx.x & 63;
  const int b = blockIdx.x, g = b & 7, i = b >> 3;
  const int tp1 = (g & 3) * 4 + (i & 3), tq1 = (g >> 2) * 8 + (i >> 2);
  const int z = tq1 >> 3, te = tq1 & 7;
  asm volatile("" :: "s"(x), "s"(sre), "s"(sim), "s"(hre), "s"(him), "s"(wr), "s"(wg), "s"(wu), "s"(wdn), "s"(ws), "s"(out));
  const rsrc_t rws = mk_rsrc(ws, WS_END);
  const u16* wtile = (z ? him : hre) + (size_t)te * 128 * D;
  if (__builtin_expect(wid == 4 || wid == 5, 1)) {
    const int L = wid - 4;
    loader(L, lane, (LAS char*)lds, L ? wtile : x + (size_t)tp1 * 128 * D, (z ? sim : sre) + (size_t)(tp1 & 7) * 128 * S,
           ws + WS_ABT + (size_t)tq1 * ABT_ST, rws, WS_FLAG1 + tq1 * 2048, b, tp1, tq1 STAMP_PASS);
  } else if (wid < 4) {
    consumer(wid, lane, b, lds, rws, mk_rsrc(out, S * D * 2), code, tp1, tq1 STAMP_PASS);
  } else {
    const int hw = wid - 6;
    helper(hw, lane, b, lds, rws, mk_rsrc(x, S * D * 2), wr, hw ? wu : wg, wdn,
           (z ? sim : sre) + (size_t)(tp1 & 7) * 128 * S, code, tp1, tq1 STAMP_PASS);
  }
}

extern "C" void kernel_launch(void* const* d_in, const int* in_sizes, int n_in, void* d_out, int out_size,
                              void* d_ws, size_t ws_size, hipStream_t stream) {
  (void)hipFuncSetAttribute((const void*)fused, hipFuncAttributeMaxDynamicSharedMemorySize, LDS_BYTES);
  fused<<<256, 512, LDS_BYTES, stream>>>((const u16*)d_in[0], (const u16*)d_in[3], (const u16*)d_in[4], (const u16*)d_in[6], (char*)d_ws,
                                         (const u16*)d_in[1], (const u16*)d_in[2], (const u16*)d_in[7], (const u16*)d_in[8],
                                         (const u16*)d_in[9], (u16*)d_out);
}
```

```cpp
#define CODE_LINES 275
#include <hip/hip_runtime.h>
#include <hip/hip_bf16.h>
#include <stdint.h>

typedef __attribute__((ext_vector_type(8))) short bf16x8;
typedef __attribute__((ext_vector_type(4))) float f32x4;
typedef __attribute__((ext_vector_type(2))) float f32x2;
typedef __attribute__((ext_vector_type(4))) unsigned u32x4;
typedef __attribute__((ext_vector_type(2))) unsigned u32x2;
typedef __attribute__((ext_vector_type(2))) __bf16 bf16x2;
typedef unsigned short u16;
typedef unsigned long long u64;
typedef __amdgpu_buffer_rsrc_t rsrc_t;
#define LAS __attribute__((address_space(3)))

constexpr int S = 2048, D = 1024, F = 3584;
constexpr int UB = 16384;
constexpr int SLOT = 3 * UB;
constexpr int L_MISC = 9 * UB;
constexpr int L_UPD = L_MISC + 64;
constexpr int L_MB = L_MISC + 512;
constexpr int L_CMB = L_MB + 2048;
constexpr int LDS_BYTES = L_CMB + 512;
constexpr int NBAR = 17 + 14 + 3;
constexpr int SC1 = 16, NT = 2;

constexpr unsigned WS_EPOCH = 0;
constexpr unsigned WS_XP = 4096;
constexpr unsigned WS_WP = WS_XP + 16 * 8192;
constexpr unsigned WS_AMAX = WS_WP + 16 * 4096;
constexpr unsigned WS_FLAG1 = WS_AMAX + 32768;
constexpr unsigned WS_FLAG2 = WS_FLAG1 + 65536;
constexpr unsigned WS_GU = WS_FLAG2 + 32768;
constexpr unsigned WS_PART = WS_GU + 8 * 2 * F * 8;
constexpr unsigned WS_XCH = WS_PART + 32 * D * 8;
constexpr int XB = 49152;
constexpr unsigned ABT_ST = 128 * S * 2;
constexpr unsigned WS_ABT = WS_XCH + 256 * XB;
constexpr unsigned WS_END = WS_ABT + 16 * ABT_ST;

#define STAMP(i)
#define HSTAMP()
#define HPRE()
#define HST(i)
#define STAMP_DECL
#define STAMP_ARG
#define STAMP_PASS
#define STAMP_OUT()

__device__ __forceinline__ float lo2f(unsigned v) { return __uint_as_float(v << 16); }
__device__ __forceinline__ float hi2f(unsigned v) { return __uint_as_float(v & 0xffff0000u); }
__device__ __forceinline__ unsigned pack2(float a, float b) {
  return __builtin_bit_cast(unsigned, __builtin_convertvector(f32x2{a, b}, bf16x2));
}
__device__ __forceinline__ float rbf(float f) { return lo2f(pack2(f, f)); }
__device__ __forceinline__ unsigned rfl(unsigned v) { return __builtin_amdgcn_readfirstlane(v); }
__device__ __forceinline__ rsrc_t mk_rsrc(const void* p, unsigned bytes) {
  return __builtin_amdgcn_make_buffer_rsrc((void*)p, 0, bytes, 0x00020000);
}
#define DPP_ADD(v, ctrl, rm, bc) v += __builtin_bit_cast(float, __builtin_amdgcn_update_dpp(0, __builtin_bit_cast(int, v), ctrl, rm, 0xf, bc))
#define DPP_MAXU(v, ctrl, rm, bc) v = max(v, (unsigned)__builtin_amdgcn_update_dpp(0, (int)v, ctrl, rm, 0xf, bc))
__device__ __forceinline__ unsigned wave_maxu(unsigned v) {
  DPP_MAXU(v, 0x111, 0xf, true); DPP_MAXU(v, 0x112, 0xf, true); DPP_MAXU(v, 0x114, 0xf, true); DPP_MAXU(v, 0x118, 0xf, true);
  DPP_MAXU(v, 0x142, 0xa, false); DPP_MAXU(v, 0x143, 0xc, false);
  return (unsigned)__builtin_amdgcn_readlane((int)v, 63);
}
template <int N> __device__ __forceinline__ void wait_vm() { asm volatile("s_waitcnt vmcnt(%0)" :: "n"(N) : "memory"); }
#define WAIT_LGKM0() asm volatile("s_waitcnt lgkmcnt(0)" ::: "memory")
#define BAR() do { asm volatile("" ::: "memory"); __builtin_amdgcn_s_barrier(); asm volatile("" ::: "memory"); } while (0)
#define MFMA(a, b, c) __builtin_amdgcn_mfma_f32_16x16x32_bf16(a, b, c, 0, 0, 0)
#define BF8(v) __builtin_bit_cast(bf16x8, v)
constexpr int SPIN_CAP = 1 << 20;
#define UP_T 0x0507060508070605ull
#define T1_T 0x090a09090a0a0909ull
#define GAD 0
#define NWA1 16
#define NWA 16
#define NWC 28
#define TM0 0
#define TM1 0
#define TMB 3
#define WB_AT 12
#define AM_AT 15
#define GV_SL 0
#define MB0 2
#define MBS 0
#define MBD 2
#define MBT 0x80402018ull
#define MBW 0xeeeeea500ull
#define BSL2 13
#define GGD 7
#define FIN_AT 34
#define EG0 1
#define EGD 1
#define PEARLY 0
#define T7 2
#define DTM 0
#define FBLK 1
#define WPOL NT
#define OPOL SC1
#define KM 6
#define QR 0
#define PI 0x2e32363c
#define SWT 0x100d0a070400ull
#define SWE (int)(SWT >> 56 ? SWT >> 56 : SWT >> 48 ? SWT >> 48 : SWT >> 40)
#define Q2E 1
#define FA_ 1
#define FB_ 7
#define AM_P 17
#define GX1 7
#define BDEF 0
#define XEJ 1
#define QS 0
#define WBS 0x11001100
#define ZJ 5
#define EW 1
#define ZBLK 2
#define TWO 0
__device__ __forceinline__ int sched(u64 tab, int te) { return (int)(tab >> (te * 8)) & 255; }
#define PIN4(a, b, c, d) asm volatile("" : "+v"(a), "+v"(b), "+v"(c), "+v"(d))
__device__ __forceinline__ unsigned vm_now() { const unsigned r = __builtin_amdgcn_s_getreg(7 | (31 << 11)); return (r & 15u) | ((r >> 18) & 0x30u); }
__device__ __forceinline__ unsigned mk_tag(unsigned epoch) { return rfl(epoch) * 0x9E3779B1u + 0x7F4A7C15u; }

template <int LD, int AUX, int MODE, int NP = 16, int J0 = 0>
__device__ __forceinline__ void issue(rsrc_t r, int voff, int koff, LAS char* dst) {
#pragma unroll
  for (int j = J0; j < NP; ++j) {
    const int so = MODE == 2 ? j * 1024 : MODE == 1 ? (32 * (j >> 3) + 64 * ((j >> 2) & 1) + 16 * (j & 1) + 4 * ((j >> 1) & 1)) * LD : j * 8 * LD;
    __builtin_amdgcn_raw_ptr_buffer_load_lds(r, (LAS void*)(dst + j * 1024), 16, voff, koff + so, 0, AUX);
  }
}
__device__ __forceinline__ unsigned ktab(int tp1, int te) {
  const int j = tp1 & 3;
  const unsigned t = (tp1 * 0x01010101u ^ 0x03020100u) | (((tp1 & 12) ^ 4) * 0x10101010u ^ (j == 1 ? 0x30002010u : j == 2 ? 0x30100020u : 0x30201000u));
  const unsigned m = t >> 4 & 0xffffffu, r = (QR >> 4 * te & 7) * 4;
  return (t & 0xf000000fu) | ((m >> r | m << (24 - r)) & 0xffffffu) << 4;
}
__device__ __forceinline__ int rot1(int tp1, int tq1) { return KM == 4 ? 4 * (tp1 + tq1) & 15 : (8 * (tp1 + tq1) + 2 * (((tp1 >> 2) + 2 * (tq1 >> 3)) & 3)) & 15; }
__device__ __forceinline__ int kstep0(unsigned kt, int n) { return (int)(kt >> (n >> 1) * 4 & 15) * 2 + (n & 1); }
__device__ __forceinline__ int kstep(unsigned kt, int n) { return kstep0(kt, n == 3 ? 4 : n == 4 ? 3 : n); }
constexpr int QROWS = 1024 + 128;

__device__ __forceinline__ void wait_sent(LAS char* lds) {
#pragma unroll 1
  for (int spins = 0; spins < SPIN_CAP; ++spins) {
    const u32x4 f = *(volatile LAS u32x4*)(lds + L_MISC + 32);
    if (rfl(f[0] & f[1] & f[2] & f[3])) break;
    __builtin_amdgcn_s_sleep(1);
  }
  asm volatile("" ::: "memory");
}

__device__ __forceinline__ void loader(int L, int lane, LAS char* lds, rsrc_t r1, int v1, int ko, const u16* gq, const char* gp, rsrc_t rws,
                                       int flag_off, int b, int tp1, int tq1 STAMP_ARG) {
  const int te = tq1 & 7;
  STAMP_DECL;
  __builtin_amdgcn_s_setprio(2);
  const int rsub = lane >> 3, ch = ((lane & 7) ^ rsub) * 16;
  const rsrc_t rq = mk_rsrc(gq, QROWS * S * 2);
  const int h = tp1 >> 3;
  unsigned kt = ktab(tp1, te);
  int vq = rsub * (S * 2) + ch, vp = lane * 16;
  int qa = h * 1024 * (S * 2), qb = ((1 - h) * 1024 + L * 64) * (S * 2);
  int q_ = (L ? qa : qb) + tp1 * 256;
  LAS char* d_ = lds + (1 - L) * UB;
  volatile LAS unsigned* misc = (volatile LAS unsigned*)(lds + L_MISC);
  unsigned fl = 0, tag;
  {
    const unsigned utab = L ? 0x8541u : 0x7230u;
    STAMP(1);
    LAS char* dst = lds + L * UB;
#pragma unroll 1
    for (int t = 0; t < 4; ++t) {
      issue<D * 2, 0, 1>(r1, v1, ko, dst);
      ko = (ko + 128) & 2047;
      dst = lds + ((utab >> (t * 4 + 4)) & 15) * UB;
      if (t == 1 || t == 2) { wait_vm<16>(); BAR(); }
    }
    LAS char* dt[4];
#pragma unroll
    for (int u = 0; u < 4; ++u) dt[u] = lds + ((utab >> (u * 4)) & 15) * UB;
    asm volatile("" : "+s"(kt), "+s"(qa), "+s"(qb), "+s"(q_), "+s"(d_) : "s"(rq));
    asm volatile("" : "+v"(vq), "+v"(vp));
#pragma unroll 1
    for (int t = 0; t < 3; ++t) {
#pragma unroll
      for (int u = 0; u < 4; ++u) {
        issue<D * 2, 0, 1, 8>(r1, v1, ko, dt[u]);
        int kn = (ko + 128) & 2047;
        asm volatile("" : "+s"(kn), "+s"(dt[(u + 1) & 3]));
        if (u == 1) asm volatile("ds_read_b32 %0, %1" : "=v"(tag) : "v"(L_MISC + 8) : "memory");
        issue<D * 2, 0, 1, 16, 8>(r1, v1, ko, dt[u]);
        ko = kn;
        wait_vm<32>();
        BAR();
      }
    }
    asm volatile("s_waitcnt lgkmcnt(0)" : "+v"(tag) :: "memory");
  }
#define POLL(o) asm volatile("buffer_load_dword %0, %1, %2, 0 offen sc1" : "+v"(fl) : "v"(o), "s"(rws) : "memory")
#define POLLW(o) asm volatile("buffer_load_dword %0, %1, %2, 0 offen sc1\n\ts_waitcnt vmcnt(0)" : "+v"(fl) : "v"(o), "s"(rws) : "memory")
  {
    issue<S * 2, 0, 0>(rq, vq, q_, d_);
    const int q2 = qa + L * 64 * (S * 2) + kstep(kt, 2) * 128;
    wait_vm<32>();
    BAR();
    if (Q2E == 1) issue<S * 2, 0, 0, 8>(rq, vq, q2, lds + 2 * SLOT + L * 8192);
    wait_vm<Q2E == 1 ? 24 : 16>();
    BAR();
    issue<S * 2, 0, 0>(rq, vq, q_ + 128, d_ + SLOT);
    tag = rfl(tag);
    BAR();
    if (Q2E == 0) { issue<S * 2, 0, 0, 8>(rq, vq, q2, lds + 2 * SLOT + L * 8192); issue<S * 2, 0, 0, 8>(rq, vq, qb + kstep(kt, 2) * 128, lds + 2 * SLOT + UB + L * 8192); }
    wait_vm<Q2E == 0 ? 32 : 16>();
    BAR();
    if (Q2E == 2) issue<S * 2, 0, 0, 8>(rq, vq, q2, lds + 2 * SLOT + L * 8192);
    if (Q2E) issue<S * 2, 0, 0, 8>(rq, vq, qb + kstep(kt, 2) * 128, lds + 2 * SLOT + UB + L * 8192);
  }
  const int vx = (lane ^ (lane >> 2 & 12)) * 16;
  if (L) {
    int k_ = kstep(kt, 3) * 128, ka = qa + k_;
    LAS char* sl = lds;
    wait_vm<Q2E == 1 ? 8 : 16>();
    BAR();
    if (QS) __builtin_amdgcn_s_sleep(QS);
#pragma unroll 1
    for (int n = 4; n < 16; ++n) {
      issue<S * 2, 0, 0>(rq, vq, ka, sl);
      const int kb = qb + k_;
      LAS char* const sb = sl + UB + 8192;
      k_ = kstep(kt, n) * 128; ka = qa + k_;
      sl = sl == lds + 2 * SLOT ? lds : sl + SLOT;
      asm volatile("" : "+s"(k_), "+s"(ka), "+s"(sl));
      issue<S * 2, 0, 0, 8>(rq, vq, kb, sb);
      wait_vm<24>();
      BAR();
    }
    issue<S * 2, 0, 0>(rq, vq, qa + (k_ ^ 2048), lds);
    const int f2o = WS_FLAG2 + (b ^ 4) * 128 + 16 + (lane & 3) * 4;
    wait_vm<16>();
    BAR();
    wait_vm<0>();
    BAR();
    issue<S * 2, 0, 0, 8>(rq, vq, qa + 64 * (S * 2) + k_, lds + 4 * UB + 8192);
    wait_vm<0>();
    BAR();
    wait_sent(lds);
#pragma unroll 1
    for (int spins = 0; spins < SPIN_CAP; ++spins) {
      POLLW(f2o);
      if (__all(fl == tag)) break;
      __builtin_amdgcn_s_sleep(1);
    }
    STAMP(4);
    issue<0, SC1, 2>(rws, vx, WS_XCH + (b ^ 4) * XB + 32768, lds + 4 * UB);
    wait_vm<0>();
    misc[12] = 1u;
    STAMP(5);
  } else {
#define QHALF(n, sl) issue<S * 2, 0, 0, 8>(rq, vq, qb + kstep(kt, n) * 128, dqb + (sl))
    LAS char* const dqb = lds + UB;
    const rsrc_t rp = mk_rsrc(gp, 128 * S * 2);
    unsigned mask = 0;
    const int fo = flag_off + 32768 + (((lane >> 2) & 7) == (tp1 & 7) ? (kt >> 28) ^ 8 : h * 8 + ((lane >> 2) & 7)) * 128 + (lane & 3) * 4;
#define KM_(k) (15u << 2 * ((k) & 14))
    const int fo1 = flag_off + (kt >> (lane & 4 ? 8 : 4) & 15) * 128 + (lane & 3) * 4;
    unsigned m1 = 0;
    if (PEARLY) {
      POLLW(fo1);
      m1 = (unsigned)__ballot(fl == tag);
      if ((m1 & 15) == 15) issue<0, SC1, 2>(rp, vp, kstep(kt, 2) * UB, lds + 2 * SLOT + 2 * UB);
    } else wait_vm<Q2E == 1 ? 8 : 16>();
    BAR();
    STAMP(2);
    {
      const bool p2 = (m1 & 15) == 15;
      unsigned f1, f2;
      asm volatile("buffer_load_dword %0, %1, %2, 0 offen sc1" : "=&v"(f1) : "v"(fo1), "s"(rws) : "memory");
      QHALF(3, 0);
      asm volatile("buffer_load_dword %0, %2, %3, 0 offen sc1\n\ts_waitcnt vmcnt(9)" : "=&v"(f2), "+v"(f1) : "v"(fo1), "s"(rws) : "memory");
      m1 |= (unsigned)__ballot(f1 == tag);
      if (!p2) {
#pragma unroll 1
        for (int spins = 0; (m1 & 15) != 15 && spins < SPIN_CAP; ++spins) {
          asm volatile("s_sleep 2\n\tbuffer_load_dword %0, %2, %3, 0 offen sc1\n\ts_waitcnt vmcnt(1)" : "+v"(f1), "+v"(f2) : "v"(fo1), "s"(rws) : "memory");
          m1 |= (unsigned)__ballot(f2 == tag);
          if ((m1 & 15) == 15) break;
          asm volatile("s_sleep 2\n\tbuffer_load_dword %0, %2, %3, 0 offen sc1\n\ts_waitcnt vmcnt(1)" : "+v"(f2), "+v"(f1) : "v"(fo1), "s"(rws) : "memory");
          m1 |= (unsigned)__ballot(f1 == tag);
        }
        STAMP(3);
        issue<0, SC1, 2>(rp, vp, kstep(kt, 2) * UB, lds + 2 * SLOT + 2 * UB);
        asm volatile("s_waitcnt vmcnt(16)" : "+v"(f1), "+v"(f2) :: "memory");
      } else asm volatile("s_waitcnt vmcnt(0)" : "+v"(f1), "+v"(f2) :: "memory");
      m1 |= (unsigned)(__ballot(f1 == tag) | __ballot(f2 == tag));
      const bool p3 = (m1 & 0xf0) == 0xf0;
      if (!p3) {
        POLL(fo1);
        wait_vm<1>();
        BAR();
        asm volatile("s_waitcnt vmcnt(0)" : "+v"(fl) :: "memory");
#pragma unroll 1
        for (int spins = 0; ((unsigned)__ballot(fl == tag) & 0xf0) != 0xf0 && spins < SPIN_CAP; ++spins) POLLW(fo1);
      }
      POLL(fo);
      issue<0, SC1, 2>(rp, vp, kstep(kt, 3) * UB, lds + 2 * UB);
      if (p3) { wait_vm<17>(); BAR(); }
      asm volatile("s_waitcnt vmcnt(16)" : "+v"(fl) :: "memory");
    }
#define NEED(need) do { if (__builtin_expect(mask != ~0u, 0)) { asm volatile("" : "+v"(fl) :: "memory"); mask |= (unsigned)__ballot(fl == tag); \
      _Pragma("unroll 1") for (int spins = 0; (mask & (need)) != (need) && spins < SPIN_CAP; ++spins) { POLLW(fo); mask |= (unsigned)__ballot(fl == tag); } \
      if (mask != ~0u) POLL(fo); } } while (0)
    int k_ = kstep(kt, 4), kp = k_ * UB;
    LAS char* sl = lds + SLOT;
#pragma unroll 1
    for (int n = 5; n < 16; ++n) {
      NEED(KM_(k_));
      issue<0, SC1, 2>(rp, vp, kp, sl + 2 * UB);
      const int kb = qb + k_ * 128;
      LAS char* const sb = sl + UB;
      k_ = kstep0(kt, n); kp = k_ * UB;
      sl = sl == lds + 2 * SLOT ? lds : sl + SLOT;
      asm volatile("" : "+s"(k_), "+s"(kp), "+s"(sl));
      issue<S * 2, 0, 0, 8>(rq, vq, kb, sb);
      wait_vm<24>();
      BAR();
    }
    NEED(~0u);
    issue<0, SC1, 2>(rp, vp, (k_ ^ 16) * UB, lds + 2 * UB);
    wait_vm<16>();
    BAR();
    issue<0, SC1, 2>(rp, vp, k_ * UB, lds + UB);
    wait_vm<16>();
    BAR();
    issue<S * 2, 0, 0, 8>(rq, vq, qa + k_ * 128, lds + 4 * UB);
    const int f2o = WS_FLAG2 + (b ^ 2 ^ (lane & 4)) * 128 + (lane & 3) * 4;
    wait_vm<0>();
    BAR();
    if (EW) wait_sent(lds);
#pragma unroll 1
    for (int spins = 0; spins < SPIN_CAP; ++spins) {
      POLLW(f2o);
      if (__all(fl == tag)) break;
      __builtin_amdgcn_s_sleep(1);
    }
    issue<0, SC1, 2>(rws, vx, WS_XCH + (b ^ 2) * XB, lds + 6 * UB);
    issue<0, SC1, 2>(rws, vx, WS_XCH + (b ^ 6) * XB + 16384, lds + 7 * UB);
    STAMP(4);
    wait_vm<0>();
    misc[13] = 1u;
    STAMP(5);
  }
  STAMP_OUT();
}

#define LDF(off) (*reinterpret_cast<const bf16x8*>(lds + (off)))
struct Frag { bf16x8 p[4], q[4]; };
#define SGB(m, n) __builtin_amdgcn_sched_group_barrier(m, n, 0)
__device__ __forceinline__ float silu_fast(float z) { return z * __builtin_amdgcn_rcpf(1.0f + __expf(-z)); }
__device__ __forceinline__ u32x4 pack8(const f32x4& a, const f32x4& c) {
  return u32x4{pack2(a[0], a[1]), pack2(a[2], a[3]), pack2(c[0], c[1]), pack2(c[2], c[3])};
}
__device__ __forceinline__ unsigned ln_pair(unsigned x, float nmb, float rden) {
  const unsigned d = pack2(lo2f(x) + nmb, hi2f(x) + nmb);
  return pack2(lo2f(d) * rden, hi2f(d) * rden);
}

template <bool BARRIER, bool LAST, bool ZERO, class X>
__device__ __forceinline__ void big_half(f32x4 (&a0)[4][4], f32x4 (&a1)[4][4], const bf16x8 (&pc)[4], bf16x8 (&pn)[4], bf16x8 (&q)[8],
                                         const char* lds, int qc, int pa, int qn, int qn2, X extra) {
  const f32x4 zero = {0.f, 0.f, 0.f, 0.f};
#pragma unroll
  for (int g = 0; g < 8; ++g) {
    if (g == 0) { q[5] = LDF(qc + UB + 2048); q[6] = LDF(qc + UB + 4096); }
    if (g == 1) q[7] = LDF(qc + UB + 6144);
    if (g == 3) { pn[0] = LDF(pa); pn[1] = LDF(pa + 2048); }
    if (g == 4) { pn[2] = LDF(pa + 4096); pn[3] = LDF(pa + 6144); }
    if (g == 5) { q[0] = LDF(qn); q[1] = LDF(qn + 2048); }
    if (g == 6) { q[2] = LDF(qn + 4096); q[3] = LDF(qn + 6144); }
    if (g == 7 && !LAST) q[4] = LDF(qn2);
#pragma unroll
    for (int i = 0; i < 4; ++i) {
      const int ii = (g & 1) ? 3 - i : i;
      if (g < 4) a0[ii][g] = MFMA(pc[ii], q[g], ZERO ? zero : a0[ii][g]); else a1[ii][g - 4] = MFMA(pc[ii], q[g], ZERO ? zero : a1[ii][g - 4]);
    }
    SGB(0x8, 1); SGB(0x100, 1); SGB(0x8, 1); SGB(0x100, 1); SGB(0x8, 2);
    __builtin_amdgcn_sched_barrier(0);
    if (g == 2 && BARRIER) { WAIT_LGKM0(); BAR(); }
    extra(g);
  }
}

template <bool JOBNEXT>
__device__ __forceinline__ void small_step(f32x4 (&a)[4][4], bf16x8 (&p0)[4], bf16x8 (&p1)[4], bf16x8 (&q)[8], const char* lds,
                                           int ph, int qh, int pn, int qn, int qn2) {
#pragma unroll
  for (int g = 0; g < 8; ++g) {
    if (g == 0) { q[4] = LDF(qh); p1[0] = LDF(ph); }
    if (g == 1) { p1[1] = LDF(ph + 2048); p1[2] = LDF(ph + 4096); }
    if (g == 2) { p1[3] = LDF(ph + 6144); q[5] = LDF(qh + 2048); }
    if (g == 3) { q[6] = LDF(qh + 4096); q[7] = LDF(qh + 6144); }
    if (g == 4) { p0[0] = LDF(pn); q[0] = LDF(qn); }
    if (g == 5) { p0[1] = LDF(pn + 2048); p0[2] = LDF(pn + (JOBNEXT ? 1024 : 4096)); if (JOBNEXT) q[1] = LDF(qn + 2048); }
    if (g == 6) { p0[3] = LDF(pn + (JOBNEXT ? 3072 : 6144)); if (JOBNEXT) { q[2] = LDF(qn + 4096); q[3] = LDF(qn + 6144); } else q[1] = LDF(qn + 2048); }
    if (g == 7) { if (JOBNEXT) { q[4] = LDF(qn2); q[5] = LDF(qn2 + 2048); } else { q[2] = LDF(qn + 4096); q[3] = LDF(qn + 6144); } }
#pragma unroll
    for (int i = 0; i < 4; ++i) {
      const int ii = (g & 1) ? 3 - i : i;
      a[ii][g & 3] = MFMA(g < 4 ? p0[ii] : p1[ii], q[g], a[ii][g & 3]);
    }
    SGB(0x8, 1); SGB(0x100, 1); SGB(0x8, 1); SGB(0x100, 1); SGB(0x8, 1); SGB(0x100, 1); SGB(0x8, 1);
    __builtin_amdgcn_sched_barrier(0);
    if (g == 3) BAR();
  }
}

__device__ __forceinline__ void consumer(int wid, int lane, int b, char* lds, rsrc_t rws, rsrc_t rout,
                                         const char* code, int tp1, int tq1 STAMP_ARG) {
  STAMP_DECL;
  const int z = tq1 >> 3, te = tq1 & 7;
  const int wp = wid >> 1, wq = wid & 1, fr = lane & 15, fq = lane >> 4;
  const int c0 = (fq ^ (fr & 7)) * 16, c1 = ((fq + 4) ^ (fr & 7)) * 16;
  int q0 = (wq * 64 + fr) * 128 + c0, q1 = (wq * 64 + fr) * 128 + c1;
  volatile LAS unsigned* misc = (volatile LAS unsigned*)(lds + L_MISC);
  f32x4 acc[4][4];
#pragma unroll
  for (int i = 0; i < 4; ++i)
#pragma unroll
    for (int j = 0; j < 4; ++j) acc[i][j] = f32x4{0.f, 0.f, 0.f, 0.f};
  unsigned tag;
  asm volatile("" :: "s"(rout));
  u32x4 ob[4]; int bo[2];
  u32x4 xv[4];
  {
    Frag f0, f1;
    const int xrow = wq * 32 + (lane & 16) + 4 * (lane >> 5) + ((lane >> 2) & 3);
    const int xr = xrow * 128 + (((wp * 4 + (lane & 3)) ^ (xrow & 7)) * 16);
#define XV(u) do { _Pragma("unroll") for (int k = 0; k < 4; ++k) \
      xv[k] = *reinterpret_cast<const u32x4*>(lds + (u) * UB + xr + (k >> 1) * 8192 + (k & 1) * 1024); } while (0)
    const int p0 = (wp * 64 + fr) * 128 + c0, p1 = (wp * 64 + fr) * 128 + c1;
    unsigned tch = 0;
    if (wid == 3 && lane < (CODE_LINES + 7) / 8) {
      __builtin_amdgcn_s_sleep(8);
      const int co = min(((b >> 3) & 7) * ((CODE_LINES + 7) / 8) + lane, CODE_LINES - 1) * 128;
      asm volatile("global_load_dword %0, %1, off" : "+v"(tch) : "v"(code + co) : "memory");
    }
    const int xt = (2 * te + z - rot1(tp1, tq1)) & 15;
#define G1H(fc, fn, po, qo, EXTRA) do { _Pragma("unroll") for (int g = 0; g < 8; ++g) { \
      EXTRA(g); { if (g == 0) { fn.q[0] = LDF(qo); fn.p[0] = LDF(po); } else if (g == 1) { fn.p[1] = LDF((po) + 2048); fn.p[2] = LDF((po) + 4096); } \
        else if (g == 2) { fn.p[3] = LDF((po) + 6144); fn.q[1] = LDF((qo) + 2048); } else if (g == 3) { fn.q[2] = LDF((qo) + 4096); fn.q[3] = LDF((qo) + 6144); } } \
      _Pragma("unroll") for (int e = 0; e < 2; ++e) { const int i0 = (g & 1) * 2 + e, ii = (g & 2) ? 3 - i0 : i0; \
        acc[ii][g >> 1] = MFMA(fc.p[ii], fc.q[g >> 1], acc[ii][g >> 1]); } \
      SGB(0x8, 1); SGB(0x100, 1); SGB(0x8, 1); SGB(0x100, 2); __builtin_amdgcn_sched_barrier(0); } } while (0)
#define X_NO(g)
#define XI(u) ((u) == 0 ? 0 : (u) == 1 ? 3 : (u) == 2 ? 2 : 7)
#define WI(u) ((u) == 0 ? 1 : (u) == 1 ? 4 : (u) == 2 ? 5 : 8)
#define XVC(t, u) do { if (__builtin_expect((t) == xt, 0)) { XV(XI(u)); PIN4(xv[0], xv[1], xv[2], xv[3]); } __builtin_amdgcn_sched_barrier(0); } while (0)
    int pA[2] = {p0, p1}, pB[2] = {p0 + 7 * UB, p1 + 7 * UB}, qA[2] = {q0 + UB, q1 + UB}, qB[2] = {q0 + 5 * UB, q1 + 5 * UB};
    PIN4(pA[0], pA[1], pB[0], pB[1]); PIN4(qA[0], qA[1], qB[0], qB[1]);
#define XA(u, h) ((u) == 3 ? pB[h] : pA[h] + XI(u) * UB)
#define WA(u, h) ((u) < 2 ? qA[h] + (u) * 3 * UB : qB[h] + ((u) - 2) * 3 * UB)
    int ta = L_MB + (wp * 32 + fq * 8) * 4, tc = L_MB + 1024 + (wq * 32 + 8 * (fr >> 2) + (fr & 3)) * 4;
    const int flip = z ? 0 : 4096;
    int ao[2], ow[2];
#pragma unroll
    for (int h = 0; h < 2; ++h) {
      ao[h] = WS_ABT + tq1 * ABT_ST + tp1 * 2 * UB + wq * 8192 + wp * 1024 + lane * 16 + (h * 4096 ^ flip);
      ow[h] = 2 * UB + wq * 8192 + wp * 1024 + lane * 16 + (h * 4096 ^ flip);
    }
    PIN4(ao[0], ao[1], ow[0], ow[1]);
    asm volatile("" : "+v"(ta), "+v"(tc));
    BAR();
    XVC(0, 0);
    f0.q[0] = LDF(WA(0, 0));
#pragma unroll
    for (int i = 0; i < 4; ++i) f0.p[i] = LDF(XA(0, 0) + i * 2048);
#pragma unroll
    for (int i = 1; i < 4; ++i) f0.q[i] = LDF(WA(0, 0) + i * 2048);
    __builtin_amdgcn_sched_barrier(0);
#pragma unroll 1
    for (int t = 0; t < 12; t += 4) {
#pragma unroll
      for (int u = 0; u < 4; ++u) {
        G1H(f0, f1, XA(u, 1), WA(u, 1), X_NO);
        WAIT_LGKM0();
        BAR();
        XVC(t + u + 1, (u + 1) & 3);
        G1H(f1, f0, XA((u + 1) & 3, 0), WA((u + 1) & 3, 0), X_NO);
      }
    }
    u32x2 rdy;
#define X_RDY(g) if ((g) == 4) rdy = *(const volatile LAS u32x2*)(lds + L_MISC + 16)
    G1H(f0, f1, XA(0, 1), WA(0, 1), X_RDY);
    asm volatile("s_waitcnt vmcnt(0) lgkmcnt(0)" : "+v"(tch) :: "memory");
    BAR();
    XVC(13, 1);
    if (__builtin_expect(!rfl(rdy[0] & rdy[1]), 0)) {
#pragma unroll 1
      for (int spins = 0; spins < SPIN_CAP && !rfl(misc[4] & misc[5]); ++spins) __builtin_amdgcn_s_sleep(1);
    }
    asm volatile("" ::: "memory");
    f32x4 mb[2][2], rd[2][2]; float cs[4];
    unsigned tagv;
#define TB(o) (*reinterpret_cast<const f32x4*>(lds + ta + (o)))
#define X_T1(g) if ((g) >= 4) cs[(g) & 3] = *reinterpret_cast<const float*>(lds + tc + (64 * (((g) & 3) >> 1) + 4 * ((g) & 1)) * 4)
#define X_T2(g) if ((g) == 4) { mb[1][0] = TB(256); mb[1][1] = TB(272); } else if ((g) == 5) tagv = *(const LAS unsigned*)(lds + L_MISC + 8)
    G1H(f1, f0, XA(1, 0), WA(1, 0), X_T1);
    G1H(f0, f1, XA(1, 1), WA(1, 1), X_T2);
    WAIT_LGKM0();
    BAR();
    XVC(14, 2);
    Frag F[4];
#define SB() __builtin_amdgcn_sched_barrier(0)
#define FQ(k, i) F[k].q[i] = LDF(WA(2 + ((k) >> 1), (k) & 1) + (i) * 2048)
#define FP(k, i) F[k].p[i] = LDF(XA(2 + ((k) >> 1), (k) & 1) + (i) * 2048)
#define FA(k, r) do { if ((r) == 0) FQ(k, 0); else if ((r) < 3) FP(k, (r) - 1); else FQ(k, (r) - 2); } while (0)
#pragma unroll
    for (int n = 0; n < 16; ++n) {
      const int j = n >> 2, ii = (j & 1) ? 3 - (n & 3) : n & 3;
      acc[ii][j] = MFMA(f1.p[ii], f1.q[j], acc[ii][j]);
      SB();
      if ((n & 3) != 3) { const int m = n - (n >> 2); FA(m / 6, m % 6); }
      SB();
    }
    BAR();
    XVC(15, 3);
#define VFMA(d, a, c) asm volatile("v_fma_f32 %0, %1, %2, %0" : "+v"(d) : "v"(a), "v"(c))
#define VMUL(d, a) asm volatile("v_mul_f32 %0, %1, %0" : "+v"(d) : "v"(a))
    float cb[2][4][4];
#pragma unroll
    for (int n = 0; n < 32; ++n) {
      const int k = n >> 3, j = (n >> 1) & 3, ii = (n ^ n >> 1) & 1;
      acc[ii][j] = MFMA(F[k].p[ii], F[k].q[j], acc[ii][j]);
      SB();
      if ((n & 3) != 3) { const int m = n - (n >> 2); if (m < 12) FA(2 + m / 6, m % 6); else if (m < 20) FP((m - 12) >> 1, 2 + (m & 1)); }
      if (n == 19) mb[0][0] = TB(0); if (n == 23) mb[0][1] = TB(16); if (n == 26) rd[0][0] = TB(1536); if (n == 27) rd[0][1] = TB(1552);
      {
        const int jj = n >> 3, h = (n >> 2) & 1, r = n & 3;
        cb[h][jj][r] = acc[2 + h][jj][r];
        VFMA(cb[h][jj][r], mb[1][h][r], cs[jj]);
        acc[2 + h][jj][r] = cb[h][jj][r];
      }
      SB();
    }
    WAIT_LGKM0();
    BAR();
    tag = rfl(tagv);
    u32x4 o;
#pragma unroll
    for (int n = 0; n < 32; ++n) {
      const int k = n >> 3, j = (n >> 1) & 3, ii = 2 + ((n ^ n >> 1) & 1);
      acc[ii][j] = MFMA(F[k].p[ii], F[k].q[j], acc[ii][j]);
      SB();
      {
        const int h = (n >> 2) & 1, r = n & 3;
        cb[h][k][r] = acc[h][k][r];
        VFMA(cb[h][k][r], mb[0][h][r], cs[k]);
        if (n) { const int n1 = n - 1; VMUL(cb[(n1 >> 2) & 1][n1 >> 3][n1 & 3], rd[0][(n1 >> 2) & 1][n1 & 3]); }
        if (n > 2 && (n & 1)) { const int n2 = n - 3;
          unsigned o_ = pack2(cb[(n2 >> 2) & 1][n2 >> 3][n2 & 3], cb[(n2 >> 2) & 1][n2 >> 3][(n2 & 3) + 1]); asm volatile("" : "+v"(o_)); o[(n2 >> 1) & 3] = o_; }
        if (n == 20) rd[1][0] = TB(1792); if (n == 22) rd[1][1] = TB(1808);
        if (n > 8 && (n & 7) == 2) {
          __builtin_amdgcn_raw_buffer_store_b128(o, rws, ao[(k - 1) >> 1] + ((k - 1) & 1) * 2048, 0, SC1);
          *reinterpret_cast<u32x4*>(lds + ow[(k - 1) >> 1] + ((k - 1) & 1) * 2048) = o;
        }
      }
      SB();
    }
    STAMP(2);
    VMUL(cb[1][3][3], rd[0][1][3]);
    o[3] = pack2(cb[1][3][2], cb[1][3][3]);
    __builtin_amdgcn_raw_buffer_store_b128(o, rws, ao[1] + 2048, 0, SC1);
    *reinterpret_cast<u32x4*>(lds + ow[1] + 2048) = o;
#pragma unroll
    for (int j = 0; j < 4; ++j) {
      ob[j] = pack8(acc[2][j] * rd[1][0], acc[3][j] * rd[1][1]);
      if (!BDEF) __builtin_amdgcn_raw_buffer_store_b128(ob[j], rws, ao[j >> 1] + UB + (j & 1) * 2048, 0, SC1);
      *reinterpret_cast<u32x4*>(lds + ow[j >> 1] + SLOT + (j & 1) * 2048) = ob[j];
    }
    bo[0] = ao[0] + UB; bo[1] = ao[1] + UB;
    STAMP(3);
  }
  f32x4 acb[4][4];
  bf16x8 pa[4], pb[4], q[8];
  int pp = wp * 8192 + lane * 16;
  const int f1o = WS_FLAG1 + (tq1 * 16 + tp1) * 128 + wid * 4;
#define FLAG1(i, n) do { wait_vm<n>(); __builtin_amdgcn_raw_buffer_store_b32(tag, rws, lane ? -1 : f1o + (i) * 32768, 0, SC1); if (i) STAMP(4); __builtin_amdgcn_sched_barrier(0); } while (0)
  auto fl0 = [&](int g) {
    if (g == FA_) FLAG1(0, BDEF ? 0 : 4);
    if (BDEF && g >= FA_ && g < FA_ + 2) {
      __builtin_amdgcn_raw_buffer_store_b128(ob[2 * (g - FA_) & 3], rws, bo[(g - FA_) & 1], 0, SC1);
      __builtin_amdgcn_raw_buffer_store_b128(ob[2 * (g - FA_) + 1 & 3], rws, bo[(g - FA_) & 1] + 2048, 0, SC1);
      __builtin_amdgcn_sched_barrier(0);
    }
    if (g == FB_) FLAG1(1, BDEF ? 0 : 1); };
  WAIT_LGKM0();
  BAR();
  STAMP(5);
  pa[0] = LDF(2 * UB + pp); q[0] = LDF(q0); pa[1] = LDF(2 * UB + pp + 2048); pa[2] = LDF(2 * UB + pp + 4096); pa[3] = LDF(2 * UB + pp + 6144);
  q[1] = LDF(q0 + 2048); q[2] = LDF(q0 + 4096); q[3] = LDF(q0 + 6144); q[4] = LDF(q0 + UB);
  __builtin_amdgcn_sched_barrier(0);
  auto none = [](int) {};
  big_half<false, false, true>(acc, acb, pa, pb, q, lds, q0, 2 * UB + pp + 1024, q1, q1 + UB, fl0);
  PIN4(q0, q1, pp, lane);
  {
    int sl = 0;
#pragma unroll 1
    for (int n = 0; n < 12; ++n) {
      const int sn = sl == 2 * SLOT ? 0 : sl + SLOT;
      auto fl1 = [&](int g) { if (g + 8 == FB_ && __builtin_expect(n == 0, 0)) FLAG1(1, BDEF ? 0 : 1); };
      auto fl2 = [&](int g) { if (g + 16 == FB_ && __builtin_expect(n == 0, 0)) FLAG1(1, BDEF ? 0 : 1); };
      big_half<true, false, false>(acc, acb, pb, pa, q, lds, sl + q1, sn + 2 * UB + pp, sn + q0, sn + q0 + UB, fl1);
      big_half<false, false, false>(acc, acb, pa, pb, q, lds, sn + q0, sn + 2 * UB + pp + 1024, sn + q1, sn + q1 + UB, fl2);
      sl = sn;
    }
  }
  int q0h = q0 + 4 * UB, q1h = q1 + 4 * UB, pph = pp + 5 * UB;
  asm volatile("" : "+v"(q0h), "+v"(q1h), "+v"(pph));
  big_half<true, true, false>(acc, acb, pb, pa, q, lds, q1, pph, q0h, 0, none);
  small_step<false>(acb, pa, pb, q, lds, pph + 1024, q1h, pph + 3 * UB, q0h + 3 * UB, 0);
  small_step<true>(acb, pa, pb, q, lds, pph + 3 * UB + 1024, q1h + 3 * UB, pph, 3 * UB + q0, 3 * UB + q1);
  int xw = WS_XCH + b * XB + wid * 4096 + lane * 16;
  asm volatile("" : "+v"(xw));
#define SEND_E(j) __builtin_amdgcn_raw_buffer_store_b128(pack8(acb[(j) < 4 ? 2 : 0][(j) & 3], acb[(j) < 4 ? 3 : 1][(j) & 3]), rws, xw + ((j) >> 2) * 16384 + ((j) & 3) * 1024, 0, SC1)
  STAMP(10);
  const int f2o = WS_FLAG2 + b * 128 + wid * 4;
#define PA(k) ((k) == 0 ? pph : (k) == 1 ? pph + 3 * UB : (k) == 2 ? pp + 2 * UB : pp + UB)
#define QA0(k) ((k) == 0 ? q0 + 3 * UB : (k) == 1 ? q0h + 2 * UB : (k) == 2 ? q0 : q0h)
#define QA1(k) ((k) == 0 ? q1 + 3 * UB : (k) == 1 ? q1h + 2 * UB : (k) == 2 ? q1 : q1h)
#define JOB(K, I0, pc, qc, LC, pn, KN, IN, qx, LN, EXTRA) do { _Pragma("unroll") for (int g = 0; g < 8; ++g) { \
    if (g < 2 && (LC)) qc[g + 6] = LDF(QA1(K) + (g + 2) * 2048); \
    if ((KN) >= 0) { \
      if (g >= 2 && (LN)) qx[g - 2] = LDF(g < 6 ? QA0(KN) + (g - 2) * 2048 : QA1(KN) + (g - 6) * 2048); \
      if (g < 4) pn[g] = LDF(PA(KN) + ((IN) + (g & 1)) * 2048 + (g >> 1) * 1024); } \
    acc[I0][g & 3] = MFMA(pc[(g >> 2) * 2], qc[g], acc[I0][g & 3]); \
    acc[(I0) + 1][g & 3] = MFMA(pc[(g >> 2) * 2 + 1], qc[g], acc[(I0) + 1][g & 3]); \
    EXTRA(g); \
    SGB(0x8, 1); SGB(0x100, 1); SGB(0x2, 2); SGB(0x8, 1); SGB(0x100, 1); SGB(0x2, 2); \
    __builtin_amdgcn_sched_barrier(0); } \
    if ((KN) < 0) { PIN4(acc[2][0], acc[2][1], acc[2][2], acc[2][3]); PIN4(acc[3][0], acc[3][1], acc[3][2], acc[3][3]); } } while (0)
#define X_NONE(g)
#define X_E(n, g) if ((n) * 8 + (g) >= EG0 && ((n) * 8 + (g) - EG0) % EGD == 0 && ((n) * 8 + (g) - EG0) / EGD < 8) SEND_E(((n) * 8 + (g) - EG0) / EGD & 7)
#define X_E0(g) X_E(0, g)
#define X_E1(g) X_E(1, g)
  bf16x8 qb[8], qc_[8];
  JOB(0, 0, pa, q, 1, pb, 1, 0, q, 1, X_E0);
  JOB(1, 0, pb, q, 1, pa, 2, 0, qb, 1, X_E1);
  BAR();
  JOB(2, 0, pa, qb, 1, pb, 3, 0, qc_, 1, X_NONE);
#define FL(i, n) do { if ((i) == FBLK) { wait_vm<n>(); __builtin_amdgcn_raw_buffer_store_b32(tag, rws, lane ? -1 : f2o, 0, SC1); STAMP(9); } } while (0)
#define X_Z(n, g) if (ZJ == (n)) { if ((g) >= 1 && (g) < 5) __builtin_amdgcn_raw_buffer_store_b128(pack8(acc[0][((g) - 1) & 3], acc[1][((g) - 1) & 3]), rws, xw + 32768 + (((g) - 1) & 3) * 1024, 0, SC1); \
    else if ((g) == 5) misc[8 + wid] = 1u; }
#define FZ(i) do { if ((i) == ZBLK) { wait_vm<0>(); __builtin_amdgcn_raw_buffer_store_b32(tag, rws, lane ? -1 : f2o + 16, 0, SC1); STAMP(1); } } while (0)
#define X_Z5(g) X_Z(5, g)
#define X_Z6(g) X_Z(6, g)
#define X_Z7(g) X_Z(7, g)
  FL(0, 0);
  JOB(3, 0, pb, qc_, 1, pa, 3, 2, q, 0, X_NONE);
  FL(1, 0);
  JOB(3, 2, pa, qc_, 0, pb, 2, 2, qb, 0, X_Z5);
  FL(2, (ZJ > 5 ? 0 : 4));
  FZ(0);
  JOB(2, 2, pb, qb, 0, pa, 1, 2, q, 0, X_Z6);
  FZ(1);
  JOB(1, 2, pa, q, 0, pb, 0, 2, qb, 1, X_Z7);
  FZ(2);
  float tm[4], tr[4]; unsigned selv;
  const char* tb = lds + L_MB + (wq * 64 + (lane >> 2)) * 4;
#define X_TAB(g) if ((g) >= 2 && (g) < 6) { tm[((g) - 2) & 3] = *reinterpret_cast<const float*>(tb + ((g) - 2) * 64); tr[((g) - 2) & 3] = *reinterpret_cast<const float*>(tb + 512 + ((g) - 2) * 64); } \
  else if ((g) == 6) selv = misc[0]; \
  X_Z(8, g)
  JOB(0, 2, pb, qb, 1, pa, -1, 0, q, 0, X_TAB);
  STAMP(6);
  FZ(3);
  wait_sent((LAS char*)lds);
  {
    char* tw = lds + wid * 8192 + (lane & 15) * 64 + (((lane >> 4) ^ (lane >> 1)) & 3) * 16;
    const char* tr_ = lds + wid * 8192 + (lane >> 2) * 64 + ((lane ^ (lane >> 3)) & 3) * 16;
#pragma unroll
    for (int k = 0; k < 8; ++k) *reinterpret_cast<f32x4*>(tw + k * 1024) = acc[2 + (k >> 2)][k & 3];
    asm volatile("" ::: "memory");
#pragma unroll
    for (int k = 0; k < 8; ++k) acc[2 + (k >> 2)][k & 3] = *reinterpret_cast<const f32x4*>(tr_ + k * 1024);
    __builtin_amdgcn_sched_barrier(0);
  }
  PIN4(xv[0], xv[1], xv[2], xv[3]);
  f32x2 gf[4][4];
#pragma unroll
  for (int k = 0; k < 4; ++k) {
    const float tn = tm[k] * tr[k];
#pragma unroll
    for (int c = 0; c < 4; ++c) {
      const f32x2 n = f32x2{lo2f(xv[k][c]), hi2f(xv[k][c])} * tr[k] + tn, a = n * -1.44269504f;
      const f32x2 d = f32x2{__builtin_amdgcn_exp2f(a[0]), __builtin_amdgcn_exp2f(a[1])} + 1.0f;
      gf[k][c] = n * f32x2{__builtin_amdgcn_rcpf(d[0]), __builtin_amdgcn_rcpf(d[1])};
    }
    PIN4(gf[k][0], gf[k][1], gf[k][2], gf[k][3]);
    FZ(4 + k);
  }
  f32x2 xf[4][4];
#pragma unroll
  for (int k = 0; k < 4; ++k) {
#pragma unroll
    for (int c = 0; c < 4; ++c) xf[k][c] = f32x2{lo2f(xv[k][c]), hi2f(xv[k][c])};
    PIN4(xf[k][0], xf[k][1], xf[k][2], xf[k][3]);
  }
  {
    unsigned sel = rfl(selv);
    if (__builtin_expect(!sel || (int)(sel - 1 >> 6) == tp1 * 2 + wq, 0)) {
#pragma unroll 1
      for (int spins = 0; spins < SPIN_CAP && !sel; ++spins) { sel = rfl(misc[0]); __builtin_amdgcn_s_sleep(2); }
      const int idx = (int)sel - 1;
      if ((idx >> 6) == tp1 * 2 + wq) {
        const int ks = (idx >> 4) & 3;
        const u32x4 xs = ks == 0 ? xv[0] : ks == 1 ? xv[1] : ks == 2 ? xv[2] : xv[3];
        const float ms = ks == 0 ? tm[0] : ks == 1 ? tm[1] : ks == 2 ? tm[2] : tm[3], rs = ks == 0 ? tr[0] : ks == 1 ? tr[1] : ks == 2 ? tr[2] : tr[3];
        u32x4 n;
#pragma unroll
        for (int c = 0; c < 4; ++c) n[c] = ln_pair(xs[c], ms, rs);
        asm volatile("" : "+v"(n));
#pragma unroll 1
        for (int spins = 0; spins < SPIN_CAP && !rfl(misc[1]); ++spins) __builtin_amdgcn_s_sleep(1);
        asm volatile("" ::: "memory");
        const LAS float* upd = (const LAS float*)(lds + L_UPD) + wp * 32 + (lane & 3) * 8;
#pragma unroll
        for (int c = 0; c < 4; ++c) {
          const unsigned u = pack2(lo2f(n[c]) + upd[2 * c], hi2f(n[c]) + upd[2 * c + 1]);
          const f32x2 gn = {silu_fast(lo2f(u)), silu_fast(hi2f(u))};
#pragma unroll
          for (int k = 0; k < 4; ++k) if (ks == k && (idx & 15) == (lane >> 2)) gf[k][c] = gn;
        }
      }
    }
  }
  int xo = ((tp1 * 128 + wq * 64 + (lane >> 2)) * D + te * 128 + z * 64 + wp * 32 + (lane & 3) * 8) * 2, le = wid * 4096 + (lane & 3) * 256 + ((lane >> 2) ^ (lane & 3) * 4) * 16 + 4 * UB;
  const unsigned one = (lane >> 4) == ((lane >> 2) & 3) ? (lane & 1 ? 0x3f800000u : 0x3f80u) : 0u;
  u32x4 s0 = {lane & 2 ? 0u : one, lane & 2 ? one : 0u, 0u, 0u}, s1 = {0u, 0u, s0[0], s0[1]};
  PIN4(xo, le, s0, s1);
  STAMP(7);
#define PIECES(i0, i1) _Pragma("unroll") for (int k = 0; k < 4; ++k) _Pragma("unroll") for (int i = i0; i < (i1); ++i) \
    pc[i][k] = LDF(le + (i ? i + 1 : 0) * UB + k * 1024)
#define ADDP(k, i0, i1) _Pragma("unroll") for (int i = i0; i < (i1); ++i) { acc[2][k] = MFMA(BF8(s0), pc[i][k], acc[2][k]); acc[3][k] = MFMA(BF8(s1), pc[i][k], acc[3][k]); }
  bf16x8 pc[3][4];
#pragma unroll 1
  for (int spins = 0; spins < SPIN_CAP; ++spins) { const u32x2 f = *(volatile LAS u32x2*)(lds + L_MISC + 48); if (rfl(f[0] & f[1])) break; }
  asm volatile("" ::: "memory");
  STAMP(11);
  PIECES(0, TWO ? 1 : 3);
  __builtin_amdgcn_sched_barrier(0);
#pragma unroll
  for (int k = 0; k < 4; ++k) ADDP(k, 0, TWO ? 1 : 3);
#pragma unroll
  for (int k = 0; k < 4; ++k) {
    u32x4 ov;
#pragma unroll
    for (int c = 0; c < 4; ++c) {
      const f32x2 r = f32x2{acc[2 + (c >> 1)][k][(c & 1) * 2], acc[2 + (c >> 1)][k][(c & 1) * 2 + 1]} * gf[k][c] + xf[k][c];
      ov[c] = pack2(r[0], r[1]);
    }
    __builtin_amdgcn_raw_buffer_store_b128(ov, rout, xo + k * 16 * D * 2, 0, OPOL);
  }
  STAMP(8);
  STAMP_OUT();
}

#define BCAST(v, k) ((unsigned)__builtin_amdgcn_mov_dpp((int)(v), 0x150 + (k), 0xf, 0xf, true))
template <int K0, int K1>
__device__ __forceinline__ void expand(u32x4 (&dg)[16], unsigned xs, const unsigned (&sel)[4]) {
  if constexpr (K0 < K1) {
    const unsigned v = BCAST(xs, K0);
    dg[K0] = u32x4{__builtin_amdgcn_perm(0u, v, sel[0]), __builtin_amdgcn_perm(0u, v, sel[1]), __builtin_amdgcn_perm(0u, v, sel[2]), __builtin_amdgcn_perm(0u, v, sel[3])};
    asm volatile("" : "+v"(dg[K0]));
    expand<K0 + 1, K1>(dg, xs, sel);
  }
}
template <int K0, int K1, int N>
__device__ __forceinline__ void gemv(f32x4& ae, f32x4& ao, unsigned xs, const unsigned (&sel)[4], const u32x4 (&w)[N]) {
  if constexpr (K0 < K1) {
    const unsigned v = BCAST(xs, K0);
    const bf16x8 dg = BF8((u32x4{__builtin_amdgcn_perm(0u, v, sel[0]), __builtin_amdgcn_perm(0u, v, sel[1]),
                                 __builtin_amdgcn_perm(0u, v, sel[2]), __builtin_amdgcn_perm(0u, v, sel[3])}));
    ae = MFMA(dg, BF8(w[2 * K0]), ae); ao = MFMA(dg, BF8(w[2 * K0 + 1]), ao);
    gemv<K0 + 1, K1, N>(ae, ao, xs, sel, w);
  }
}

__device__ __forceinline__ void helper(int hw, int lane, int b, char* lds, rsrc_t rws, rsrc_t rx, const u16* wr,
                                       const u16* wgu, const u16* wdn, const u16* gq, const char* code, int tp1, int tq1 STAMP_ARG) {
  STAMP_DECL;
  int nb = 0, lim = 14;
  unsigned tch = 0;
  volatile LAS unsigned* misc = (volatile LAS unsigned*)(lds + L_MISC);
  __builtin_amdgcn_s_setprio(3);
  const int z = tq1 >> 3, te = tq1 & 7;
  const int lc = lane & 15, lq = lane >> 4;
  const int r1 = rot1(tp1, tq1), kb = r1 >> 2, amo = WS_AMAX + (tq1 * 16 + tp1) * 128;
#define JOIN() do { HPRE(); BAR(); HSTAMP(); ++nb; } while (0)
#define NEXT() do { if (nb < lim) JOIN(); } while (0)
#define UNTIL(m) do { _Pragma("unroll 1") while (nb < (m)) JOIN(); } while (0)
#define POLLVM(m) do { _Pragma("unroll 1") while (vm_now() > (m) && nb < lim) JOIN(); wait_vm<m>(); } while (0)
  unsigned tag, key, sg; int idx;
  float sw;
  const int rg = b / 28, cg = b - rg * 28;
  const int fg = b >> 3, eg = b & 7;
  u32x4 w[32], wd[14];
  const rsrc_t rgu = mk_rsrc(wgu + (size_t)(rg & 7) * 128 * F + cg * 128, b < 224 ? 128 * F * 2 : 0);
  const rsrc_t rd = mk_rsrc(wdn + (size_t)(fg * 112 + hw * 56) * D + eg * 128, 56 * D * 2);
  const int wvo = lq * 32 * F * 2 + lc * 16, dvo = lq * 14 * D * 2 + lc * 16;
#define LOADW(k0, k1) do { _Pragma("unroll") for (int k = k0; k < (k1); ++k) \
    w[k] = __builtin_amdgcn_raw_buffer_load_b128(rgu, wvo, k * F * 2, WPOL); asm volatile("" ::: "memory"); } while (0)
#define LOADD(k0, k1) do { _Pragma("unroll") for (int k = k0; k < (k1); ++k) \
    wd[k] = __builtin_amdgcn_raw_buffer_load_b128(rd, dvo, k * D * 2, WPOL); asm volatile("" ::: "memory"); } while (0)
  {
    const rsrc_t rr = mk_rsrc(wr, D * 2);
    u32x4 wf[8];
#pragma unroll
    for (int i = 0; i < 8; ++i) wf[i] = __builtin_amdgcn_raw_buffer_load_b128(rr, lq * 16 + (i & 3) * 64, ((r1 + (i >> 2) * 2) & 15) * 128, 0);
    const unsigned epoch = __builtin_amdgcn_raw_buffer_load_b32(rws, lane == 1 ? (hw ? amo : WS_FLAG2 + b * 128) : lane == 2 ? amo + (WS_FLAG1 - WS_AMAX) + hw * 32768 : WS_EPOCH, 0, SC1);
    const u32x4 ra = __builtin_amdgcn_raw_buffer_load_b128(rr, lane * 16, 0, 0), rb2 = __builtin_amdgcn_raw_buffer_load_b128(rr, lane * 16 + 1024, 0, 0);
    misc[lc] = 0u;
    u32x4 ones = {0x3f803f80u, 0x3f803f80u, 0x3f803f80u, 0x3f803f80u};
    const int sx = (KM == 4 ? (te >> 2) + 2 * z : te >> 1) * 32 + hw * 16, sv = (KM == 4 ? tp1 >> 2 : tp1 >> 1 & 3) * 32 + hw * 16;
    int xa[2], wa[2];
    xa[0] = (sx + lc) * 128 + (lq ^ (lc & 7)) * 16; wa[0] = xa[0] + (sv - sx) * 128 + UB;
    xa[1] = xa[0] ^ 64; wa[1] = wa[0] ^ 64;
    asm volatile("" : "+v"(ones));
    f32x4 d1 = {0.f, 0.f, 0.f, 0.f}, d2 = d1, d3 = d1, d4 = d1;
    HST(9);
#pragma unroll
    for (int u = 0; u < 4; ++u) {
      if (u == 2) { wa[0] += 4 * UB; wa[1] += 4 * UB; }
      if (u == 3) { xa[0] += 7 * UB; xa[1] += 7 * UB; }
      PIN4(xa[0], xa[1], wa[0], wa[1]);
      WAIT_LGKM0();
      JOIN();
      const unsigned tj = (unsigned)__builtin_amdgcn_s_memrealtime();
      bf16x8 xf[2], vf[2];
#pragma unroll
      for (int h = 0; h < 2; ++h) { xf[h] = LDF(xa[h] + (u == 3 ? 0 : XI(u) * UB)); vf[h] = LDF(wa[h] + (u & 1) * 3 * UB); }
      __builtin_amdgcn_sched_barrier(0);
      if (u) LOADW((int)(SWT >> 8 * u & 63), (int)(SWT >> 8 * u + 8 & 63));
#pragma unroll
      for (int h = 0; h < 2; ++h) {
        d2 = MFMA(xf[h], xf[h], d2); d4 = MFMA(BF8(ones), xf[h], d4); d3 = MFMA(BF8(ones), vf[h], d3); d1 = MFMA(BF8(wf[2 * u + h]), xf[h], d1);
      }
      PIN4(d1, d2, d3, d4);
      if (u == 0) {
        tag = mk_tag(epoch);
        asm volatile("" : "+s"(tag));
        misc[2] = tag;
        const f32x4 d5 = MFMA(BF8(rb2), BF8(ones), MFMA(BF8(ra), BF8(ones), (f32x4{0.f, 0.f, 0.f, 0.f})));
        const int s4 = __builtin_bit_cast(int, (d5[0] + d5[1]) + (d5[2] + d5[3]));
#define RL(l) __builtin_bit_cast(float, __builtin_amdgcn_readlane(s4, l))
        sw = (RL(0) + RL(16)) + (RL(32) + RL(48));
        asm volatile("" : "+v"(sw));
        LOADW(0, (int)(SWT >> 8 & 63));
      }
      if (PI >> 8 * u & 255) {
#pragma unroll 1
        while ((unsigned)__builtin_amdgcn_s_memrealtime() - tj < (PI >> 8 * u & 255)) ;
      }
      __builtin_amdgcn_sched_barrier(0);
    }
    JOIN();
    const float sq = (lc & 2) ? ((lc & 1) ? d2[3] : d2[2]) : ((lc & 1) ? d2[1] : d2[0]);
    __builtin_amdgcn_raw_buffer_store_b128(u32x4{__float_as_uint(d4[0]), __float_as_uint(sq), __float_as_uint(d1[0]), tag}, rws,
                                           lq == lc >> 2 ? WS_XP + (tp1 * 4 + kb) * 2048 + (sx + lc) * 16 : -1, 0, SC1);
    __builtin_amdgcn_raw_buffer_store_b64(u32x2{__float_as_uint(d3[0]), tag}, rws, lane < 16 ? WS_WP + (tq1 * 4 + kb) * 1024 + (sv + lc) * 8 : -1, 0, SC1);
    __builtin_amdgcn_sched_barrier(0);
    LOADW((int)(SWT >> 32 & 63), (int)(SWT >> 40 & 63));
    if (SWT >> 48) { JOIN(); LOADW((int)(SWT >> 40 & 63), (int)(SWT >> 48 & 63)); }
    if (SWT >> 56) { JOIN(); LOADW((int)(SWT >> 48 & 63), (int)(SWT >> 56 & 63)); }
  }
  const rsrc_t rq = mk_rsrc(gq, QROWS * S * 2);
  const unsigned kt = ktab(tp1, te);
  const int trow = (te >> 2) * 1024 + (te & 3) * 32 + hw * 16;
  const int t1o = (trow + ((lane >> 1) & 15)) * (S * 2) + (lane & 1) * 128;
#define QTOUCH0(o, so) asm volatile("buffer_load_dword %0, %1, %2, %3 offen" : "+v"(tch) : "v"(o), "s"(rq), "s"(so) : "memory")
#define QTOUCH1(o, so) asm volatile("buffer_load_dword %0, %1, %2, %3 offen nt\n\tbuffer_load_dword %0, %1, %2, %3 offen" : "+v"(tch) : "v"(o), "s"(rq), "s"(so) : "memory")
#define QTOUCH(o, so) do { if (DTM >> i_ & 1) QTOUCH1(o, so); else QTOUCH0(o, so); } while (0)
#define TCH(m) do { _Pragma("unroll") for (int i_ = 0; i_ < 8; ++i_) if ((m) >> i_ & 1) { \
      if (i_ < 7) QTOUCH(t1o, kstep0(kt, 2 * i_) * 128); \
      else QTOUCH(lane & 16 ? ((tp1 >> 3) * 1024 + te * 16 + lc) * (S * 2) + (kstep(kt, 15) ^ hw * 16) * 128 : (trow + lc) * (S * 2) + kstep(kt, 14) * 128, 0); } } while (0)
  const int srow = hw * 32 + (lane & 3) + 2 * (lane & 44) + ((lane >> 2) & 4);
  {
    const int gxo = WS_XP + tp1 * 8192 + (hw * 64 + lane) * 16, gwo = WS_WP + tq1 * 4096 + (hw * 64 + lane) * 8;
    u32x4 gx[4]; u32x2 gw[4];
#define GATHER() asm volatile("buffer_load_dwordx4 %0, %8, %10, 0 offen sc1\n\tbuffer_load_dwordx4 %1, %8, %10, 0 offen offset:2048 sc1\n\t" \
                 "buffer_load_dwordx4 %2, %8, %10, %11 offen sc1\n\tbuffer_load_dwordx4 %3, %8, %10, %11 offen offset:2048 sc1\n\t" \
                 "buffer_load_dwordx2 %4, %9, %10, 0 offen sc1\n\tbuffer_load_dwordx2 %5, %9, %10, 0 offen offset:1024 sc1\n\t" \
                 "buffer_load_dwordx2 %6, %9, %10, 0 offen offset:2048 sc1\n\tbuffer_load_dwordx2 %7, %9, %10, 0 offen offset:3072 sc1" \
                 : "=&v"(gx[0]), "=&v"(gx[1]), "=&v"(gx[2]), "=&v"(gx[3]), "=&v"(gw[0]), "=&v"(gw[1]), "=&v"(gw[2]), "=&v"(gw[3]) \
                 : "v"(gxo), "v"(gwo), "s"(rws), "s"(4096) : "memory")
    UNTIL(sched(UP_T, te));
#define TCH7(i) do { if (T7 >= 0 && te == 7) { const int i_ = T7 & 7, o_ = (lane >> 1) * (S * 2) + (lane & 1) * 128, so_ = ((i) * 1024 + hw * 64) * (S * 2) + kstep0(kt, 2 * T7 & 15) * 128; \
      QTOUCH(o_, so_); QTOUCH(o_, so_ + 32 * S * 2); } } while (0)
    TCH(TM0);
    TCH7(0);
    LOADW(SWE, NWA1);
    if (NWA > NWA1) { JOIN(); LOADW(NWA1, NWA); }
    UNTIL(sched(T1_T, te) - GAD);
    GATHER();
    UNTIL(sched(T1_T, te));
    TCH(TM1);
    LOADW(NWA, NWC);
    wait_vm<NWC - NWA + __builtin_popcount(TM1) + __builtin_popcount(TM1 & DTM)>();
    TCH7(1);
#pragma unroll 1
    for (int spins = 0;; ++spins) {
      asm volatile("" : "+v"(gx[0]), "+v"(gx[1]), "+v"(gx[2]), "+v"(gx[3]), "+v"(gw[0]), "+v"(gw[1]), "+v"(gw[2]), "+v"(gw[3]) :: "memory");
      unsigned bad = 0;
#pragma unroll
      for (int q = 0; q < 4; ++q) bad |= (gx[q][3] ^ tag) | (gw[q][1] ^ tag);
      if (__builtin_expect(__all(bad == 0) || spins > SPIN_CAP / 64, 1)) break;
      GATHER();
      NEXT();
      POLLVM(0);
    }
    HST(3);
#define SUM4(v, c) ((__uint_as_float(v[0][c]) + __uint_as_float(v[1][c])) + (__uint_as_float(v[2][c]) + __uint_as_float(v[3][c])))
    float sum = SUM4(gx, 0), sq = SUM4(gx, 1), dot = SUM4(gx, 2);
    *(LAS float*)(lds + L_MB + 1024 + srow * 4) = SUM4(gw, 0);
    asm volatile("" : "+v"(sum), "+v"(sq), "+v"(dot));
    const float mn = sum * (1.0f / D);
    const float var = fmaxf(fmaf(-mn, mn, sq * (1.0f / D)), 0.f);
    const float mbv = rbf(mn), den = rbf(__builtin_amdgcn_sqrtf(rbf(rbf(var) + 1e-5f))), rden = __builtin_amdgcn_rcpf(den);
    *(LAS float*)(lds + L_MB + srow * 4) = -mbv;
    *(LAS float*)(lds + L_MB + 512 + srow * 4) = rden;
    *(LAS float*)(lds + L_MB + 1536 + srow * 4) = z ? -rden : rden;
    asm volatile("" ::: "memory");
    misc[4 + hw] = 1u;
    float rs = (dot - mbv * sw) * rden;
    sg = pack2(mbv, den);
    asm volatile("" : "+v"(rs), "+v"(sg));
    lim = NBAR;
    const unsigned rb = pack2(rs, rs) & 0xffffu;
    unsigned srt = (rb & 0x8000u) ? (~rb & 0xffffu) : (rb | 0x8000u);
    if (rs != rs) srt = 0u;
    key = (srt << 16) | (unsigned)(0xffff - tp1 * 128 - srow);
    const unsigned mk = wave_maxu(key);
    if (key == mk) __builtin_amdgcn_raw_buffer_store_b128(u32x4{key, tag, sg, tag}, rws, amo + hw * 16, 0, SC1);
    HST(2);
    UNTIL(WB_AT + (WBS >> 4 * te & 3));
    TCH(TMB);
    LOADW(NWC, 32);
  }
  unsigned sel[4];
#pragma unroll
  for (int d = 0; d < 4; ++d) {
    const unsigned src = (lc & 2) ? 0x0302u : 0x0100u;
    sel[d] = (lc >> 2) == d ? ((lc & 1) ? (src << 16 | 0x0c0cu) : (0x0c0c0000u | src)) : 0x0c0c0c0cu;
  }
  PIN4(sel[0], sel[1], sel[2], sel[3]);
  JOIN();
  const int hc = fg * 112 + hw * 56 + (lq * 7 + min(lc & 7, 6)) * 2;
  int guo = WS_GU + ((lc >> 3) * F + (hc & ~127)) * 8 + ((hc & 6) * 8 + ((hc >> 3) & 15)) * 16;
  int gao = WS_AMAX + tq1 * 2048 + ((lane >> 1) & 15) * 128 + (lane & 1) * 16, gso = b < 224 ? WS_GU + ((rg * 2 + hw) * F + cg * 128) * 8 + lane * 16 : -1;
  asm volatile("" : "+v"(guo), "+v"(gao), "+v"(gso));
  UNTIL(AM_AT);
#pragma unroll 1
  for (int spins = 0;; ++spins) {
    const u32x4 am = __builtin_amdgcn_raw_buffer_load_b128(rws, gao, 0, SC1);
    NEXT();
    UNTIL(AM_P);
#pragma unroll 1
    while (vm_now() > 0 && nb < lim && nb != 19 && nb != 17) JOIN();
    key = am[0]; sg = am[2];
    if (__builtin_expect(__all(((am[1] ^ tag) | (am[3] ^ tag)) == 0) || spins > SPIN_CAP / 64, 1)) break;
  }
  {
    const unsigned mk = wave_maxu(key);
    sg = __builtin_amdgcn_readlane(sg, __builtin_ctzll(__ballot(key == mk)));
    key = mk;
  }
  idx = min(0xffff - (int)(key & 0xffffu), S - 1);
  const unsigned xe = __builtin_amdgcn_raw_buffer_load_b32(rx, (idx * D + (rg & 7) * 128) * 2 + lane * 4, 0, 0);
  misc[0] = idx + 1;
  HST(7);
  if (nb != 19) NEXT();
#pragma unroll 1
  while (vm_now() > 0 && nb < lim && nb != 19 && nb != 18 + XEJ) JOIN();
  {
    const unsigned xs = ln_pair(xe, -lo2f(sg), __builtin_amdgcn_rcpf(hi2f(sg)));
    if (b == 0 && lane == 0 && hw == 1) __builtin_amdgcn_raw_buffer_store_b32(tag, rws, WS_EPOCH, 0, SC1);
    u32x4 dg[16];
    expand<0, GX1>(dg, xs, sel);
    if (nb != 19) NEXT();
    expand<GX1, 16>(dg, xs, sel);
    UNTIL(19);
    const bool idle = nb == 19;
    if (idle) { if (GV_SL) __builtin_amdgcn_s_sleep(GV_SL); } else NEXT();
    f32x4 ae = {0.f, 0.f, 0.f, 0.f}, ao = ae;
#pragma unroll
    for (int k = 0; k < 16; ++k) {
      ae = MFMA(BF8(dg[k]), BF8(w[2 * k]), ae); ao = MFMA(BF8(dg[k]), BF8(w[2 * k + 1]), ao);
      if (k == 7) { asm volatile("" : "+v"(ae), "+v"(ao)); if (__builtin_expect(!idle, 0)) NEXT(); }
    }
    __builtin_amdgcn_raw_buffer_store_b128(u32x4{__float_as_uint(ae[0] + ao[2]), tag, __float_as_uint(ae[1] + ao[3]), tag}, rws, gso, 0, SC1);
  }
  HST(4);
  if (nb == 19) JOIN();
  {
    const int m0_ = 18 + MB0 + (te & MBS);
#define MB(i) do { if ((MBT >> 8 * (i) & 255) || (MBW >> 4 * (i) & 15) != (MBW >> 4 * (i) + 4 & 15)) { UNTIL(m0_ + MBD * (i)); if (BSL2) __builtin_amdgcn_s_sleep(BSL2); \
      TCH(MBT >> 8 * (i) & 255); LOADD((int)(MBW >> 4 * (i) & 15), (int)(MBW >> 4 * (i) + 4 & 15)); } } while (0)
    MB(0); MB(1); MB(2); MB(3); MB(4); MB(5); MB(6); MB(7);
    if (!hw && lane < (CODE_LINES + 7) / 8) {
      const int co = min(((b >> 3) & 7) * ((CODE_LINES + 7) / 8) + lane, CODE_LINES - 1) * 128;
      asm volatile("global_load_dword %0, %1, off" : "+v"(tch) : "v"(code + co) : "memory");
    }
    UNTIL(m0_ + GGD);
  }
  {
    float s0, s1;
#pragma unroll 1
    for (int spins = 0;; ++spins) {
#pragma unroll
      for (int k = 0; k < 8; ++k) w[k] = __builtin_amdgcn_raw_buffer_load_b128(rws, guo, k * 2 * F * 8, SC1);
      NEXT();
      POLLVM(0);
      unsigned bad = 0; s0 = 0.f; s1 = 0.f;
#pragma unroll
      for (int k = 0; k < 8; ++k) { bad |= (w[k][1] ^ tag) | (w[k][3] ^ tag); s0 += __uint_as_float(w[k][0]); s1 += __uint_as_float(w[k][2]); }
      if (__builtin_expect(__all(bad == 0) || spins > SPIN_CAP / 64, 1)) break;
    }
    asm volatile("" : "+v"(s0), "+v"(s1));
    HST(8);
    NEXT();
    const unsigned gp = pack2(s0, s1);
    const unsigned up = (unsigned)__builtin_amdgcn_mov_dpp((int)gp, 0x128, 0xf, 0xf, true);
    const unsigned sl2 = pack2(silu_fast(lo2f(gp)), silu_fast(hi2f(gp)));
    const unsigned hp = pack2(lo2f(sl2) * lo2f(up), hi2f(sl2) * hi2f(up));
    f32x4 ae = {0.f, 0.f, 0.f, 0.f}, ao = ae;
    gemv<0, 2>(ae, ao, hp, sel, wd);
    asm volatile("" : "+v"(ae), "+v"(ao));
    NEXT();
    gemv<2, 7>(ae, ao, hp, sel, wd);
    f32x2 a = {ae[0] + ao[2], ae[1] + ao[3]};
    HST(5);
    if (hw == 1) {
      *(LAS f32x2*)(lds + L_CMB + lane * 8) = a;
      asm volatile("" ::: "memory");
      misc[6] = 1u;
    } else {
      f32x2 o;
#pragma unroll 1
      for (int spins = 0; spins < SPIN_CAP; ++spins) {
        NEXT();
        const unsigned f = misc[6];
        o = *(volatile LAS f32x2*)(lds + L_CMB + lane * 8);
        if (rfl(f)) break;
      }
      __builtin_amdgcn_raw_buffer_store_b128(u32x4{__float_as_uint(a[0] + o[0]), tag, __float_as_uint(a[1] + o[1]), tag}, rws,
                                             WS_PART + (fg * D + eg * 128) * 8 + lane * 16, 0, SC1);
      HST(6);
    }
  }
  if (hw == 1 && (idx >> 7) == tp1) {
    const unsigned srt = key >> 16;
    const float r = lo2f((srt & 0x8000u) ? (srt & 0x7fffu) : (~srt & 0xffffu));
    const float rsel = rbf(1.0f / (1.0f + __expf(-r)));
    const int cp = lane & 31;
    const int e = z * 64 + cp * 2;
    const int po = WS_PART + ((lane >> 5) * D + te * 128) * 8 + ((e & 6) * 8 + (e >> 3)) * 16;
    float y0, y1;
    UNTIL(FIN_AT);
    wait_sent((LAS char*)lds);
#pragma unroll 1
    for (int spins = 0;; ++spins) {
#pragma unroll
      for (int k = 0; k < 16; ++k) w[k] = __builtin_amdgcn_raw_buffer_load_b128(rws, po, k * 2 * D * 8, SC1);
      NEXT();
      POLLVM(0);
      unsigned bad = 0;
#pragma unroll
      for (int k = 0; k < 16; ++k) bad |= (w[k][1] ^ tag) | (w[k][3] ^ tag);
      if (__all(bad == 0) || spins > SPIN_CAP / 64) break;
    }
    NEXT();
    y0 = 0.f; y1 = 0.f;
#pragma unroll
    for (int k = 0; k < 16; ++k) { y0 += __uint_as_float(w[k][0]); y1 += __uint_as_float(w[k][2]); }
    asm volatile("" : "+v"(y0), "+v"(y1));
    NEXT();
    { const auto p0 = __builtin_amdgcn_permlane32_swap(__float_as_uint(y0), __float_as_uint(y0), false, false);
      const auto p1 = __builtin_amdgcn_permlane32_swap(__float_as_uint(y1), __float_as_uint(y1), false, false);
      y0 = __uint_as_float(p0[0]) + __uint_as_float(p0[1]); y1 = __uint_as_float(p1[0]) + __uint_as_float(p1[1]); }
    *(LAS f32x2*)(lds + L_UPD + cp * 8) = f32x2{rbf(rbf(y0) * rsel), rbf(rbf(y1) * rsel)};
    asm volatile("" ::: "memory");
    misc[1] = 1u;
    HST(6);
  }
  HST(10);
  POLLVM(0);
  asm volatile("" : "+v"(tch));
  STAMP_OUT();
}

typedef __attribute__((ext_vector_type(8))) u64 u64x8;
typedef __attribute__((ext_vector_type(2))) u64 u64x2;
__global__ __launch_bounds__(512, 2) void fused(const u16*, const u16*, const u16*, const u16*, char*, const u16*, const u16*, const u16*,
                                                const u16*, const u16*, u16*) {
  extern __shared__ __attribute__((aligned(16))) char lds[];
  u64x8 a0; u64x2 a1; u64 a2;
  u64 pc;
  asm volatile("s_load_dwordx16 %0, %4, 0\n\ts_load_dwordx4 %1, %4, 64\n\ts_load_dwordx2 %2, %4, 80\n\ts_getpc_b64 %3"
               : "=&s"(a0), "=&s"(a1), "=&s"(a2), "=&s"(pc) : "s"(__builtin_amdgcn_kernarg_segment_ptr()) : "memory");
  __builtin_amdgcn_sched_barrier(0);
  const char* code = (const char*)(pc & ~255ull);
  const int wid = __builtin_amdgcn_readfirstlane(threadIdx.x >> 6), lane = threadIdx.x & 63;
  const int b = blockIdx.x, g = b & 7, i = b >> 3;
  const int tp1 = (g & 3) * 4 + (i & 3), tq1 = (g >> 2) * 8 + (i >> 2);
  const int z = tq1 >> 3, te = tq1 & 7;
  const int L = wid & 1, rsub = lane >> 3;
  int v1 = ((L ? te : tp1) * 128 + 8 * (rsub >> 2) + (rsub & 3)) * (D * 2) + ((lane & 7) ^ rsub) * 16, ko = rot1(tp1, tq1) * 128;
  unsigned g1n = (L ? D : S) * D * 2;
  int role = wid >> 1;
  asm volatile("" : "+v"(v1));
  asm volatile("" : "+s"(ko), "+s"(g1n), "+s"(role));
  __builtin_amdgcn_sched_barrier(0);
  asm volatile("s_waitcnt lgkmcnt(0)" : "+s"(a0), "+s"(a1), "+s"(a2) :: "memory");
  const u16 *x = (const u16*)a0[0], *wr = (const u16*)a0[3], *sre = (const u16*)a0[5], *sim = (const u16*)a0[6], *wg = (const u16*)a0[7];
  const u16 *wu = (const u16*)a1[0], *wdn = (const u16*)a1[1];
  char* ws = (char*)a0[4];
  u16* out = (u16*)a2;
  const rsrc_t rws = mk_rsrc(ws, WS_END);
  if (__builtin_expect(role == 2, 1)) {
    loader(L, lane, (LAS char*)lds, mk_rsrc((const void*)(L ? (z ? a0[2] : a0[1]) : a0[0]), g1n), v1, ko, (z ? sim : sre) + (size_t)(tp1 & 7) * 128 * S,
           ws + WS_ABT + (size_t)tq1 * ABT_ST, rws, WS_FLAG1 + tq1 * 2048, b, tp1, tq1 STAMP_PASS);
  } else if (wid < 4) {
    consumer(wid, lane, b, lds, rws, mk_rsrc(out, S * D * 2), code, tp1, tq1 STAMP_PASS);
  } else {
    const int hw = wid - 6;
    helper(hw, lane, b, lds, rws, mk_rsrc(x, S * D * 2), wr, hw ? wu : wg, wdn,
           (z ? sim : sre) + (size_t)(tp1 & 7) * 128 * S, code, tp1, tq1 STAMP_PASS);
  }
}

extern "C" void kernel_launch(void* const* d_in, const int* in_sizes, int n_in, void* d_out, int out_size,
                              void* d_ws, size_t ws_size, hipStream_t stream) {
  (void)hipFuncSetAttribute((const void*)fused, hipFuncAttributeMaxDynamicSharedMemorySize, LDS_BYTES);
  fused<<<256, 512, LDS_BYTES, stream>>>((const u16*)d_in[0], (const u16*)d_in[3], (const u16*)d_in[4], (const u16*)d_in[6], (char*)d_ws,
                                         (const u16*)d_in[1], (const u16*)d_in[2], (const u16*)d_in[7], (const u16*)d_in[8],
                                         (const u16*)d_in[9], (u16*)d_out);
}
```

```cpp
#define CODE_LINES 277
#include <hip/hip_runtime.h>
#include <hip/hip_bf16.h>
#include <stdint.h>

typedef __attribute__((ext_vector_type(8))) short bf16x8;
typedef __attribute__((ext_vector_type(4))) float f32x4;
typedef __attribute__((ext_vector_type(2))) float f32x2;
typedef __attribute__((ext_vector_type(4))) unsigned u32x4;
typedef __attribute__((ext_vector_type(2))) unsigned u32x2;
typedef __attribute__((ext_vector_type(2))) __bf16 bf16x2;
typedef unsigned short u16;
typedef unsigned long long u64;
typedef __amdgpu_buffer_rsrc_t rsrc_t;
#define LAS __attribute__((address_space(3)))

constexpr int S = 2048, D = 1024, F = 3584;
constexpr int UB = 16384;
constexpr int SLOT = 3 * UB;
constexpr int L_MISC = 9 * UB;
constexpr int L_UPD = L_MISC + 64;
constexpr int L_MB = L_MISC + 512;
constexpr int L_CMB = L_MB + 2048;
constexpr int LDS_BYTES = L_CMB + 512;
constexpr int NBAR = 17 + 14 + 3;
constexpr int SC1 = 16, NT = 2;

constexpr unsigned WS_EPOCH = 0;
constexpr unsigned WS_XP = 4096;
constexpr unsigned WS_WP = WS_XP + 16 * 8192;
constexpr unsigned WS_AMAX = WS_WP + 16 * 4096;
constexpr unsigned WS_FLAG1 = WS_AMAX + 32768;
constexpr unsigned WS_FLAG2 = WS_FLAG1 + 65536;
constexpr unsigned WS_GU = WS_FLAG2 + 32768;
constexpr unsigned WS_H = WS_GU + 8 * 2 * F * 8;
constexpr unsigned WS_PART = WS_H + 16384;
constexpr unsigned WS_XCH = WS_PART + 32 * D * 8;
constexpr int XB = 49152;
constexpr unsigned ABT_ST = 128 * S * 2;
constexpr unsigned WS_ABT = WS_XCH + 256 * XB;
constexpr unsigned WS_END = WS_ABT + 16 * ABT_ST;

#define STAMP(i)
#define HSTAMP()
#define HPRE()
#define HST(i)
#define STAMP_DECL
#define STAMP_ARG
#define STAMP_PASS
#define STAMP_OUT()

__device__ __forceinline__ float lo2f(unsigned v) { return __uint_as_float(v << 16); }
__device__ __forceinline__ float hi2f(unsigned v) { return __uint_as_float(v & 0xffff0000u); }
__device__ __forceinline__ unsigned pack2(float a, float b) {
  return __builtin_bit_cast(unsigned, __builtin_convertvector(f32x2{a, b}, bf16x2));
}
__device__ __forceinline__ float rbf(float f) { return lo2f(pack2(f, f)); }
__device__ __forceinline__ unsigned rfl(unsigned v) { return __builtin_amdgcn_readfirstlane(v); }
__device__ __forceinline__ rsrc_t mk_rsrc(const void* p, unsigned bytes) {
  return __builtin_amdgcn_make_buffer_rsrc((void*)p, 0, bytes, 0x00020000);
}
#define DPP_ADD(v, ctrl, rm, bc) v += __builtin_bit_cast(float, __builtin_amdgcn_update_dpp(0, __builtin_bit_cast(int, v), ctrl, rm, 0xf, bc))
#define DPP_MAXU(v, ctrl, rm, bc) v = max(v, (unsigned)__builtin_amdgcn_update_dpp(0, (int)v, ctrl, rm, 0xf, bc))
__device__ __forceinline__ unsigned wave_maxu(unsigned v) {
  DPP_MAXU(v, 0x111, 0xf, true); DPP_MAXU(v, 0x112, 0xf, true); DPP_MAXU(v, 0x114, 0xf, true); DPP_MAXU(v, 0x118, 0xf, true);
  DPP_MAXU(v, 0x142, 0xa, false); DPP_MAXU(v, 0x143, 0xc, false);
  return (unsigned)__builtin_amdgcn_readlane((int)v, 63);
}
template <int N> __device__ __forceinline__ void wait_vm() { asm volatile("s_waitcnt vmcnt(%0)" :: "n"(N) : "memory"); }
#define WAIT_LGKM0() asm volatile("s_waitcnt lgkmcnt(0)" ::: "memory")
#define BAR() do { asm volatile("" ::: "memory"); __builtin_amdgcn_s_barrier(); asm volatile("" ::: "memory"); } while (0)
#define MFMA(a, b, c) __builtin_amdgcn_mfma_f32_16x16x32_bf16(a, b, c, 0, 0, 0)
#define BF8(v) __builtin_bit_cast(bf16x8, v)
constexpr int SPIN_CAP = 1 << 20;
#define UP_T 0x0507060508070605ull
#define T1_T 0x090a09090a0a0909ull
#define GAD 0
#define NWA1 16
#define NWA 16
#define NWC 30
#define TM0 0
#define TM1 0
#define TMB 3
#define WB_AT 12
#define AM_AT 15
#define GV_SL 0
#define MBS 1
#define BSL2 13
#define FIN_AT 34
#define EG0 1
#define EGD 1
#define PEARLY 0
#define T7 2
#define DTM 0
#define FBLK 1
#define WPOL NT
#define OPOL SC1
#define KM 6
#define KO 0x10101010u
#define KT0 0x33221100u
#define KT1 KT0
#define KT2 0x33122100u
#define KT3 KT2
#define PI 0x2e32363c
#define SWT 0x100d0a070400ull
#define SWE (int)(SWT >> 56 ? SWT >> 56 : SWT >> 48 ? SWT >> 48 : SWT >> 40)
#define Q2E 1
#define FA_ 1
#define FB_ 7
#define AM_P 17
#define GX1 7
#define BDEF 0
#define XEJ 1
#define QS 0
#define WBS 0x11001100
#define ZJ 5
#define EW 1
#define ZBLK 2
#define TWO 0
#define WD1 5
#define WD2 10
#define HB 6
#define DG1 5
#define DG2 7
#define HPR 0
#define TE0 0x18
#define TE1 0
#define TGV 0
#define TGS 0
__device__ __forceinline__ int sched(u64 tab, int te) { return (int)(tab >> (te * 8)) & 255; }
#define PIN4(a, b, c, d) asm volatile("" : "+v"(a), "+v"(b), "+v"(c), "+v"(d))
__device__ __forceinline__ unsigned vm_now() { const unsigned r = __builtin_amdgcn_s_getreg(7 | (31 << 11)); return (r & 15u) | ((r >> 18) & 0x30u); }
__device__ __forceinline__ unsigned mk_tag(unsigned epoch) { return rfl(epoch) * 0x9E3779B1u + 0x7F4A7C15u; }

template <int LD, int AUX, int MODE, int NP = 16, int J0 = 0>
__device__ __forceinline__ void issue(rsrc_t r, int voff, int koff, LAS char* dst) {
#pragma unroll
  for (int j = J0; j < NP; ++j) {
    const int so = MODE == 2 ? j * 1024 : MODE == 1 ? (32 * (j >> 3) + 64 * ((j >> 2) & 1) + 16 * (j & 1) + 4 * ((j >> 1) & 1)) * LD : j * 8 * LD;
    __builtin_amdgcn_raw_ptr_buffer_load_lds(r, (LAS void*)(dst + j * 1024), 16, voff, koff + so, 0, AUX);
  }
}
__device__ __forceinline__ unsigned ktab(int tp1) {
  const int j = tp1 & 3;
  return (tp1 * (0x11111111u ^ KO) | ((tp1 & 12) ^ 4) * KO) ^ (j == 0 ? KT0 : j == 1 ? KT1 : j == 2 ? KT2 : KT3);
}
__device__ __forceinline__ int rot1(int tp1, int tq1) { return KM == 4 ? 4 * (tp1 + tq1) & 15 : (8 * (tp1 + tq1) + 2 * (((tp1 >> 2) + 2 * (tq1 >> 3)) & 3)) & 15; }
__device__ __forceinline__ int kstep0(unsigned kt, int n) { return (int)(kt >> (n >> 1) * 4 & 15) * 2 + (n & 1); }
__device__ __forceinline__ int kstep(unsigned kt, int n) { return kstep0(kt, n == 3 ? 4 : n == 4 ? 3 : n); }
constexpr int QROWS = 1024 + 128;

__device__ __forceinline__ void wait_sent(LAS char* lds) {
#pragma unroll 1
  for (int spins = 0; spins < SPIN_CAP; ++spins) {
    const u32x4 f = *(volatile LAS u32x4*)(lds + L_MISC + 32);
    if (rfl(f[0] & f[1] & f[2] & f[3])) break;
    __builtin_amdgcn_s_sleep(1);
  }
  asm volatile("" ::: "memory");
}

__device__ __forceinline__ void loader(int L, int lane, LAS char* lds, rsrc_t r1, int v1, int ko, const u16* gq, const char* gp, rsrc_t rws,
                                       int flag_off, int b, int tp1, int tq1 STAMP_ARG) {
  const int te = tq1 & 7;
  STAMP_DECL;
  __builtin_amdgcn_s_setprio(2);
  const int rsub = lane >> 3, ch = ((lane & 7) ^ rsub) * 16;
  const rsrc_t rq = mk_rsrc(gq, QROWS * S * 2);
  const int h = tp1 >> 3;
  unsigned kt = ktab(tp1);
  int vq = rsub * (S * 2) + ch, vp = lane * 16;
  int qa = h * 1024 * (S * 2), qb = ((1 - h) * 1024 + L * 64) * (S * 2);
  int q_ = (L ? qa : qb) + tp1 * 256;
  LAS char* d_ = lds + (1 - L) * UB;
  volatile LAS unsigned* misc = (volatile LAS unsigned*)(lds + L_MISC);
  unsigned fl = 0, tag;
  {
    const unsigned utab = L ? 0x8541u : 0x7230u;
    STAMP(1);
    LAS char* dst = lds + L * UB;
#pragma unroll 1
    for (int t = 0; t < 4; ++t) {
      issue<D * 2, 0, 1>(r1, v1, ko, dst);
      ko = (ko + 128) & 2047;
      dst = lds + ((utab >> (t * 4 + 4)) & 15) * UB;
      if (t == 1 || t == 2) { wait_vm<16>(); BAR(); }
    }
    LAS char* dt[4];
#pragma unroll
    for (int u = 0; u < 4; ++u) dt[u] = lds + ((utab >> (u * 4)) & 15) * UB;
    asm volatile("" : "+s"(kt), "+s"(qa), "+s"(qb), "+s"(q_), "+s"(d_) : "s"(rq));
    asm volatile("" : "+v"(vq), "+v"(vp));
#pragma unroll 1
    for (int t = 0; t < 3; ++t) {
#pragma unroll
      for (int u = 0; u < 4; ++u) {
        issue<D * 2, 0, 1, 8>(r1, v1, ko, dt[u]);
        int kn = (ko + 128) & 2047;
        asm volatile("" : "+s"(kn), "+s"(dt[(u + 1) & 3]));
        if (u == 1) asm volatile("ds_read_b32 %0, %1" : "=v"(tag) : "v"(L_MISC + 8) : "memory");
        issue<D * 2, 0, 1, 16, 8>(r1, v1, ko, dt[u]);
        ko = kn;
        wait_vm<32>();
        BAR();
      }
    }
    asm volatile("s_waitcnt lgkmcnt(0)" : "+v"(tag) :: "memory");
  }
#define POLL(o) asm volatile("buffer_load_dword %0, %1, %2, 0 offen sc1" : "+v"(fl) : "v"(o), "s"(rws) : "memory")
#define POLLW(o) asm volatile("buffer_load_dword %0, %1, %2, 0 offen sc1\n\ts_waitcnt vmcnt(0)" : "+v"(fl) : "v"(o), "s"(rws) : "memory")
  {
    issue<S * 2, 0, 0>(rq, vq, q_, d_);
    const int q2 = qa + L * 64 * (S * 2) + kstep(kt, 2) * 128;
    wait_vm<32>();
    BAR();
    if (Q2E == 1) issue<S * 2, 0, 0, 8>(rq, vq, q2, lds + 2 * SLOT + L * 8192);
    wait_vm<Q2E == 1 ? 24 : 16>();
    BAR();
    issue<S * 2, 0, 0>(rq, vq, q_ + 128, d_ + SLOT);
    tag = rfl(tag);
    BAR();
    if (Q2E == 0) { issue<S * 2, 0, 0, 8>(rq, vq, q2, lds + 2 * SLOT + L * 8192); issue<S * 2, 0, 0, 8>(rq, vq, qb + kstep(kt, 2) * 128, lds + 2 * SLOT + UB + L * 8192); }
    wait_vm<Q2E == 0 ? 32 : 16>();
    BAR();
    if (Q2E == 2) issue<S * 2, 0, 0, 8>(rq, vq, q2, lds + 2 * SLOT + L * 8192);
    if (Q2E) issue<S * 2, 0, 0, 8>(rq, vq, qb + kstep(kt, 2) * 128, lds + 2 * SLOT + UB + L * 8192);
  }
  const int vx = (lane ^ (lane >> 2 & 12)) * 16;
  if (L) {
    int k_ = kstep(kt, 3) * 128, ka = qa + k_;
    LAS char* sl = lds;
    wait_vm<Q2E == 1 ? 8 : 16>();
    BAR();
    if (QS) __builtin_amdgcn_s_sleep(QS);
#pragma unroll 1
    for (int n = 4; n < 16; ++n) {
      issue<S * 2, 0, 0>(rq, vq, ka, sl);
      const int kb = qb + k_;
      LAS char* const sb = sl + UB + 8192;
      k_ = kstep(kt, n) * 128; ka = qa + k_;
      sl = sl == lds + 2 * SLOT ? lds : sl + SLOT;
      asm volatile("" : "+s"(k_), "+s"(ka), "+s"(sl));
      issue<S * 2, 0, 0, 8>(rq, vq, kb, sb);
      wait_vm<24>();
      BAR();
    }
    issue<S * 2, 0, 0>(rq, vq, qa + (k_ ^ 2048), lds);
    const int f2o = WS_FLAG2 + (b ^ 4) * 128 + 16 + (lane & 3) * 4;
    wait_vm<16>();
    BAR();
    wait_vm<0>();
    BAR();
    issue<S * 2, 0, 0, 8>(rq, vq, qa + 64 * (S * 2) + k_, lds + 4 * UB + 8192);
    wait_vm<0>();
    BAR();
    wait_sent(lds);
#pragma unroll 1
    for (int spins = 0; spins < SPIN_CAP; ++spins) {
      POLLW(f2o);
      if (__all(fl == tag)) break;
      __builtin_amdgcn_s_sleep(1);
    }
    STAMP(4);
    issue<0, SC1, 2>(rws, vx, WS_XCH + (b ^ 4) * XB + 32768, lds + 4 * UB);
    wait_vm<0>();
    misc[12] = 1u;
    STAMP(5);
  } else {
#define QHALF(n, sl) issue<S * 2, 0, 0, 8>(rq, vq, qb + kstep(kt, n) * 128, dqb + (sl))
    LAS char* const dqb = lds + UB;
    const rsrc_t rp = mk_rsrc(gp, 128 * S * 2);
    unsigned mask = 0;
    const int fo = flag_off + 32768 + (((lane >> 2) & 7) == (tp1 & 7) ? (kt >> 28) ^ 8 : h * 8 + ((lane >> 2) & 7)) * 128 + (lane & 3) * 4;
#define KM_(k) (15u << 2 * ((k) & 14))
    const int fo1 = flag_off + (kt >> (lane & 4 ? 8 : 4) & 15) * 128 + (lane & 3) * 4;
    unsigned m1 = 0;
    if (PEARLY) {
      POLLW(fo1);
      m1 = (unsigned)__ballot(fl == tag);
      if ((m1 & 15) == 15) issue<0, SC1, 2>(rp, vp, kstep(kt, 2) * UB, lds + 2 * SLOT + 2 * UB);
    } else wait_vm<Q2E == 1 ? 8 : 16>();
    BAR();
    STAMP(2);
    {
      const bool p2 = (m1 & 15) == 15;
      unsigned f1, f2;
      asm volatile("buffer_load_dword %0, %1, %2, 0 offen sc1" : "=&v"(f1) : "v"(fo1), "s"(rws) : "memory");
      QHALF(3, 0);
      asm volatile("buffer_load_dword %0, %2, %3, 0 offen sc1\n\ts_waitcnt vmcnt(9)" : "=&v"(f2), "+v"(f1) : "v"(fo1), "s"(rws) : "memory");
      m1 |= (unsigned)__ballot(f1 == tag);
      if (!p2) {
#pragma unroll 1
        for (int spins = 0; (m1 & 15) != 15 && spins < SPIN_CAP; ++spins) {
          asm volatile("s_sleep 2\n\tbuffer_load_dword %0, %2, %3, 0 offen sc1\n\ts_waitcnt vmcnt(1)" : "+v"(f1), "+v"(f2) : "v"(fo1), "s"(rws) : "memory");
          m1 |= (unsigned)__ballot(f2 == tag);
          if ((m1 & 15) == 15) break;
          asm volatile("s_sleep 2\n\tbuffer_load_dword %0, %2, %3, 0 offen sc1\n\ts_waitcnt vmcnt(1)" : "+v"(f2), "+v"(f1) : "v"(fo1), "s"(rws) : "memory");
          m1 |= (unsigned)__ballot(f1 == tag);
        }
        STAMP(3);
        issue<0, SC1, 2>(rp, vp, kstep(kt, 2) * UB, lds + 2 * SLOT + 2 * UB);
        asm volatile("s_waitcnt vmcnt(16)" : "+v"(f1), "+v"(f2) :: "memory");
      } else asm volatile("s_waitcnt vmcnt(0)" : "+v"(f1), "+v"(f2) :: "memory");
      m1 |= (unsigned)(__ballot(f1 == tag) | __ballot(f2 == tag));
      const bool p3 = (m1 & 0xf0) == 0xf0;
      if (!p3) {
        POLL(fo1);
        wait_vm<1>();
        BAR();
        asm volatile("s_waitcnt vmcnt(0)" : "+v"(fl) :: "memory");
#pragma unroll 1
        for (int spins = 0; ((unsigned)__ballot(fl == tag) & 0xf0) != 0xf0 && spins < SPIN_CAP; ++spins) POLLW(fo1);
      }
      POLL(fo);
      issue<0, SC1, 2>(rp, vp, kstep(kt, 3) * UB, lds + 2 * UB);
      if (p3) { wait_vm<17>(); BAR(); }
      asm volatile("s_waitcnt vmcnt(16)" : "+v"(fl) :: "memory");
    }
#define NEED(need) do { if (__builtin_expect(mask != ~0u, 0)) { asm volatile("" : "+v"(fl) :: "memory"); mask |= (unsigned)__ballot(fl == tag); \
      _Pragma("unroll 1") for (int spins = 0; (mask & (need)) != (need) && spins < SPIN_CAP; ++spins) { POLLW(fo); mask |= (unsigned)__ballot(fl == tag); } \
      if (mask != ~0u) POLL(fo); } } while (0)
    int k_ = kstep(kt, 4), kp = k_ * UB;
    LAS char* sl = lds + SLOT;
#pragma unroll 1
    for (int n = 5; n < 16; ++n) {
      NEED(KM_(k_));
      issue<0, SC1, 2>(rp, vp, kp, sl + 2 * UB);
      const int kb = qb + k_ * 128;
      LAS char* const sb = sl + UB;
      k_ = kstep0(kt, n); kp = k_ * UB;
      sl = sl == lds + 2 * SLOT ? lds : sl + SLOT;
      asm volatile("" : "+s"(k_), "+s"(kp), "+s"(sl));
      issue<S * 2, 0, 0, 8>(rq, vq, kb, sb);
      wait_vm<24>();
      BAR();
    }
    NEED(~0u);
    issue<0, SC1, 2>(rp, vp, (k_ ^ 16) * UB, lds + 2 * UB);
    wait_vm<16>();
    BAR();
    issue<0, SC1, 2>(rp, vp, k_ * UB, lds + UB);
    wait_vm<16>();
    BAR();
    issue<S * 2, 0, 0, 8>(rq, vq, qa + k_ * 128, lds + 4 * UB);
    const int f2o = WS_FLAG2 + (b ^ 2 ^ (lane & 4)) * 128 + (lane & 3) * 4;
    wait_vm<0>();
    BAR();
    if (EW) wait_sent(lds);
#pragma unroll 1
    for (int spins = 0; spins < SPIN_CAP; ++spins) {
      POLLW(f2o);
      if (__all(fl == tag)) break;
      __builtin_amdgcn_s_sleep(1);
    }
    issue<0, SC1, 2>(rws, vx, WS_XCH + (b ^ 2) * XB, lds + 6 * UB);
    issue<0, SC1, 2>(rws, vx, WS_XCH + (b ^ 6) * XB + 16384, lds + 7 * UB);
    STAMP(4);
    wait_vm<0>();
    misc[13] = 1u;
    STAMP(5);
  }
  STAMP_OUT();
}

#define LDF(off) (*reinterpret_cast<const bf16x8*>(lds + (off)))
struct Frag { bf16x8 p[4], q[4]; };
#define SGB(m, n) __builtin_amdgcn_sched_group_barrier(m, n, 0)
__device__ __forceinline__ float silu_fast(float z) { return z * __builtin_amdgcn_rcpf(1.0f + __expf(-z)); }
__device__ __forceinline__ u32x4 pack8(const f32x4& a, const f32x4& c) {
  return u32x4{pack2(a[0], a[1]), pack2(a[2], a[3]), pack2(c[0], c[1]), pack2(c[2], c[3])};
}
__device__ __forceinline__ unsigned ln_pair(unsigned x, float nmb, float rden) {
  const unsigned d = pack2(lo2f(x) + nmb, hi2f(x) + nmb);
  return pack2(lo2f(d) * rden, hi2f(d) * rden);
}

template <bool BARRIER, bool LAST, bool ZERO, class X>
__device__ __forceinline__ void big_half(f32x4 (&a0)[4][4], f32x4 (&a1)[4][4], const bf16x8 (&pc)[4], bf16x8 (&pn)[4], bf16x8 (&q)[8],
                                         const char* lds, int qc, int pa, int qn, int qn2, X extra) {
  const f32x4 zero = {0.f, 0.f, 0.f, 0.f};
#pragma unroll
  for (int g = 0; g < 8; ++g) {
    if (g == 0) { q[5] = LDF(qc + UB + 2048); q[6] = LDF(qc + UB + 4096); }
    if (g == 1) q[7] = LDF(qc + UB + 6144);
    if (g == 3) { pn[0] = LDF(pa); pn[1] = LDF(pa + 2048); }
    if (g == 4) { pn[2] = LDF(pa + 4096); pn[3] = LDF(pa + 6144); }
    if (g == 5) { q[0] = LDF(qn); q[1] = LDF(qn + 2048); }
    if (g == 6) { q[2] = LDF(qn + 4096); q[3] = LDF(qn + 6144); }
    if (g == 7 && !LAST) q[4] = LDF(qn2);
#pragma unroll
    for (int i = 0; i < 4; ++i) {
      const int ii = (g & 1) ? 3 - i : i;
      if (g < 4) a0[ii][g] = MFMA(pc[ii], q[g], ZERO ? zero : a0[ii][g]); else a1[ii][g - 4] = MFMA(pc[ii], q[g], ZERO ? zero : a1[ii][g - 4]);
    }
    SGB(0x8, 1); SGB(0x100, 1); SGB(0x8, 1); SGB(0x100, 1); SGB(0x8, 2);
    __builtin_amdgcn_sched_barrier(0);
    if (g == 2 && BARRIER) { WAIT_LGKM0(); BAR(); }
    extra(g);
  }
}

template <bool JOBNEXT>
__device__ __forceinline__ void small_step(f32x4 (&a)[4][4], bf16x8 (&p0)[4], bf16x8 (&p1)[4], bf16x8 (&q)[8], const char* lds,
                                           int ph, int qh, int pn, int qn, int qn2) {
#pragma unroll
  for (int g = 0; g < 8; ++g) {
    if (g == 0) { q[4] = LDF(qh); p1[0] = LDF(ph); }
    if (g == 1) { p1[1] = LDF(ph + 2048); p1[2] = LDF(ph + 4096); }
    if (g == 2) { p1[3] = LDF(ph + 6144); q[5] = LDF(qh + 2048); }
    if (g == 3) { q[6] = LDF(qh + 4096); q[7] = LDF(qh + 6144); }
    if (g == 4) { p0[0] = LDF(pn); q[0] = LDF(qn); }
    if (g == 5) { p0[1] = LDF(pn + 2048); p0[2] = LDF(pn + (JOBNEXT ? 1024 : 4096)); if (JOBNEXT) q[1] = LDF(qn + 2048); }
    if (g == 6) { p0[3] = LDF(pn + (JOBNEXT ? 3072 : 6144)); if (JOBNEXT) { q[2] = LDF(qn + 4096); q[3] = LDF(qn + 6144); } else q[1] = LDF(qn + 2048); }
    if (g == 7) { if (JOBNEXT) { q[4] = LDF(qn2); q[5] = LDF(qn2 + 2048); } else { q[2] = LDF(qn + 4096); q[3] = LDF(qn + 6144); } }
#pragma unroll
    for (int i = 0; i < 4; ++i) {
      const int ii = (g & 1) ? 3 - i : i;
      a[ii][g & 3] = MFMA(g < 4 ? p0[ii] : p1[ii], q[g], a[ii][g & 3]);
    }
    SGB(0x8, 1); SGB(0x100, 1); SGB(0x8, 1); SGB(0x100, 1); SGB(0x8, 1); SGB(0x100, 1); SGB(0x8, 1);
    __builtin_amdgcn_sched_barrier(0);
    if (g == 3) BAR();
  }
}

__device__ __forceinline__ void consumer(int wid, int lane, int b, char* lds, rsrc_t rws, rsrc_t rout,
                                         const char* code, int tp1, int tq1 STAMP_ARG) {
  STAMP_DECL;
  const int z = tq1 >> 3, te = tq1 & 7;
  const int wp = wid >> 1, wq = wid & 1, fr = lane & 15, fq = lane >> 4;
  const int c0 = (fq ^ (fr & 7)) * 16, c1 = ((fq + 4) ^ (fr & 7)) * 16;
  int q0 = (wq * 64 + fr) * 128 + c0, q1 = (wq * 64 + fr) * 128 + c1;
  volatile LAS unsigned* misc = (volatile LAS unsigned*)(lds + L_MISC);
  f32x4 acc[4][4];
#pragma unroll
  for (int i = 0; i < 4; ++i)
#pragma unroll
    for (int j = 0; j < 4; ++j) acc[i][j] = f32x4{0.f, 0.f, 0.f, 0.f};
  unsigned tag;
  asm volatile("" :: "s"(rout));
  u32x4 ob[4]; int bo[2];
  u32x4 xv[4];
  {
    Frag f0, f1;
    const int xrow = wq * 32 + (lane & 16) + 4 * (lane >> 5) + ((lane >> 2) & 3);
    const int xr = xrow * 128 + (((wp * 4 + (lane & 3)) ^ (xrow & 7)) * 16);
#define XV(u) do { _Pragma("unroll") for (int k = 0; k < 4; ++k) \
      xv[k] = *reinterpret_cast<const u32x4*>(lds + (u) * UB + xr + (k >> 1) * 8192 + (k & 1) * 1024); } while (0)
    const int p0 = (wp * 64 + fr) * 128 + c0, p1 = (wp * 64 + fr) * 128 + c1;
    unsigned tch = 0;
    if (wid == 3 && lane < (CODE_LINES + 7) / 8) {
      __builtin_amdgcn_s_sleep(8);
      const int co = min(((b >> 3) & 7) * ((CODE_LINES + 7) / 8) + lane, CODE_LINES - 1) * 128;
      asm volatile("global_load_dword %0, %1, off" : "+v"(tch) : "v"(code + co) : "memory");
    }
    const int xt = (2 * te + z - rot1(tp1, tq1)) & 15;
#define G1H(fc, fn, po, qo, EXTRA) do { _Pragma("unroll") for (int g = 0; g < 8; ++g) { \
      EXTRA(g); { if (g == 0) { fn.q[0] = LDF(qo); fn.p[0] = LDF(po); } else if (g == 1) { fn.p[1] = LDF((po) + 2048); fn.p[2] = LDF((po) + 4096); } \
        else if (g == 2) { fn.p[3] = LDF((po) + 6144); fn.q[1] = LDF((qo) + 2048); } else if (g == 3) { fn.q[2] = LDF((qo) + 4096); fn.q[3] = LDF((qo) + 6144); } } \
      _Pragma("unroll") for (int e = 0; e < 2; ++e) { const int i0 = (g & 1) * 2 + e, ii = (g & 2) ? 3 - i0 : i0; \
        acc[ii][g >> 1] = MFMA(fc.p[ii], fc.q[g >> 1], acc[ii][g >> 1]); } \
      SGB(0x8, 1); SGB(0x100, 1); SGB(0x8, 1); SGB(0x100, 2); __builtin_amdgcn_sched_barrier(0); } } while (0)
#define X_NO(g)
#define XI(u) ((u) == 0 ? 0 : (u) == 1 ? 3 : (u) == 2 ? 2 : 7)
#define WI(u) ((u) == 0 ? 1 : (u) == 1 ? 4 : (u) == 2 ? 5 : 8)
#define XVC(t, u) do { if (__builtin_expect((t) == xt, 0)) { XV(XI(u)); PIN4(xv[0], xv[1], xv[2], xv[3]); } __builtin_amdgcn_sched_barrier(0); } while (0)
    int pA[2] = {p0, p1}, pB[2] = {p0 + 7 * UB, p1 + 7 * UB}, qA[2] = {q0 + UB, q1 + UB}, qB[2] = {q0 + 5 * UB, q1 + 5 * UB};
    PIN4(pA[0], pA[1], pB[0], pB[1]); PIN4(qA[0], qA[1], qB[0], qB[1]);
#define XA(u, h) ((u) == 3 ? pB[h] : pA[h] + XI(u) * UB)
#define WA(u, h) ((u) < 2 ? qA[h] + (u) * 3 * UB : qB[h] + ((u) - 2) * 3 * UB)
    int ta = L_MB + (wp * 32 + fq * 8) * 4, tc = L_MB + 1024 + (wq * 32 + 8 * (fr >> 2) + (fr & 3)) * 4;
    const int flip = z ? 0 : 4096;
    int ao[2], ow[2];
#pragma unroll
    for (int h = 0; h < 2; ++h) {
      ao[h] = WS_ABT + tq1 * ABT_ST + tp1 * 2 * UB + wq * 8192 + wp * 1024 + lane * 16 + (h * 4096 ^ flip);
      ow[h] = 2 * UB + wq * 8192 + wp * 1024 + lane * 16 + (h * 4096 ^ flip);
    }
    PIN4(ao[0], ao[1], ow[0], ow[1]);
    asm volatile("" : "+v"(ta), "+v"(tc));
    BAR();
    XVC(0, 0);
    f0.q[0] = LDF(WA(0, 0));
#pragma unroll
    for (int i = 0; i < 4; ++i) f0.p[i] = LDF(XA(0, 0) + i * 2048);
#pragma unroll
    for (int i = 1; i < 4; ++i) f0.q[i] = LDF(WA(0, 0) + i * 2048);
    __builtin_amdgcn_sched_barrier(0);
#pragma unroll 1
    for (int t = 0; t < 12; t += 4) {
#pragma unroll
      for (int u = 0; u < 4; ++u) {
        G1H(f0, f1, XA(u, 1), WA(u, 1), X_NO);
        WAIT_LGKM0();
        BAR();
        XVC(t + u + 1, (u + 1) & 3);
        G1H(f1, f0, XA((u + 1) & 3, 0), WA((u + 1) & 3, 0), X_NO);
      }
    }
    u32x2 rdy;
#define X_RDY(g) if ((g) == 4) rdy = *(const volatile LAS u32x2*)(lds + L_MISC + 16)
    G1H(f0, f1, XA(0, 1), WA(0, 1), X_RDY);
    asm volatile("s_waitcnt vmcnt(0) lgkmcnt(0)" : "+v"(tch) :: "memory");
    BAR();
    XVC(13, 1);
    if (__builtin_expect(!rfl(rdy[0] & rdy[1]), 0)) {
#pragma unroll 1
      for (int spins = 0; spins < SPIN_CAP && !rfl(misc[4] & misc[5]); ++spins) __builtin_amdgcn_s_sleep(1);
    }
    asm volatile("" ::: "memory");
    f32x4 mb[2][2], rd[2][2]; float cs[4];
    unsigned tagv;
#define TB(o) (*reinterpret_cast<const f32x4*>(lds + ta + (o)))
#define X_T1(g) if ((g) >= 4) cs[(g) & 3] = *reinterpret_cast<const float*>(lds + tc + (64 * (((g) & 3) >> 1) + 4 * ((g) & 1)) * 4)
#define X_T2(g) if ((g) == 4) { mb[1][0] = TB(256); mb[1][1] = TB(272); } else if ((g) == 5) tagv = *(const LAS unsigned*)(lds + L_MISC + 8)
    G1H(f1, f0, XA(1, 0), WA(1, 0), X_T1);
    G1H(f0, f1, XA(1, 1), WA(1, 1), X_T2);
    WAIT_LGKM0();
    BAR();
    XVC(14, 2);
    Frag F[4];
#define SB() __builtin_amdgcn_sched_barrier(0)
#define FQ(k, i) F[k].q[i] = LDF(WA(2 + ((k) >> 1), (k) & 1) + (i) * 2048)
#define FP(k, i) F[k].p[i] = LDF(XA(2 + ((k) >> 1), (k) & 1) + (i) * 2048)
#define FA(k, r) do { if ((r) == 0) FQ(k, 0); else if ((r) < 3) FP(k, (r) - 1); else FQ(k, (r) - 2); } while (0)
#pragma unroll
    for (int n = 0; n < 16; ++n) {
      const int j = n >> 2, ii = (j & 1) ? 3 - (n & 3) : n & 3;
      acc[ii][j] = MFMA(f1.p[ii], f1.q[j], acc[ii][j]);
      SB();
      if ((n & 3) != 3) { const int m = n - (n >> 2); FA(m / 6, m % 6); }
      SB();
    }
    BAR();
    XVC(15, 3);
#define VFMA(d, a, c) asm volatile("v_fma_f32 %0, %1, %2, %0" : "+v"(d) : "v"(a), "v"(c))
#define VMUL(d, a) asm volatile("v_mul_f32 %0, %1, %0" : "+v"(d) : "v"(a))
    float cb[2][4][4];
#pragma unroll
    for (int n = 0; n < 32; ++n) {
      const int k = n >> 3, j = (n >> 1) & 3, ii = (n ^ n >> 1) & 1;
      acc[ii][j] = MFMA(F[k].p[ii], F[k].q[j], acc[ii][j]);
      SB();
      if ((n & 3) != 3) { const int m = n - (n >> 2); if (m < 12) FA(2 + m / 6, m % 6); else if (m < 20) FP((m - 12) >> 1, 2 + (m & 1)); }
      if (n == 19) mb[0][0] = TB(0); if (n == 23) mb[0][1] = TB(16); if (n == 26) rd[0][0] = TB(1536); if (n == 27) rd[0][1] = TB(1552);
      {
        const int jj = n >> 3, h = (n >> 2) & 1, r = n & 3;
        cb[h][jj][r] = acc[2 + h][jj][r];
        VFMA(cb[h][jj][r], mb[1][h][r], cs[jj]);
        acc[2 + h][jj][r] = cb[h][jj][r];
      }
      SB();
    }
    WAIT_LGKM0();
    BAR();
    tag = rfl(tagv);
    u32x4 o;
#pragma unroll
    for (int n = 0; n < 32; ++n) {
      const int k = n >> 3, j = (n >> 1) & 3, ii = 2 + ((n ^ n >> 1) & 1);
      acc[ii][j] = MFMA(F[k].p[ii], F[k].q[j], acc[ii][j]);
      SB();
      {
        const int h = (n >> 2) & 1, r = n & 3;
        cb[h][k][r] = acc[h][k][r];
        VFMA(cb[h][k][r], mb[0][h][r], cs[k]);
        if (n) { const int n1 = n - 1; VMUL(cb[(n1 >> 2) & 1][n1 >> 3][n1 & 3], rd[0][(n1 >> 2) & 1][n1 & 3]); }
        if (n > 2 && (n & 1)) { const int n2 = n - 3;
          unsigned o_ = pack2(cb[(n2 >> 2) & 1][n2 >> 3][n2 & 3], cb[(n2 >> 2) & 1][n2 >> 3][(n2 & 3) + 1]); asm volatile("" : "+v"(o_)); o[(n2 >> 1) & 3] = o_; }
        if (n == 20) rd[1][0] = TB(1792); if (n == 22) rd[1][1] = TB(1808);
        if (n > 8 && (n & 7) == 2) {
          __builtin_amdgcn_raw_buffer_store_b128(o, rws, ao[(k - 1) >> 1] + ((k - 1) & 1) * 2048, 0, SC1);
          *reinterpret_cast<u32x4*>(lds + ow[(k - 1) >> 1] + ((k - 1) & 1) * 2048) = o;
        }
      }
      SB();
    }
    STAMP(2);
    VMUL(cb[1][3][3], rd[0][1][3]);
    o[3] = pack2(cb[1][3][2], cb[1][3][3]);
    __builtin_amdgcn_raw_buffer_store_b128(o, rws, ao[1] + 2048, 0, SC1);
    *reinterpret_cast<u32x4*>(lds + ow[1] + 2048) = o;
#pragma unroll
    for (int j = 0; j < 4; ++j) {
      ob[j] = pack8(acc[2][j] * rd[1][0], acc[3][j] * rd[1][1]);
      if (!BDEF) __builtin_amdgcn_raw_buffer_store_b128(ob[j], rws, ao[j >> 1] + UB + (j & 1) * 2048, 0, SC1);
      *reinterpret_cast<u32x4*>(lds + ow[j >> 1] + SLOT + (j & 1) * 2048) = ob[j];
    }
    bo[0] = ao[0] + UB; bo[1] = ao[1] + UB;
    STAMP(3);
  }
  f32x4 acb[4][4];
  bf16x8 pa[4], pb[4], q[8];
  int pp = wp * 8192 + lane * 16;
  const int f1o = WS_FLAG1 + (tq1 * 16 + tp1) * 128 + wid * 4;
#define FLAG1(i, n) do { wait_vm<n>(); __builtin_amdgcn_raw_buffer_store_b32(tag, rws, lane ? -1 : f1o + (i) * 32768, 0, SC1); if (i) STAMP(4); __builtin_amdgcn_sched_barrier(0); } while (0)
  auto fl0 = [&](int g) {
    if (g == FA_) FLAG1(0, BDEF ? 0 : 4);
    if (BDEF && g >= FA_ && g < FA_ + 2) {
      __builtin_amdgcn_raw_buffer_store_b128(ob[2 * (g - FA_) & 3], rws, bo[(g - FA_) & 1], 0, SC1);
      __builtin_amdgcn_raw_buffer_store_b128(ob[2 * (g - FA_) + 1 & 3], rws, bo[(g - FA_) & 1] + 2048, 0, SC1);
      __builtin_amdgcn_sched_barrier(0);
    }
    if (g == FB_) FLAG1(1, BDEF ? 0 : 1); };
  WAIT_LGKM0();
  BAR();
  STAMP(5);
  pa[0] = LDF(2 * UB + pp); q[0] = LDF(q0); pa[1] = LDF(2 * UB + pp + 2048); pa[2] = LDF(2 * UB + pp + 4096); pa[3] = LDF(2 * UB + pp + 6144);
  q[1] = LDF(q0 + 2048); q[2] = LDF(q0 + 4096); q[3] = LDF(q0 + 6144); q[4] = LDF(q0 + UB);
  __builtin_amdgcn_sched_barrier(0);
  auto none = [](int) {};
  big_half<false, false, true>(acc, acb, pa, pb, q, lds, q0, 2 * UB + pp + 1024, q1, q1 + UB, fl0);
  PIN4(q0, q1, pp, lane);
  {
    int sl = 0;
#pragma unroll 1
    for (int n = 0; n < 12; ++n) {
      const int sn = sl == 2 * SLOT ? 0 : sl + SLOT;
      auto fl1 = [&](int g) { if (g + 8 == FB_ && __builtin_expect(n == 0, 0)) FLAG1(1, BDEF ? 0 : 1); };
      auto fl2 = [&](int g) { if (g + 16 == FB_ && __builtin_expect(n == 0, 0)) FLAG1(1, BDEF ? 0 : 1); };
      big_half<true, false, false>(acc, acb, pb, pa, q, lds, sl + q1, sn + 2 * UB + pp, sn + q0, sn + q0 + UB, fl1);
      big_half<false, false, false>(acc, acb, pa, pb, q, lds, sn + q0, sn + 2 * UB + pp + 1024, sn + q1, sn + q1 + UB, fl2);
      sl = sn;
    }
  }
  int q0h = q0 + 4 * UB, q1h = q1 + 4 * UB, pph = pp + 5 * UB;
  asm volatile("" : "+v"(q0h), "+v"(q1h), "+v"(pph));
  big_half<true, true, false>(acc, acb, pb, pa, q, lds, q1, pph, q0h, 0, none);
  small_step<false>(acb, pa, pb, q, lds, pph + 1024, q1h, pph + 3 * UB, q0h + 3 * UB, 0);
  small_step<true>(acb, pa, pb, q, lds, pph + 3 * UB + 1024, q1h + 3 * UB, pph, 3 * UB + q0, 3 * UB + q1);
  int xw = WS_XCH + b * XB + wid * 4096 + lane * 16;
  asm volatile("" : "+v"(xw));
#define SEND_E(j) __builtin_amdgcn_raw_buffer_store_b128(pack8(acb[(j) < 4 ? 2 : 0][(j) & 3], acb[(j) < 4 ? 3 : 1][(j) & 3]), rws, xw + ((j) >> 2) * 16384 + ((j) & 3) * 1024, 0, SC1)
  STAMP(10);
  const int f2o = WS_FLAG2 + b * 128 + wid * 4;
#define PA(k) ((k) == 0 ? pph : (k) == 1 ? pph + 3 * UB : (k) == 2 ? pp + 2 * UB : pp + UB)
#define QA0(k) ((k) == 0 ? q0 + 3 * UB : (k) == 1 ? q0h + 2 * UB : (k) == 2 ? q0 : q0h)
#define QA1(k) ((k) == 0 ? q1 + 3 * UB : (k) == 1 ? q1h + 2 * UB : (k) == 2 ? q1 : q1h)
#define JOB(K, I0, pc, qc, LC, pn, KN, IN, qx, LN, EXTRA) do { _Pragma("unroll") for (int g = 0; g < 8; ++g) { \
    if (g < 2 && (LC)) qc[g + 6] = LDF(QA1(K) + (g + 2) * 2048); \
    if ((KN) >= 0) { \
      if (g >= 2 && (LN)) qx[g - 2] = LDF(g < 6 ? QA0(KN) + (g - 2) * 2048 : QA1(KN) + (g - 6) * 2048); \
      if (g < 4) pn[g] = LDF(PA(KN) + ((IN) + (g & 1)) * 2048 + (g >> 1) * 1024); } \
    acc[I0][g & 3] = MFMA(pc[(g >> 2) * 2], qc[g], acc[I0][g & 3]); \
    acc[(I0) + 1][g & 3] = MFMA(pc[(g >> 2) * 2 + 1], qc[g], acc[(I0) + 1][g & 3]); \
    EXTRA(g); \
    SGB(0x8, 1); SGB(0x100, 1); SGB(0x2, 2); SGB(0x8, 1); SGB(0x100, 1); SGB(0x2, 2); \
    __builtin_amdgcn_sched_barrier(0); } \
    if ((KN) < 0) { PIN4(acc[2][0], acc[2][1], acc[2][2], acc[2][3]); PIN4(acc[3][0], acc[3][1], acc[3][2], acc[3][3]); } } while (0)
#define X_NONE(g)
#define X_E(n, g) if ((n) * 8 + (g) >= EG0 && ((n) * 8 + (g) - EG0) % EGD == 0 && ((n) * 8 + (g) - EG0) / EGD < 8) SEND_E(((n) * 8 + (g) - EG0) / EGD & 7)
#define X_E0(g) X_E(0, g)
#define X_E1(g) X_E(1, g)
  bf16x8 qb[8], qc_[8];
  JOB(0, 0, pa, q, 1, pb, 1, 0, q, 1, X_E0);
  JOB(1, 0, pb, q, 1, pa, 2, 0, qb, 1, X_E1);
  BAR();
  JOB(2, 0, pa, qb, 1, pb, 3, 0, qc_, 1, X_NONE);
#define FL(i, n) do { if ((i) == FBLK) { wait_vm<n>(); __builtin_amdgcn_raw_buffer_store_b32(tag, rws, lane ? -1 : f2o, 0, SC1); STAMP(9); } } while (0)
#define X_Z(n, g) if (ZJ == (n)) { if ((g) >= 1 && (g) < 5) __builtin_amdgcn_raw_buffer_store_b128(pack8(acc[0][((g) - 1) & 3], acc[1][((g) - 1) & 3]), rws, xw + 32768 + (((g) - 1) & 3) * 1024, 0, SC1); \
    else if ((g) == 5) misc[8 + wid] = 1u; }
#define FZ(i) do { if ((i) == ZBLK) { wait_vm<0>(); __builtin_amdgcn_raw_buffer_store_b32(tag, rws, lane ? -1 : f2o + 16, 0, SC1); STAMP(1); } } while (0)
#define X_Z5(g) X_Z(5, g)
#define X_Z6(g) X_Z(6, g)
#define X_Z7(g) X_Z(7, g)
  FL(0, 0);
  JOB(3, 0, pb, qc_, 1, pa, 3, 2, q, 0, X_NONE);
  FL(1, 0);
  JOB(3, 2, pa, qc_, 0, pb, 2, 2, qb, 0, X_Z5);
  FL(2, (ZJ > 5 ? 0 : 4));
  FZ(0);
  JOB(2, 2, pb, qb, 0, pa, 1, 2, q, 0, X_Z6);
  FZ(1);
  JOB(1, 2, pa, q, 0, pb, 0, 2, qb, 1, X_Z7);
  FZ(2);
  float tm[4], tr[4]; unsigned selv;
  const char* tb = lds + L_MB + (wq * 64 + (lane >> 2)) * 4;
#define X_TAB(g) if ((g) >= 2 && (g) < 6) { tm[((g) - 2) & 3] = *reinterpret_cast<const float*>(tb + ((g) - 2) * 64); tr[((g) - 2) & 3] = *reinterpret_cast<const float*>(tb + 512 + ((g) - 2) * 64); } \
  else if ((g) == 6) selv = misc[0]; \
  X_Z(8, g)
  JOB(0, 2, pb, qb, 1, pa, -1, 0, q, 0, X_TAB);
  STAMP(6);
  FZ(3);
  wait_sent((LAS char*)lds);
  {
    char* tw = lds + wid * 8192 + (lane & 15) * 64 + (((lane >> 4) ^ (lane >> 1)) & 3) * 16;
    const char* tr_ = lds + wid * 8192 + (lane >> 2) * 64 + ((lane ^ (lane >> 3)) & 3) * 16;
#pragma unroll
    for (int k = 0; k < 8; ++k) *reinterpret_cast<f32x4*>(tw + k * 1024) = acc[2 + (k >> 2)][k & 3];
    asm volatile("" ::: "memory");
#pragma unroll
    for (int k = 0; k < 8; ++k) acc[2 + (k >> 2)][k & 3] = *reinterpret_cast<const f32x4*>(tr_ + k * 1024);
    __builtin_amdgcn_sched_barrier(0);
  }
  PIN4(xv[0], xv[1], xv[2], xv[3]);
  f32x2 gf[4][4];
#pragma unroll
  for (int k = 0; k < 4; ++k) {
    const float tn = tm[k] * tr[k];
#pragma unroll
    for (int c = 0; c < 4; ++c) {
      const f32x2 n = f32x2{lo2f(xv[k][c]), hi2f(xv[k][c])} * tr[k] + tn, a = n * -1.44269504f;
      const f32x2 d = f32x2{__builtin_amdgcn_exp2f(a[0]), __builtin_amdgcn_exp2f(a[1])} + 1.0f;
      gf[k][c] = n * f32x2{__builtin_amdgcn_rcpf(d[0]), __builtin_amdgcn_rcpf(d[1])};
    }
    PIN4(gf[k][0], gf[k][1], gf[k][2], gf[k][3]);
    FZ(4 + k);
  }
  f32x2 xf[4][4];
#pragma unroll
  for (int k = 0; k < 4; ++k) {
#pragma unroll
    for (int c = 0; c < 4; ++c) xf[k][c] = f32x2{lo2f(xv[k][c]), hi2f(xv[k][c])};
    PIN4(xf[k][0], xf[k][1], xf[k][2], xf[k][3]);
  }
  {
    unsigned sel = rfl(selv);
    if (__builtin_expect(!sel || (int)(sel - 1 >> 6) == tp1 * 2 + wq, 0)) {
#pragma unroll 1
      for (int spins = 0; spins < SPIN_CAP && !sel; ++spins) { sel = rfl(misc[0]); __builtin_amdgcn_s_sleep(2); }
      const int idx = (int)sel - 1;
      if ((idx >> 6) == tp1 * 2 + wq) {
        const int ks = (idx >> 4) & 3;
        const u32x4 xs = ks == 0 ? xv[0] : ks == 1 ? xv[1] : ks == 2 ? xv[2] : xv[3];
        const float ms = ks == 0 ? tm[0] : ks == 1 ? tm[1] : ks == 2 ? tm[2] : tm[3], rs = ks == 0 ? tr[0] : ks == 1 ? tr[1] : ks == 2 ? tr[2] : tr[3];
        u32x4 n;
#pragma unroll
        for (int c = 0; c < 4; ++c) n[c] = ln_pair(xs[c], ms, rs);
        asm volatile("" : "+v"(n));
#pragma unroll 1
        for (int spins = 0; spins < SPIN_CAP && !rfl(misc[1]); ++spins) __builtin_amdgcn_s_sleep(1);
        asm volatile("" ::: "memory");
        const LAS float* upd = (const LAS float*)(lds + L_UPD) + wp * 32 + (lane & 3) * 8;
#pragma unroll
        for (int c = 0; c < 4; ++c) {
          const unsigned u = pack2(lo2f(n[c]) + upd[2 * c], hi2f(n[c]) + upd[2 * c + 1]);
          const f32x2 gn = {silu_fast(lo2f(u)), silu_fast(hi2f(u))};
#pragma unroll
          for (int k = 0; k < 4; ++k) if (ks == k && (idx & 15) == (lane >> 2)) gf[k][c] = gn;
        }
      }
    }
  }
  int xo = ((tp1 * 128 + wq * 64 + (lane >> 2)) * D + te * 128 + z * 64 + wp * 32 + (lane & 3) * 8) * 2, le = wid * 4096 + (lane & 3) * 256 + ((lane >> 2) ^ (lane & 3) * 4) * 16 + 4 * UB;
  const unsigned one = (lane >> 4) == ((lane >> 2) & 3) ? (lane & 1 ? 0x3f800000u : 0x3f80u) : 0u;
  u32x4 s0 = {lane & 2 ? 0u : one, lane & 2 ? one : 0u, 0u, 0u}, s1 = {0u, 0u, s0[0], s0[1]};
  PIN4(xo, le, s0, s1);
  STAMP(7);
#define PIECES(i0, i1) _Pragma("unroll") for (int k = 0; k < 4; ++k) _Pragma("unroll") for (int i = i0; i < (i1); ++i) \
    pc[i][k] = LDF(le + (i ? i + 1 : 0) * UB + k * 1024)
#define ADDP(k, i0, i1) _Pragma("unroll") for (int i = i0; i < (i1); ++i) { acc[2][k] = MFMA(BF8(s0), pc[i][k], acc[2][k]); acc[3][k] = MFMA(BF8(s1), pc[i][k], acc[3][k]); }
  bf16x8 pc[3][4];
#pragma unroll 1
  for (int spins = 0; spins < SPIN_CAP; ++spins) { const u32x2 f = *(volatile LAS u32x2*)(lds + L_MISC + 48); if (rfl(f[0] & f[1])) break; }
  asm volatile("" ::: "memory");
  STAMP(11);
  PIECES(0, TWO ? 1 : 3);
  __builtin_amdgcn_sched_barrier(0);
#pragma unroll
  for (int k = 0; k < 4; ++k) ADDP(k, 0, TWO ? 1 : 3);
#pragma unroll
  for (int k = 0; k < 4; ++k) {
    u32x4 ov;
#pragma unroll
    for (int c = 0; c < 4; ++c) {
      const f32x2 r = f32x2{acc[2 + (c >> 1)][k][(c & 1) * 2], acc[2 + (c >> 1)][k][(c & 1) * 2 + 1]} * gf[k][c] + xf[k][c];
      ov[c] = pack2(r[0], r[1]);
    }
    __builtin_amdgcn_raw_buffer_store_b128(ov, rout, xo + k * 16 * D * 2, 0, OPOL);
  }
  STAMP(8);
  STAMP_OUT();
}

#define BCAST(v, k) ((unsigned)__builtin_amdgcn_mov_dpp((int)(v), 0x150 + (k), 0xf, 0xf, true))
template <int K0, int K1>
__device__ __forceinline__ void expand(u32x4 (&dg)[16], unsigned xs, const unsigned (&sel)[4]) {
  if constexpr (K0 < K1) {
    const unsigned v = BCAST(xs, K0);
    dg[K0] = u32x4{__builtin_amdgcn_perm(0u, v, sel[0]), __builtin_amdgcn_perm(0u, v, sel[1]), __builtin_amdgcn_perm(0u, v, sel[2]), __builtin_amdgcn_perm(0u, v, sel[3])};
    asm volatile("" : "+v"(dg[K0]));
    expand<K0 + 1, K1>(dg, xs, sel);
  }
}
template <int K0, int K1, int N>
__device__ __forceinline__ void gemv(f32x4& ae, f32x4& ao, unsigned xs, const unsigned (&sel)[4], const u32x4 (&w)[N]) {
  if constexpr (K0 < K1) {
    const unsigned v = BCAST(xs, K0);
    const bf16x8 dg = BF8((u32x4{__builtin_amdgcn_perm(0u, v, sel[0]), __builtin_amdgcn_perm(0u, v, sel[1]),
                                 __builtin_amdgcn_perm(0u, v, sel[2]), __builtin_amdgcn_perm(0u, v, sel[3])}));
    ae = MFMA(dg, BF8(w[2 * K0]), ae); ao = MFMA(dg, BF8(w[2 * K0 + 1]), ao);
    gemv<K0 + 1, K1, N>(ae, ao, xs, sel, w);
  }
}

__device__ __forceinline__ void helper(int hw, int lane, int b, char* lds, rsrc_t rws, rsrc_t rx, const u16* wr,
                                       const u16* wgu, const u16* wdn, const u16* gq, const char* code, int tp1, int tq1 STAMP_ARG) {
  STAMP_DECL;
  int nb = 0, lim = 14;
  unsigned tch = 0;
  volatile LAS unsigned* misc = (volatile LAS unsigned*)(lds + L_MISC);
  __builtin_amdgcn_s_setprio(3);
  const int z = tq1 >> 3, te = tq1 & 7;
  const int lc = lane & 15, lq = lane >> 4;
  const int r1 = rot1(tp1, tq1), kb = r1 >> 2, amo = WS_AMAX + (tq1 * 16 + tp1) * 128;
#define JOIN() do { HPRE(); BAR(); HSTAMP(); ++nb; } while (0)
#define NEXT() do { if (nb < lim) JOIN(); } while (0)
#define UNTIL(m) do { _Pragma("unroll 1") while (nb < (m)) JOIN(); } while (0)
#define POLLVM(m) do { _Pragma("unroll 1") while (vm_now() > (m) && nb < lim) JOIN(); wait_vm<m>(); } while (0)
  unsigned tag, key, sg; int idx;
  float sw;
  const int rg = b / 28, cg = b - rg * 28;
  const int fg = b >> 3, eg = b & 7;
  u32x4 w[32], wd[14];
  const rsrc_t rgu = mk_rsrc(wgu + (size_t)(rg & 7) * 128 * F + cg * 128, b < 224 ? 128 * F * 2 : 0);
  const rsrc_t rd = mk_rsrc(wdn + (size_t)(fg * 112 + hw * 56) * D + eg * 128, 56 * D * 2);
  const int wvo = lq * 32 * F * 2 + lc * 16, dvo = lq * 14 * D * 2 + lc * 16;
#define LOADW(k0, k1) do { _Pragma("unroll") for (int k = k0; k < (k1); ++k) \
    w[k] = __builtin_amdgcn_raw_buffer_load_b128(rgu, wvo, k * F * 2, WPOL); asm volatile("" ::: "memory"); } while (0)
#define LOADD(k0, k1) do { _Pragma("unroll") for (int k = k0; k < (k1); ++k) \
    wd[k] = __builtin_amdgcn_raw_buffer_load_b128(rd, dvo, k * D * 2, WPOL); asm volatile("" ::: "memory"); } while (0)
  {
    const rsrc_t rr = mk_rsrc(wr, D * 2);
    u32x4 wf[8];
#pragma unroll
    for (int i = 0; i < 8; ++i) wf[i] = __builtin_amdgcn_raw_buffer_load_b128(rr, lq * 16 + (i & 3) * 64, ((r1 + (i >> 2) * 2) & 15) * 128, 0);
    const unsigned epoch = __builtin_amdgcn_raw_buffer_load_b32(rws, lane == 1 ? (hw ? amo : WS_FLAG2 + b * 128) : lane == 2 ? amo + (WS_FLAG1 - WS_AMAX) + hw * 32768 : lane == 3 ? WS_H + b * 64 : WS_EPOCH, 0, SC1);
    const u32x4 ra = __builtin_amdgcn_raw_buffer_load_b128(rr, lane * 16, 0, 0), rb2 = __builtin_amdgcn_raw_buffer_load_b128(rr, lane * 16 + 1024, 0, 0);
    misc[lc] = 0u;
    u32x4 ones = {0x3f803f80u, 0x3f803f80u, 0x3f803f80u, 0x3f803f80u};
    const int sx = (KM == 4 ? (te >> 2) + 2 * z : te >> 1) * 32 + hw * 16, sv = (KM == 4 ? tp1 >> 2 : tp1 >> 1 & 3) * 32 + hw * 16;
    int xa[2], wa[2];
    xa[0] = (sx + lc) * 128 + (lq ^ (lc & 7)) * 16; wa[0] = xa[0] + (sv - sx) * 128 + UB;
    xa[1] = xa[0] ^ 64; wa[1] = wa[0] ^ 64;
    asm volatile("" : "+v"(ones));
    f32x4 d1 = {0.f, 0.f, 0.f, 0.f}, d2 = d1, d3 = d1, d4 = d1;
    HST(9);
#pragma unroll
    for (int u = 0; u < 4; ++u) {
      if (u == 2) { wa[0] += 4 * UB; wa[1] += 4 * UB; }
      if (u == 3) { xa[0] += 7 * UB; xa[1] += 7 * UB; }
      PIN4(xa[0], xa[1], wa[0], wa[1]);
      WAIT_LGKM0();
      JOIN();
      const unsigned tj = (unsigned)__builtin_amdgcn_s_memrealtime();
      bf16x8 xf[2], vf[2];
#pragma unroll
      for (int h = 0; h < 2; ++h) { xf[h] = LDF(xa[h] + (u == 3 ? 0 : XI(u) * UB)); vf[h] = LDF(wa[h] + (u & 1) * 3 * UB); }
      __builtin_amdgcn_sched_barrier(0);
      if (u) LOADW((int)(SWT >> 8 * u & 63), (int)(SWT >> 8 * u + 8 & 63));
#pragma unroll
      for (int h = 0; h < 2; ++h) {
        d2 = MFMA(xf[h], xf[h], d2); d4 = MFMA(BF8(ones), xf[h], d4); d3 = MFMA(BF8(ones), vf[h], d3); d1 = MFMA(BF8(wf[2 * u + h]), xf[h], d1);
      }
      PIN4(d1, d2, d3, d4);
      if (u == 0) {
        tag = mk_tag(epoch);
        asm volatile("" : "+s"(tag));
        misc[2] = tag;
        const f32x4 d5 = MFMA(BF8(rb2), BF8(ones), MFMA(BF8(ra), BF8(ones), (f32x4{0.f, 0.f, 0.f, 0.f})));
        const int s4 = __builtin_bit_cast(int, (d5[0] + d5[1]) + (d5[2] + d5[3]));
#define RL(l) __builtin_bit_cast(float, __builtin_amdgcn_readlane(s4, l))
        sw = (RL(0) + RL(16)) + (RL(32) + RL(48));
        asm volatile("" : "+v"(sw));
        LOADW(0, (int)(SWT >> 8 & 63));
      }
      if (PI >> 8 * u & 255) {
#pragma unroll 1
        while ((unsigned)__builtin_amdgcn_s_memrealtime() - tj < (PI >> 8 * u & 255)) ;
      }
      __builtin_amdgcn_sched_barrier(0);
    }
    JOIN();
    const float sq = (lc & 2) ? ((lc & 1) ? d2[3] : d2[2]) : ((lc & 1) ? d2[1] : d2[0]);
    __builtin_amdgcn_raw_buffer_store_b128(u32x4{__float_as_uint(d4[0]), __float_as_uint(sq), __float_as_uint(d1[0]), tag}, rws,
                                           lq == lc >> 2 ? WS_XP + (tp1 * 4 + kb) * 2048 + (sx + lc) * 16 : -1, 0, SC1);
    __builtin_amdgcn_raw_buffer_store_b64(u32x2{__float_as_uint(d3[0]), tag}, rws, lane < 16 ? WS_WP + (tq1 * 4 + kb) * 1024 + (sv + lc) * 8 : -1, 0, SC1);
    __builtin_amdgcn_sched_barrier(0);
    LOADW((int)(SWT >> 32 & 63), (int)(SWT >> 40 & 63));
    if (SWT >> 48) { JOIN(); LOADW((int)(SWT >> 40 & 63), (int)(SWT >> 48 & 63)); }
    if (SWT >> 56) { JOIN(); LOADW((int)(SWT >> 48 & 63), (int)(SWT >> 56 & 63)); }
  }
  const rsrc_t rq = mk_rsrc(gq, QROWS * S * 2);
  const unsigned kt = ktab(tp1);
  const int trow = (te >> 2) * 1024 + (te & 3) * 32 + hw * 16;
  const int t1o = (trow + ((lane >> 1) & 15)) * (S * 2) + (lane & 1) * 128;
#define QTOUCH0(o, so) asm volatile("buffer_load_dword %0, %1, %2, %3 offen" : "+v"(tch) : "v"(o), "s"(rq), "s"(so) : "memory")
#define QTOUCH1(o, so) asm volatile("buffer_load_dword %0, %1, %2, %3 offen nt\n\tbuffer_load_dword %0, %1, %2, %3 offen" : "+v"(tch) : "v"(o), "s"(rq), "s"(so) : "memory")
#define QTOUCH(o, so) do { if (DTM >> i_ & 1) QTOUCH1(o, so); else QTOUCH0(o, so); } while (0)
#define TCH(m) do { _Pragma("unroll") for (int i_ = 0; i_ < 8; ++i_) if ((m) >> i_ & 1) { \
      if (i_ < 7) QTOUCH(t1o, kstep0(kt, 2 * i_) * 128); \
      else QTOUCH(lane & 16 ? ((tp1 >> 3) * 1024 + te * 16 + lc) * (S * 2) + (kstep(kt, 15) ^ hw * 16) * 128 : (trow + lc) * (S * 2) + kstep(kt, 14) * 128, 0); } } while (0)
  const int srow = hw * 32 + (lane & 3) + 2 * (lane & 44) + ((lane >> 2) & 4);
  {
    const int gxo = WS_XP + tp1 * 8192 + (hw * 64 + lane) * 16, gwo = WS_WP + tq1 * 4096 + (hw * 64 + lane) * 8;
    u32x4 gx[4]; u32x2 gw[4];
#define GATHER() asm volatile("buffer_load_dwordx4 %0, %8, %10, 0 offen sc1\n\tbuffer_load_dwordx4 %1, %8, %10, 0 offen offset:2048 sc1\n\t" \
                 "buffer_load_dwordx4 %2, %8, %10, %11 offen sc1\n\tbuffer_load_dwordx4 %3, %8, %10, %11 offen offset:2048 sc1\n\t" \
                 "buffer_load_dwordx2 %4, %9, %10, 0 offen sc1\n\tbuffer_load_dwordx2 %5, %9, %10, 0 offen offset:1024 sc1\n\t" \
                 "buffer_load_dwordx2 %6, %9, %10, 0 offen offset:2048 sc1\n\tbuffer_load_dwordx2 %7, %9, %10, 0 offen offset:3072 sc1" \
                 : "=&v"(gx[0]), "=&v"(gx[1]), "=&v"(gx[2]), "=&v"(gx[3]), "=&v"(gw[0]), "=&v"(gw[1]), "=&v"(gw[2]), "=&v"(gw[3]) \
                 : "v"(gxo), "v"(gwo), "s"(rws), "s"(4096) : "memory")
    UNTIL(sched(UP_T, te));
#define TCH7(i) do { if (T7 >= 0 && te == 7) { const int i_ = T7 & 7, o_ = (lane >> 1) * (S * 2) + (lane & 1) * 128, so_ = ((i) * 1024 + hw * 64) * (S * 2) + kstep0(kt, 2 * T7 & 15) * 128; \
      QTOUCH(o_, so_); QTOUCH(o_, so_ + 32 * S * 2); } } while (0)
    TCH(TM0);
    TCH7(0);
    LOADW(SWE, NWA1);
    if (NWA > NWA1) { JOIN(); LOADW(NWA1, NWA); }
    UNTIL(sched(T1_T, te) - GAD);
    GATHER();
    UNTIL(sched(T1_T, te));
    TCH(TM1);
    LOADW(NWA, NWC);
    wait_vm<NWC - NWA + __builtin_popcount(TM1) + __builtin_popcount(TM1 & DTM)>();
    TCH7(1);
#pragma unroll 1
    for (int spins = 0;; ++spins) {
      asm volatile("" : "+v"(gx[0]), "+v"(gx[1]), "+v"(gx[2]), "+v"(gx[3]), "+v"(gw[0]), "+v"(gw[1]), "+v"(gw[2]), "+v"(gw[3]) :: "memory");
      unsigned bad = 0;
#pragma unroll
      for (int q = 0; q < 4; ++q) bad |= (gx[q][3] ^ tag) | (gw[q][1] ^ tag);
      if (__builtin_expect(__all(bad == 0) || spins > SPIN_CAP / 64, 1)) break;
      GATHER();
      NEXT();
      POLLVM(0);
    }
    HST(3);
#define SUM4(v, c) ((__uint_as_float(v[0][c]) + __uint_as_float(v[1][c])) + (__uint_as_float(v[2][c]) + __uint_as_float(v[3][c])))
    float sum = SUM4(gx, 0), sq = SUM4(gx, 1), dot = SUM4(gx, 2);
    *(LAS float*)(lds + L_MB + 1024 + srow * 4) = SUM4(gw, 0);
    asm volatile("" : "+v"(sum), "+v"(sq), "+v"(dot));
    const float mn = sum * (1.0f / D);
    const float var = fmaxf(fmaf(-mn, mn, sq * (1.0f / D)), 0.f);
    const float mbv = rbf(mn), den = rbf(__builtin_amdgcn_sqrtf(rbf(rbf(var) + 1e-5f))), rden = __builtin_amdgcn_rcpf(den);
    *(LAS float*)(lds + L_MB + srow * 4) = -mbv;
    *(LAS float*)(lds + L_MB + 512 + srow * 4) = rden;
    *(LAS float*)(lds + L_MB + 1536 + srow * 4) = z ? -rden : rden;
    asm volatile("" ::: "memory");
    misc[4 + hw] = 1u;
    float rs = (dot - mbv * sw) * rden;
    sg = pack2(mbv, den);
    asm volatile("" : "+v"(rs), "+v"(sg));
    lim = NBAR;
    const unsigned rb = pack2(rs, rs) & 0xffffu;
    unsigned srt = (rb & 0x8000u) ? (~rb & 0xffffu) : (rb | 0x8000u);
    if (rs != rs) srt = 0u;
    key = (srt << 16) | (unsigned)(0xffff - tp1 * 128 - srow);
    const unsigned mk = wave_maxu(key);
    if (key == mk) __builtin_amdgcn_raw_buffer_store_b128(u32x4{key, tag, sg, tag}, rws, amo + hw * 16, 0, SC1);
    HST(2);
    UNTIL(WB_AT + (WBS >> 4 * te & 3));
    TCH(TMB);
    LOADW(NWC, 32);
  }
  unsigned sel[4];
#pragma unroll
  for (int d = 0; d < 4; ++d) {
    const unsigned src = (lc & 2) ? 0x0302u : 0x0100u;
    sel[d] = (lc >> 2) == d ? ((lc & 1) ? (src << 16 | 0x0c0cu) : (0x0c0c0000u | src)) : 0x0c0c0c0cu;
  }
  PIN4(sel[0], sel[1], sel[2], sel[3]);
  JOIN();
  const int hc = fg * 112 + hw * 56 + (lq * 7 + min(lc, 6)) * 2;
  int hio = WS_H + (hc & ~127) * 4 + ((hc & 6) * 8 + ((hc >> 3) & 15)) * 8;
  const int hs = min(b, 223) * 8 + hw * 4 + ((lane >> 3) & 3);
  int guo = WS_GU + ((lane & 7) * 2 + (lane >> 5)) * F * 8 + hs * 16, hoo = b < 224 && !(lane & 39) ? WS_H + hs * 8 : -1;
  asm volatile("" : "+v"(hio), "+v"(hoo));
  int gao = WS_AMAX + tq1 * 2048 + ((lane >> 1) & 15) * 128 + (lane & 1) * 16, gso = b < 224 ? WS_GU + ((rg * 2 + hw) * F + cg * 128) * 8 + lane * 16 : -1;
  asm volatile("" : "+v"(guo), "+v"(gao), "+v"(gso));
  UNTIL(AM_AT);
#pragma unroll 1
  for (int spins = 0;; ++spins) {
    const u32x4 am = __builtin_amdgcn_raw_buffer_load_b128(rws, gao, 0, SC1);
    NEXT();
    UNTIL(AM_P);
#pragma unroll 1
    while (vm_now() > 0 && nb < lim && nb != 19 && nb != 17) JOIN();
    key = am[0]; sg = am[2];
    if (__builtin_expect(__all(((am[1] ^ tag) | (am[3] ^ tag)) == 0) || spins > SPIN_CAP / 64, 1)) break;
  }
  {
    const unsigned mk = wave_maxu(key);
    sg = __builtin_amdgcn_readlane(sg, __builtin_ctzll(__ballot(key == mk)));
    key = mk;
  }
  idx = min(0xffff - (int)(key & 0xffffu), S - 1);
  const unsigned xe = __builtin_amdgcn_raw_buffer_load_b32(rx, (idx * D + (rg & 7) * 128) * 2 + lane * 4, 0, 0);
  misc[0] = idx + 1;
  HST(7);
  if (nb != 19) NEXT();
#pragma unroll 1
  while (vm_now() > 0 && nb < lim && nb != 19 && nb != 18 + XEJ) JOIN();
  {
    const unsigned xs = ln_pair(xe, -lo2f(sg), __builtin_amdgcn_rcpf(hi2f(sg)));
    if (b == 0 && lane == 0 && hw == 1) __builtin_amdgcn_raw_buffer_store_b32(tag, rws, WS_EPOCH, 0, SC1);
    u32x4 dg[16];
    expand<0, GX1>(dg, xs, sel);
    if (nb != 19) NEXT();
    expand<GX1, 16>(dg, xs, sel);
    UNTIL(19);
    const bool idle = nb == 19;
    if (idle) { if (GV_SL) __builtin_amdgcn_s_sleep(GV_SL); } else NEXT();
    f32x4 ae = {0.f, 0.f, 0.f, 0.f}, ao = ae;
#pragma unroll
    for (int k = 0; k < 16; ++k) {
      ae = MFMA(BF8(dg[k]), BF8(w[2 * k]), ae); ao = MFMA(BF8(dg[k]), BF8(w[2 * k + 1]), ao);
      if (k == 7) { asm volatile("" : "+v"(ae), "+v"(ao)); if (__builtin_expect(!idle, 0)) NEXT(); }
    }
    __builtin_amdgcn_raw_buffer_store_b128(u32x4{__float_as_uint(ae[0] + ao[2]), tag, __float_as_uint(ae[1] + ao[3]), tag}, rws, gso, 0, SC1);
  }
  HST(4);
  if (TGV) { if (TGS) __builtin_amdgcn_s_sleep(TGS); TCH(TGV); }
  if (nb == 19) JOIN();
  {
    const int m0_ = 20 + (te & MBS);
#define LATE(i) do { UNTIL(m0_ + (i)); __builtin_amdgcn_s_sleep(BSL2); } while (0)
    if (TE0) { LATE(0); TCH(TE0); }
    if (TE1) { LATE(1); TCH(TE1); }
    LATE(2);
    u32x4 gu;
    asm volatile("buffer_load_dwordx4 %0, %1, %2, 0 offen sc1" : "=&v"(gu) : "v"(guo), "s"(rws) : "memory");
    TCH(0x20);
    LOADD(0, WD1);
    JOIN();
    asm volatile("s_waitcnt vmcnt(%1)" : "+v"(gu) : "n"(WD1 + 1) : "memory");
#pragma unroll 1
    for (int spins = 0; !__all(((gu[1] ^ tag) | (gu[3] ^ tag)) == 0) && spins < SPIN_CAP / 64; ++spins) {
      asm volatile("buffer_load_dwordx4 %0, %1, %2, 0 offen sc1" : "=&v"(gu) : "v"(guo), "s"(rws) : "memory");
      NEXT();
      POLLVM(0);
      asm volatile("" : "+v"(gu) :: "memory");
    }
    {
      float s0 = __uint_as_float(gu[0]), s1 = __uint_as_float(gu[2]);
      DPP_ADD(s0, 0xb1, 0xf, true); DPP_ADD(s1, 0xb1, 0xf, true); DPP_ADD(s0, 0x4e, 0xf, true); DPP_ADD(s1, 0x4e, 0xf, true);
      DPP_ADD(s0, 0x141, 0xf, true); DPP_ADD(s1, 0x141, 0xf, true);
      const unsigned gp = pack2(s0, s1);
      const unsigned up = __builtin_amdgcn_permlane32_swap(gp, gp, false, false)[1];
      const unsigned sl2 = pack2(silu_fast(lo2f(gp)), silu_fast(hi2f(gp)));
      __builtin_amdgcn_raw_buffer_store_b64(u32x2{pack2(lo2f(sl2) * lo2f(up), hi2f(sl2) * hi2f(up)), tag}, rws, hoo, 0, SC1);
    }
    HST(8);
    LATE(4);
    TCH(0x40);
    LOADD(WD1, WD2);
    if (!hw && lane < (CODE_LINES + 7) / 8) {
      const int co = min(((b >> 3) & 7) * ((CODE_LINES + 7) / 8) + lane, CODE_LINES - 1) * 128;
      asm volatile("global_load_dword %0, %1, off" : "+v"(tch) : "v"(code + co) : "memory");
    }
    LATE(HB);
    u32x2 hv;
    asm volatile("buffer_load_dwordx2 %0, %1, %2, 0 offen sc1" : "=&v"(hv) : "v"(hio), "s"(rws) : "memory");
    TCH(0x80);
    LOADD(WD2, 14);
    NEXT();
    asm volatile("s_waitcnt vmcnt(%1)" : "+v"(hv) : "n"(14 - WD2 + 1) : "memory");
#pragma unroll 1
    for (int spins = 0; !__all(hv[1] == tag) && spins < SPIN_CAP / 64; ++spins) {
      asm volatile("buffer_load_dwordx2 %0, %1, %2, 0 offen sc1" : "=&v"(hv) : "v"(hio), "s"(rws) : "memory");
      NEXT();
      POLLVM(0);
      asm volatile("" : "+v"(hv) :: "memory");
    }
    f32x4 ae = {0.f, 0.f, 0.f, 0.f}, ao = ae;
    gemv<0, DG1>(ae, ao, hv[0], sel, wd);
    asm volatile("" : "+v"(ae), "+v"(ao));
    NEXT();
    if (DG2 < 7) {
      gemv<DG1, DG2>(ae, ao, hv[0], sel, wd);
      asm volatile("" : "+v"(ae), "+v"(ao));
      NEXT();
    } else POLLVM(0);
    if (HPR) __builtin_amdgcn_s_setprio(HPR - 1);
    gemv<DG2 < 7 ? DG2 : DG1, 7>(ae, ao, hv[0], sel, wd);
    f32x2 a = {ae[0] + ao[2], ae[1] + ao[3]};
    HST(5);
    if (hw == 1) {
      *(LAS f32x2*)(lds + L_CMB + lane * 8) = a;
      asm volatile("" ::: "memory");
      misc[6] = 1u;
    } else {
      f32x2 o;
#pragma unroll 1
      for (int spins = 0; spins < SPIN_CAP; ++spins) {
        NEXT();
        const unsigned f = misc[6];
        o = *(volatile LAS f32x2*)(lds + L_CMB + lane * 8);
        if (rfl(f)) break;
      }
      __builtin_amdgcn_raw_buffer_store_b128(u32x4{__float_as_uint(a[0] + o[0]), tag, __float_as_uint(a[1] + o[1]), tag}, rws,
                                             WS_PART + (fg * D + eg * 128) * 8 + lane * 16, 0, SC1);
      HST(6);
    }
  }
  if (hw == 1 && (idx >> 7) == tp1) {
    const unsigned srt = key >> 16;
    const float r = lo2f((srt & 0x8000u) ? (srt & 0x7fffu) : (~srt & 0xffffu));
    const float rsel = rbf(1.0f / (1.0f + __expf(-r)));
    const int cp = lane & 31;
    const int e = z * 64 + cp * 2;
    const int po = WS_PART + ((lane >> 5) * D + te * 128) * 8 + ((e & 6) * 8 + (e >> 3)) * 16;
    float y0, y1;
    UNTIL(FIN_AT);
    wait_sent((LAS char*)lds);
#pragma unroll 1
    for (int spins = 0;; ++spins) {
#pragma unroll
      for (int k = 0; k < 16; ++k) w[k] = __builtin_amdgcn_raw_buffer_load_b128(rws, po, k * 2 * D * 8, SC1);
      NEXT();
      POLLVM(0);
      unsigned bad = 0;
#pragma unroll
      for (int k = 0; k < 16; ++k) bad |= (w[k][1] ^ tag) | (w[k][3] ^ tag);
      if (__all(bad == 0) || spins > SPIN_CAP / 64) break;
    }
    NEXT();
    y0 = 0.f; y1 = 0.f;
#pragma unroll
    for (int k = 0; k < 16; ++k) { y0 += __uint_as_float(w[k][0]); y1 += __uint_as_float(w[k][2]); }
    asm volatile("" : "+v"(y0), "+v"(y1));
    NEXT();
    { const auto p0 = __builtin_amdgcn_permlane32_swap(__float_as_uint(y0), __float_as_uint(y0), false, false);
      const auto p1 = __builtin_amdgcn_permlane32_swap(__float_as_uint(y1), __float_as_uint(y1), false, false);
      y0 = __uint_as_float(p0[0]) + __uint_as_float(p0[1]); y1 = __uint_as_float(p1[0]) + __uint_as_float(p1[1]); }
    *(LAS f32x2*)(lds + L_UPD + cp * 8) = f32x2{rbf(rbf(y0) * rsel), rbf(rbf(y1) * rsel)};
    asm volatile("" ::: "memory");
    misc[1] = 1u;
    HST(6);
  }
  HST(10);
  POLLVM(0);
  asm volatile("" : "+v"(tch));
  STAMP_OUT();
}

typedef __attribute__((ext_vector_type(8))) u64 u64x8;
typedef __attribute__((ext_vector_type(2))) u64 u64x2;
__global__ __launch_bounds__(512, 2) void fused(const u16*, const u16*, const u16*, const u16*, char*, const u16*, const u16*, const u16*,
                                                const u16*, const u16*, u16*) {
  extern __shared__ __attribute__((aligned(16))) char lds[];
  u64x8 a0; u64x2 a1; u64 a2;
  u64 pc;
  asm volatile("s_load_dwordx16 %0, %4, 0\n\ts_load_dwordx4 %1, %4, 64\n\ts_load_dwordx2 %2, %4, 80\n\ts_getpc_b64 %3"
               : "=&s"(a0), "=&s"(a1), "=&s"(a2), "=&s"(pc) : "s"(__builtin_amdgcn_kernarg_segment_ptr()) : "memory");
  __builtin_amdgcn_sched_barrier(0);
  const char* code = (const char*)(pc & ~255ull);
  const int wid = __builtin_amdgcn_readfirstlane(threadIdx.x >> 6), lane = threadIdx.x & 63;
  const int b = blockIdx.x, g = b & 7, i = b >> 3;
  const int tp1 = (g & 3) * 4 + (i & 3), tq1 = (g >> 2) * 8 + (i >> 2);
  const int z = tq1 >> 3, te = tq1 & 7;
  const int L = wid & 1, rsub = lane >> 3;
  int v1 = ((L ? te : tp1) * 128 + 8 * (rsub >> 2) + (rsub & 3)) * (D * 2) + ((lane & 7) ^ rsub) * 16, ko = rot1(tp1, tq1) * 128;
  unsigned g1n = (L ? D : S) * D * 2;
  int role = wid >> 1;
  asm volatile("" : "+v"(v1));
  asm volatile("" : "+s"(ko), "+s"(g1n), "+s"(role));
  __builtin_amdgcn_sched_barrier(0);
  asm volatile("s_waitcnt lgkmcnt(0)" : "+s"(a0), "+s"(a1), "+s"(a2) :: "memory");
  const u16 *x = (const u16*)a0[0], *wr = (const u16*)a0[3], *sre = (const u16*)a0[5], *sim = (const u16*)a0[6], *wg = (const u16*)a0[7];
  const u16 *wu = (const u16*)a1[0], *wdn = (const u16*)a1[1];
  char* ws = (char*)a0[4];
  u16* out = (u16*)a2;
  const rsrc_t rws = mk_rsrc(ws, WS_END);
  if (__builtin_expect(role == 2, 1)) {
    loader(L, lane, (LAS char*)lds, mk_rsrc((const void*)(L ? (z ? a0[2] : a0[1]) : a0[0]), g1n), v1, ko, (z ? sim : sre) + (size_t)(tp1 & 7) * 128 * S,
           ws + WS_ABT + (size_t)tq1 * ABT_ST, rws, WS_FLAG1 + tq1 * 2048, b, tp1, tq1 STAMP_PASS);
  } else if (wid < 4) {
    consumer(wid, lane, b, lds, rws, mk_rsrc(out, S * D * 2), code, tp1, tq1 STAMP_PASS);
  } else {
    const int hw = wid - 6;
    helper(hw, lane, b, lds, rws, mk_rsrc(x, S * D * 2), wr, hw ? wu : wg, wdn,
           (z ? sim : sre) + (size_t)(tp1 & 7) * 128 * S, code, tp1, tq1 STAMP_PASS);
  }
}

extern "C" void kernel_launch(void* const* d_in, const int* in_sizes, int n_in, void* d_out, int out_size,
                              void* d_ws, size_t ws_size, hipStream_t stream) {
  (void)hipFuncSetAttribute((const void*)fused, hipFuncAttributeMaxDynamicSharedMemorySize, LDS_BYTES);
  fused<<<256, 512, LDS_BYTES, stream>>>((const u16*)d_in[0], (const u16*)d_in[3], (const u16*)d_in[4], (const u16*)d_in[6], (char*)d_ws,
                                         (const u16*)d_in[1], (const u16*)d_in[2], (const u16*)d_in[7], (const u16*)d_in[8],
                                         (const u16*)d_in[9], (u16*)d_out);
}
```

```cpp
#define CODE_LINES 277
#include <hip/hip_runtime.h>
#include <hip/hip_bf16.h>
#include <stdint.h>

typedef __attribute__((ext_vector_type(8))) short bf16x8;
typedef __attribute__((ext_vector_type(4))) float f32x4;
typedef __attribute__((ext_vector_type(2))) float f32x2;
typedef __attribute__((ext_vector_type(4))) unsigned u32x4;
typedef __attribute__((ext_vector_type(2))) unsigned u32x2;
typedef __attribute__((ext_vector_type(2))) __bf16 bf16x2;
typedef unsigned short u16;
typedef unsigned long long u64;
typedef __amdgpu_buffer_rsrc_t rsrc_t;
#define LAS __attribute__((address_space(3)))

constexpr int S = 2048, D = 1024, F = 3584;
constexpr int UB = 16384;
constexpr int SLOT = 3 * UB;
constexpr int L_MISC = 9 * UB;
constexpr int L_UPD = L_MISC + 64;
constexpr int L_MB = L_MISC + 512;
constexpr int L_CMB = L_MB + 2048;
constexpr int LDS_BYTES = L_CMB + 512;
constexpr int NBAR = 17 + 14 + 3;
constexpr int SC1 = 16, NT = 2;

constexpr unsigned WS_EPOCH = 0;
constexpr unsigned WS_XP = 4096;
constexpr unsigned WS_WP = WS_XP + 16 * 8192;
constexpr unsigned WS_AMAX = WS_WP + 16 * 4096;
constexpr unsigned WS_FLAG1 = WS_AMAX + 32768;
constexpr unsigned WS_FLAG2 = WS_FLAG1 + 65536;
constexpr unsigned WS_GU = WS_FLAG2 + 32768;
constexpr unsigned WS_H = WS_GU + 8 * 2 * F * 8;
constexpr unsigned WS_PART = WS_H + 16384;
constexpr unsigned WS_XCH = WS_PART + 32 * D * 8;
constexpr int XB = 49152;
constexpr unsigned ABT_ST = 128 * S * 2;
constexpr unsigned WS_ABT = WS_XCH + 256 * XB;
constexpr unsigned WS_END = WS_ABT + 16 * ABT_ST;

#define STAMP(i)
#define HSTAMP()
#define HPRE()
#define HST(i)
#define STAMP_DECL
#define STAMP_ARG
#define STAMP_PASS
#define STAMP_OUT()

__device__ __forceinline__ float lo2f(unsigned v) { return __uint_as_float(v << 16); }
__device__ __forceinline__ float hi2f(unsigned v) { return __uint_as_float(v & 0xffff0000u); }
__device__ __forceinline__ unsigned pack2(float a, float b) {
  return __builtin_bit_cast(unsigned, __builtin_convertvector(f32x2{a, b}, bf16x2));
}
__device__ __forceinline__ float rbf(float f) { return lo2f(pack2(f, f)); }
__device__ __forceinline__ unsigned rfl(unsigned v) { return __builtin_amdgcn_readfirstlane(v); }
__device__ __forceinline__ rsrc_t mk_rsrc(const void* p, unsigned bytes) {
  return __builtin_amdgcn_make_buffer_rsrc((void*)p, 0, bytes, 0x00020000);
}
#define DPP_ADD(v, ctrl, rm, bc) v += __builtin_bit_cast(float, __builtin_amdgcn_update_dpp(0, __builtin_bit_cast(int, v), ctrl, rm, 0xf, bc))
#define DPP_MAXU(v, ctrl, rm, bc) v = max(v, (unsigned)__builtin_amdgcn_update_dpp(0, (int)v, ctrl, rm, 0xf, bc))
__device__ __forceinline__ unsigned wave_maxu(unsigned v) {
  DPP_MAXU(v, 0x111, 0xf, true); DPP_MAXU(v, 0x112, 0xf, true); DPP_MAXU(v, 0x114, 0xf, true); DPP_MAXU(v, 0x118, 0xf, true);
  DPP_MAXU(v, 0x142, 0xa, false); DPP_MAXU(v, 0x143, 0xc, false);
  return (unsigned)__builtin_amdgcn_readlane((int)v, 63);
}
template <int N> __device__ __forceinline__ void wait_vm() { asm volatile("s_waitcnt vmcnt(%0)" :: "n"(N) : "memory"); }
#define WAIT_LGKM0() asm volatile("s_waitcnt lgkmcnt(0)" ::: "memory")
#define BAR() do { asm volatile("" ::: "memory"); __builtin_amdgcn_s_barrier(); asm volatile("" ::: "memory"); } while (0)
#define MFMA(a, b, c) __builtin_amdgcn_mfma_f32_16x16x32_bf16(a, b, c, 0, 0, 0)
#define BF8(v) __builtin_bit_cast(bf16x8, v)
constexpr int SPIN_CAP = 1 << 20;
#define T1_T 0x090a09090a0a0909ull
#define NWC 30
#define WB_AT 12
#define WBS 0x11001100
#define SWT 0x100d0a070400ull
#define PI 0x2e32363c
#define BSL 13
#define WD1 5
#define WD2 10
__device__ __forceinline__ int sched(u64 tab, int te) { return (int)(tab >> (te * 8)) & 255; }
#define PIN4(a, b, c, d) asm volatile("" : "+v"(a), "+v"(b), "+v"(c), "+v"(d))
__device__ __forceinline__ unsigned vm_now() { const unsigned r = __builtin_amdgcn_s_getreg(7 | (31 << 11)); return (r & 15u) | ((r >> 18) & 0x30u); }
__device__ __forceinline__ unsigned mk_tag(unsigned epoch) { return rfl(epoch) * 0x9E3779B1u + 0x7F4A7C15u; }

template <int LD, int AUX, int MODE, int NP = 16, int J0 = 0>
__device__ __forceinline__ void issue(rsrc_t r, int voff, int koff, LAS char* dst) {
#pragma unroll
  for (int j = J0; j < NP; ++j) {
    const int so = MODE == 2 ? j * 1024 : MODE == 1 ? (32 * (j >> 3) + 64 * ((j >> 2) & 1) + 16 * (j & 1) + 4 * ((j >> 1) & 1)) * LD : j * 8 * LD;
    __builtin_amdgcn_raw_ptr_buffer_load_lds(r, (LAS void*)(dst + j * 1024), 16, voff, koff + so, 0, AUX);
  }
}
__device__ __forceinline__ unsigned ktab(int tp1) { return (tp1 * 0x01010101u | ((tp1 & 12) ^ 4) * 0x10101010u) ^ (tp1 & 2 ? 0x33122100u : 0x33221100u); }
__device__ __forceinline__ int rot1(int tp1, int tq1) { return (8 * (tp1 + tq1) + 2 * (((tp1 >> 2) + 2 * (tq1 >> 3)) & 3)) & 15; }
__device__ __forceinline__ int kstep0(unsigned kt, int n) { return (int)(kt >> (n >> 1) * 4 & 15) * 2 + (n & 1); }
__device__ __forceinline__ int kstep(unsigned kt, int n) { return kstep0(kt, n == 3 ? 4 : n == 4 ? 3 : n); }
constexpr int QROWS = 1024 + 128;

__device__ __forceinline__ void wait_sent(LAS char* lds) {
#pragma unroll 1
  for (int spins = 0; spins < SPIN_CAP; ++spins) {
    const u32x4 f = *(volatile LAS u32x4*)(lds + L_MISC + 32);
    if (rfl(f[0] & f[1] & f[2] & f[3])) break;
    __builtin_amdgcn_s_sleep(1);
  }
  asm volatile("" ::: "memory");
}

__device__ __forceinline__ void loader(int L, int lane, LAS char* lds, rsrc_t r1, int v1, int ko, const u16* gq, const char* gp, rsrc_t rws,
                                       int flag_off, int b, int tp1 STAMP_ARG) {
  STAMP_DECL;
  __builtin_amdgcn_s_setprio(2);
  const int rsub = lane >> 3, ch = ((lane & 7) ^ rsub) * 16;
  const rsrc_t rq = mk_rsrc(gq, QROWS * S * 2);
  const int h = tp1 >> 3;
  unsigned kt = ktab(tp1);
  int vq = rsub * (S * 2) + ch, vp = lane * 16;
  int qa = h * 1024 * (S * 2), qb = ((1 - h) * 1024 + L * 64) * (S * 2);
  int q_ = (L ? qa : qb) + tp1 * 256;
  LAS char* d_ = lds + (1 - L) * UB;
  volatile LAS unsigned* misc = (volatile LAS unsigned*)(lds + L_MISC);
  unsigned fl = 0, tag;
  {
    const unsigned utab = L ? 0x8541u : 0x7230u;
    STAMP(1);
    LAS char* dst = lds + L * UB;
#pragma unroll 1
    for (int t = 0; t < 4; ++t) {
      issue<D * 2, 0, 1>(r1, v1, ko, dst);
      ko = (ko + 128) & 2047;
      dst = lds + ((utab >> (t * 4 + 4)) & 15) * UB;
      if (t == 1 || t == 2) { wait_vm<16>(); BAR(); }
    }
    LAS char* dt[4];
#pragma unroll
    for (int u = 0; u < 4; ++u) dt[u] = lds + ((utab >> (u * 4)) & 15) * UB;
    asm volatile("" : "+s"(kt), "+s"(qa), "+s"(qb), "+s"(q_), "+s"(d_) : "s"(rq));
    asm volatile("" : "+v"(vq), "+v"(vp));
#pragma unroll 1
    for (int t = 0; t < 3; ++t) {
#pragma unroll
      for (int u = 0; u < 4; ++u) {
        issue<D * 2, 0, 1, 8>(r1, v1, ko, dt[u]);
        int kn = (ko + 128) & 2047;
        asm volatile("" : "+s"(kn), "+s"(dt[(u + 1) & 3]));
        if (u == 1) asm volatile("ds_read_b32 %0, %1" : "=v"(tag) : "v"(L_MISC + 8) : "memory");
        issue<D * 2, 0, 1, 16, 8>(r1, v1, ko, dt[u]);
        ko = kn;
        wait_vm<32>();
        BAR();
      }
    }
    asm volatile("s_waitcnt lgkmcnt(0)" : "+v"(tag) :: "memory");
  }
#define POLL(o) asm volatile("buffer_load_dword %0, %1, %2, 0 offen sc1" : "+v"(fl) : "v"(o), "s"(rws) : "memory")
#define POLLW(o) asm volatile("buffer_load_dword %0, %1, %2, 0 offen sc1\n\ts_waitcnt vmcnt(0)" : "+v"(fl) : "v"(o), "s"(rws) : "memory")
  {
    issue<S * 2, 0, 0>(rq, vq, q_, d_);
    const int q2 = qa + L * 64 * (S * 2) + kstep(kt, 2) * 128;
    wait_vm<32>();
    BAR();
    issue<S * 2, 0, 0, 8>(rq, vq, q2, lds + 2 * SLOT + L * 8192);
    wait_vm<24>();
    BAR();
    issue<S * 2, 0, 0>(rq, vq, q_ + 128, d_ + SLOT);
    tag = rfl(tag);
    BAR();
    wait_vm<16>();
    BAR();
    issue<S * 2, 0, 0, 8>(rq, vq, qb + kstep(kt, 2) * 128, lds + 2 * SLOT + UB + L * 8192);
    wait_vm<8>();
    BAR();
  }
  const int vx = (lane ^ (lane >> 2 & 12)) * 16;
  if (L) {
    int k_ = kstep(kt, 3) * 128, ka = qa + k_;
    LAS char* sl = lds;
#pragma unroll 1
    for (int n = 4; n < 16; ++n) {
      issue<S * 2, 0, 0>(rq, vq, ka, sl);
      const int kb = qb + k_;
      LAS char* const sb = sl + UB + 8192;
      k_ = kstep(kt, n) * 128; ka = qa + k_;
      sl = sl == lds + 2 * SLOT ? lds : sl + SLOT;
      asm volatile("" : "+s"(k_), "+s"(ka), "+s"(sl));
      issue<S * 2, 0, 0, 8>(rq, vq, kb, sb);
      wait_vm<24>();
      BAR();
    }
    issue<S * 2, 0, 0>(rq, vq, qa + (k_ ^ 2048), lds);
    const int f2o = WS_FLAG2 + (b ^ 4) * 128 + 16 + (lane & 3) * 4;
    wait_vm<16>();
    BAR();
    wait_vm<0>();
    BAR();
    issue<S * 2, 0, 0, 8>(rq, vq, qa + 64 * (S * 2) + k_, lds + 4 * UB + 8192);
    wait_vm<0>();
    BAR();
    wait_sent(lds);
#pragma unroll 1
    for (int spins = 0; spins < SPIN_CAP; ++spins) {
      POLLW(f2o);
      if (__all(fl == tag)) break;
      __builtin_amdgcn_s_sleep(1);
    }
    STAMP(4);
    issue<0, SC1, 2>(rws, vx, WS_XCH + (b ^ 4) * XB + 32768, lds + 4 * UB);
    wait_vm<0>();
    misc[12] = 1u;
    STAMP(5);
  } else {
    const rsrc_t rp = mk_rsrc(gp, 128 * S * 2);
    unsigned mask = 0;
    const int fo = flag_off + 32768 + (((lane >> 2) & 7) == (tp1 & 7) ? (kt >> 28) ^ 8 : h * 8 + ((lane >> 2) & 7)) * 128 + (lane & 3) * 4;
#define KM_(k) (15u << 2 * ((k) & 14))
    const int fo1 = flag_off + (kt >> (lane & 4 ? 8 : 4) & 15) * 128 + (lane & 3) * 4;
    STAMP(2);
    {
      unsigned f1, f2;
      asm volatile("buffer_load_dword %0, %1, %2, 0 offen sc1" : "=&v"(f1) : "v"(fo1), "s"(rws) : "memory");
      issue<S * 2, 0, 0, 8>(rq, vq, qb + kstep(kt, 3) * 128, lds + UB);
      asm volatile("buffer_load_dword %0, %2, %3, 0 offen sc1\n\ts_waitcnt vmcnt(9)" : "=&v"(f2), "+v"(f1) : "v"(fo1), "s"(rws) : "memory");
      unsigned m1 = (unsigned)__ballot(f1 == tag);
#pragma unroll 1
      for (int spins = 0; (m1 & 15) != 15 && spins < SPIN_CAP; ++spins) {
        asm volatile("s_sleep 2\n\tbuffer_load_dword %0, %2, %3, 0 offen sc1\n\ts_waitcnt vmcnt(1)" : "+v"(f1), "+v"(f2) : "v"(fo1), "s"(rws) : "memory");
        m1 |= (unsigned)__ballot(f2 == tag);
        if ((m1 & 15) == 15) break;
        asm volatile("s_sleep 2\n\tbuffer_load_dword %0, %2, %3, 0 offen sc1\n\ts_waitcnt vmcnt(1)" : "+v"(f2), "+v"(f1) : "v"(fo1), "s"(rws) : "memory");
        m1 |= (unsigned)__ballot(f1 == tag);
      }
      STAMP(3);
      issue<0, SC1, 2>(rp, vp, kstep(kt, 2) * UB, lds + 2 * SLOT + 2 * UB);
      asm volatile("s_waitcnt vmcnt(16)" : "+v"(f1), "+v"(f2) :: "memory");
      m1 |= (unsigned)(__ballot(f1 == tag) | __ballot(f2 == tag));
      const bool p3 = (m1 & 0xf0) == 0xf0;
      if (!p3) {
        POLL(fo1);
        wait_vm<1>();
        BAR();
        asm volatile("s_waitcnt vmcnt(0)" : "+v"(fl) :: "memory");
#pragma unroll 1
        for (int spins = 0; ((unsigned)__ballot(fl == tag) & 0xf0) != 0xf0 && spins < SPIN_CAP; ++spins) POLLW(fo1);
      }
      POLL(fo);
      issue<0, SC1, 2>(rp, vp, kstep(kt, 3) * UB, lds + 2 * UB);
      if (p3) { wait_vm<17>(); BAR(); }
      asm volatile("s_waitcnt vmcnt(16)" : "+v"(fl) :: "memory");
    }
#define NEED(need) do { if (__builtin_expect(mask != ~0u, 0)) { asm volatile("" : "+v"(fl) :: "memory"); mask |= (unsigned)__ballot(fl == tag); \
      _Pragma("unroll 1") for (int spins = 0; (mask & (need)) != (need) && spins < SPIN_CAP; ++spins) { POLLW(fo); mask |= (unsigned)__ballot(fl == tag); } \
      if (mask != ~0u) POLL(fo); } } while (0)
    int k_ = kstep(kt, 4), kp = k_ * UB;
    LAS char* sl = lds + SLOT;
#pragma unroll 1
    for (int n = 5; n < 16; ++n) {
      NEED(KM_(k_));
      issue<0, SC1, 2>(rp, vp, kp, sl + 2 * UB);
      const int kb = qb + k_ * 128;
      LAS char* const sb = sl + UB;
      k_ = kstep0(kt, n); kp = k_ * UB;
      sl = sl == lds + 2 * SLOT ? lds : sl + SLOT;
      asm volatile("" : "+s"(k_), "+s"(kp), "+s"(sl));
      issue<S * 2, 0, 0, 8>(rq, vq, kb, sb);
      wait_vm<24>();
      BAR();
    }
    NEED(~0u);
    issue<0, SC1, 2>(rp, vp, (k_ ^ 16) * UB, lds + 2 * UB);
    wait_vm<16>();
    BAR();
    issue<0, SC1, 2>(rp, vp, k_ * UB, lds + UB);
    wait_vm<16>();
    BAR();
    issue<S * 2, 0, 0, 8>(rq, vq, qa + k_ * 128, lds + 4 * UB);
    const int f2o = WS_FLAG2 + (b ^ 2 ^ (lane & 4)) * 128 + (lane & 3) * 4;
    wait_vm<0>();
    BAR();
    wait_sent(lds);
#pragma unroll 1
    for (int spins = 0; spins < SPIN_CAP; ++spins) {
      POLLW(f2o);
      if (__all(fl == tag)) break;
      __builtin_amdgcn_s_sleep(1);
    }
    issue<0, SC1, 2>(rws, vx, WS_XCH + (b ^ 2) * XB, lds + 6 * UB);
    issue<0, SC1, 2>(rws, vx, WS_XCH + (b ^ 6) * XB + 16384, lds + 7 * UB);
    STAMP(4);
    wait_vm<0>();
    misc[13] = 1u;
    STAMP(5);
  }
  STAMP_OUT();
}

#define LDF(off) (*reinterpret_cast<const bf16x8*>(lds + (off)))
struct Frag { bf16x8 p[4], q[4]; };
#define SGB(m, n) __builtin_amdgcn_sched_group_barrier(m, n, 0)
__device__ __forceinline__ float silu_fast(float z) { return z * __builtin_amdgcn_rcpf(1.0f + __expf(-z)); }
__device__ __forceinline__ u32x4 pack8(const f32x4& a, const f32x4& c) {
  return u32x4{pack2(a[0], a[1]), pack2(a[2], a[3]), pack2(c[0], c[1]), pack2(c[2], c[3])};
}
__device__ __forceinline__ unsigned ln_pair(unsigned x, float nmb, float rden) {
  const unsigned d = pack2(lo2f(x) + nmb, hi2f(x) + nmb);
  return pack2(lo2f(d) * rden, hi2f(d) * rden);
}

template <bool BARRIER, bool LAST, bool ZERO, class X>
__device__ __forceinline__ void big_half(f32x4 (&a0)[4][4], f32x4 (&a1)[4][4], const bf16x8 (&pc)[4], bf16x8 (&pn)[4], bf16x8 (&q)[8],
                                         const char* lds, int qc, int pa, int qn, int qn2, X extra) {
  const f32x4 zero = {0.f, 0.f, 0.f, 0.f};
#pragma unroll
  for (int g = 0; g < 8; ++g) {
    if (g == 0) { q[5] = LDF(qc + UB + 2048); q[6] = LDF(qc + UB + 4096); }
    if (g == 1) q[7] = LDF(qc + UB + 6144);
    if (g == 3) { pn[0] = LDF(pa); pn[1] = LDF(pa + 2048); }
    if (g == 4) { pn[2] = LDF(pa + 4096); pn[3] = LDF(pa + 6144); }
    if (g == 5) { q[0] = LDF(qn); q[1] = LDF(qn + 2048); }
    if (g == 6) { q[2] = LDF(qn + 4096); q[3] = LDF(qn + 6144); }
    if (g == 7 && !LAST) q[4] = LDF(qn2);
#pragma unroll
    for (int i = 0; i < 4; ++i) {
      const int ii = (g & 1) ? 3 - i : i;
      if (g < 4) a0[ii][g] = MFMA(pc[ii], q[g], ZERO ? zero : a0[ii][g]); else a1[ii][g - 4] = MFMA(pc[ii], q[g], ZERO ? zero : a1[ii][g - 4]);
    }
    SGB(0x8, 1); SGB(0x100, 1); SGB(0x8, 1); SGB(0x100, 1); SGB(0x8, 2);
    __builtin_amdgcn_sched_barrier(0);
    if (g == 2 && BARRIER) { WAIT_LGKM0(); BAR(); }
    extra(g);
  }
}

template <bool JOBNEXT>
__device__ __forceinline__ void small_step(f32x4 (&a)[4][4], bf16x8 (&p0)[4], bf16x8 (&p1)[4], bf16x8 (&q)[8], const char* lds,
                                           int ph, int qh, int pn, int qn, int qn2) {
#pragma unroll
  for (int g = 0; g < 8; ++g) {
    if (g == 0) { q[4] = LDF(qh); p1[0] = LDF(ph); }
    if (g == 1) { p1[1] = LDF(ph + 2048); p1[2] = LDF(ph + 4096); }
    if (g == 2) { p1[3] = LDF(ph + 6144); q[5] = LDF(qh + 2048); }
    if (g == 3) { q[6] = LDF(qh + 4096); q[7] = LDF(qh + 6144); }
    if (g == 4) { p0[0] = LDF(pn); q[0] = LDF(qn); }
    if (g == 5) { p0[1] = LDF(pn + 2048); p0[2] = LDF(pn + (JOBNEXT ? 1024 : 4096)); if (JOBNEXT) q[1] = LDF(qn + 2048); }
    if (g == 6) { p0[3] = LDF(pn + (JOBNEXT ? 3072 : 6144)); if (JOBNEXT) { q[2] = LDF(qn + 4096); q[3] = LDF(qn + 6144); } else q[1] = LDF(qn + 2048); }
    if (g == 7) { if (JOBNEXT) { q[4] = LDF(qn2); q[5] = LDF(qn2 + 2048); } else { q[2] = LDF(qn + 4096); q[3] = LDF(qn + 6144); } }
#pragma unroll
    for (int i = 0; i < 4; ++i) {
      const int ii = (g & 1) ? 3 - i : i;
      a[ii][g & 3] = MFMA(g < 4 ? p0[ii] : p1[ii], q[g], a[ii][g & 3]);
    }
    SGB(0x8, 1); SGB(0x100, 1); SGB(0x8, 1); SGB(0x100, 1); SGB(0x8, 1); SGB(0x100, 1); SGB(0x8, 1);
    __builtin_amdgcn_sched_barrier(0);
    if (g == 3) BAR();
  }
}

__device__ __forceinline__ void consumer(int wid, int lane, int b, char* lds, rsrc_t rws, rsrc_t rout,
                                         const char* code, int tp1, int tq1 STAMP_ARG) {
  STAMP_DECL;
  const int z = tq1 >> 3, te = tq1 & 7;
  const int wp = wid >> 1, wq = wid & 1, fr = lane & 15, fq = lane >> 4;
  const int c0 = (fq ^ (fr & 7)) * 16, c1 = ((fq + 4) ^ (fr & 7)) * 16;
  int q0 = (wq * 64 + fr) * 128 + c0, q1 = (wq * 64 + fr) * 128 + c1;
  volatile LAS unsigned* misc = (volatile LAS unsigned*)(lds + L_MISC);
  f32x4 acc[4][4];
#pragma unroll
  for (int i = 0; i < 4; ++i)
#pragma unroll
    for (int j = 0; j < 4; ++j) acc[i][j] = f32x4{0.f, 0.f, 0.f, 0.f};
  unsigned tag;
  u32x4 xv[4];
  {
    Frag f0, f1;
    const int xrow = wq * 32 + (lane & 16) + 4 * (lane >> 5) + ((lane >> 2) & 3);
    const int xr = xrow * 128 + (((wp * 4 + (lane & 3)) ^ (xrow & 7)) * 16);
#define XV(u) do { _Pragma("unroll") for (int k = 0; k < 4; ++k) \
      xv[k] = *reinterpret_cast<const u32x4*>(lds + (u) * UB + xr + (k >> 1) * 8192 + (k & 1) * 1024); } while (0)
    const int p0 = (wp * 64 + fr) * 128 + c0, p1 = (wp * 64 + fr) * 128 + c1;
    unsigned tch = 0;
    if (wid == 3 && lane < (CODE_LINES + 7) / 8) {
      __builtin_amdgcn_s_sleep(8);
      const int co = min(((b >> 3) & 7) * ((CODE_LINES + 7) / 8) + lane, CODE_LINES - 1) * 128;
      asm volatile("global_load_dword %0, %1, off" : "+v"(tch) : "v"(code + co) : "memory");
    }
    const int xt = (2 * te + z - rot1(tp1, tq1)) & 15;
#define G1H(fc, fn, po, qo, EXTRA) do { _Pragma("unroll") for (int g = 0; g < 8; ++g) { \
      EXTRA(g); { if (g == 0) { fn.q[0] = LDF(qo); fn.p[0] = LDF(po); } else if (g == 1) { fn.p[1] = LDF((po) + 2048); fn.p[2] = LDF((po) + 4096); } \
        else if (g == 2) { fn.p[3] = LDF((po) + 6144); fn.q[1] = LDF((qo) + 2048); } else if (g == 3) { fn.q[2] = LDF((qo) + 4096); fn.q[3] = LDF((qo) + 6144); } } \
      _Pragma("unroll") for (int e = 0; e < 2; ++e) { const int i0 = (g & 1) * 2 + e, ii = (g & 2) ? 3 - i0 : i0; \
        acc[ii][g >> 1] = MFMA(fc.p[ii], fc.q[g >> 1], acc[ii][g >> 1]); } \
      SGB(0x8, 1); SGB(0x100, 1); SGB(0x8, 1); SGB(0x100, 2); __builtin_amdgcn_sched_barrier(0); } } while (0)
#define X_NO(g)
#define XI(u) ((u) == 0 ? 0 : (u) == 1 ? 3 : (u) == 2 ? 2 : 7)
#define XVC(t, u) do { if (__builtin_expect((t) == xt, 0)) { XV(XI(u)); PIN4(xv[0], xv[1], xv[2], xv[3]); } __builtin_amdgcn_sched_barrier(0); } while (0)
    int pA[2] = {p0, p1}, pB[2] = {p0 + 7 * UB, p1 + 7 * UB}, qA[2] = {q0 + UB, q1 + UB}, qB[2] = {q0 + 5 * UB, q1 + 5 * UB};
    PIN4(pA[0], pA[1], pB[0], pB[1]); PIN4(qA[0], qA[1], qB[0], qB[1]);
#define XA(u, h) ((u) == 3 ? pB[h] : pA[h] + XI(u) * UB)
#define WA(u, h) ((u) < 2 ? qA[h] + (u) * 3 * UB : qB[h] + ((u) - 2) * 3 * UB)
    int ta = L_MB + (wp * 32 + fq * 8) * 4, tc = L_MB + 1024 + (wq * 32 + 8 * (fr >> 2) + (fr & 3)) * 4;
    const int flip = z ? 0 : 4096;
    int ao[2], ow[2];
#pragma unroll
    for (int h = 0; h < 2; ++h) {
      ao[h] = WS_ABT + tq1 * ABT_ST + tp1 * 2 * UB + wq * 8192 + wp * 1024 + lane * 16 + (h * 4096 ^ flip);
      ow[h] = 2 * UB + wq * 8192 + wp * 1024 + lane * 16 + (h * 4096 ^ flip);
    }
    PIN4(ao[0], ao[1], ow[0], ow[1]);
    asm volatile("" : "+v"(ta), "+v"(tc));
    BAR();
    XVC(0, 0);
    f0.q[0] = LDF(WA(0, 0));
#pragma unroll
    for (int i = 0; i < 4; ++i) f0.p[i] = LDF(XA(0, 0) + i * 2048);
#pragma unroll
    for (int i = 1; i < 4; ++i) f0.q[i] = LDF(WA(0, 0) + i * 2048);
    __builtin_amdgcn_sched_barrier(0);
#pragma unroll 1
    for (int t = 0; t < 12; t += 4) {
#pragma unroll
      for (int u = 0; u < 4; ++u) {
        G1H(f0, f1, XA(u, 1), WA(u, 1), X_NO);
        WAIT_LGKM0();
        BAR();
        XVC(t + u + 1, (u + 1) & 3);
        G1H(f1, f0, XA((u + 1) & 3, 0), WA((u + 1) & 3, 0), X_NO);
      }
    }
    u32x2 rdy;
#define X_RDY(g) if ((g) == 4) rdy = *(const volatile LAS u32x2*)(lds + L_MISC + 16)
    G1H(f0, f1, XA(0, 1), WA(0, 1), X_RDY);
    asm volatile("s_waitcnt vmcnt(0) lgkmcnt(0)" : "+v"(tch) :: "memory");
    BAR();
    XVC(13, 1);
    if (__builtin_expect(!rfl(rdy[0] & rdy[1]), 0)) {
#pragma unroll 1
      for (int spins = 0; spins < SPIN_CAP && !rfl(misc[4] & misc[5]); ++spins) __builtin_amdgcn_s_sleep(1);
    }
    asm volatile("" ::: "memory");
    f32x4 mb[2][2], rd[2][2]; float cs[4];
    unsigned tagv;
#define TB(o) (*reinterpret_cast<const f32x4*>(lds + ta + (o)))
#define X_T1(g) if ((g) >= 4) cs[(g) & 3] = *reinterpret_cast<const float*>(lds + tc + (64 * (((g) & 3) >> 1) + 4 * ((g) & 1)) * 4)
#define X_T2(g) if ((g) == 4) { mb[1][0] = TB(256); mb[1][1] = TB(272); } else if ((g) == 5) tagv = *(const LAS unsigned*)(lds + L_MISC + 8)
    G1H(f1, f0, XA(1, 0), WA(1, 0), X_T1);
    G1H(f0, f1, XA(1, 1), WA(1, 1), X_T2);
    WAIT_LGKM0();
    BAR();
    XVC(14, 2);
    Frag F[4];
#define SB() __builtin_amdgcn_sched_barrier(0)
#define FQ(k, i) F[k].q[i] = LDF(WA(2 + ((k) >> 1), (k) & 1) + (i) * 2048)
#define FP(k, i) F[k].p[i] = LDF(XA(2 + ((k) >> 1), (k) & 1) + (i) * 2048)
#define FA(k, r) do { if ((r) == 0) FQ(k, 0); else if ((r) < 3) FP(k, (r) - 1); else FQ(k, (r) - 2); } while (0)
#pragma unroll
    for (int n = 0; n < 16; ++n) {
      const int j = n >> 2, ii = (j & 1) ? 3 - (n & 3) : n & 3;
      acc[ii][j] = MFMA(f1.p[ii], f1.q[j], acc[ii][j]);
      SB();
      if ((n & 3) != 3) { const int m = n - (n >> 2); FA(m / 6, m % 6); }
      SB();
    }
    BAR();
    XVC(15, 3);
#define VFMA(d, a, c) asm volatile("v_fma_f32 %0, %1, %2, %0" : "+v"(d) : "v"(a), "v"(c))
#define VMUL(d, a) asm volatile("v_mul_f32 %0, %1, %0" : "+v"(d) : "v"(a))
    float cb[2][4][4];
#pragma unroll
    for (int n = 0; n < 32; ++n) {
      const int k = n >> 3, j = (n >> 1) & 3, ii = (n ^ n >> 1) & 1;
      acc[ii][j] = MFMA(F[k].p[ii], F[k].q[j], acc[ii][j]);
      SB();
      if ((n & 3) != 3) { const int m = n - (n >> 2); if (m < 12) FA(2 + m / 6, m % 6); else if (m < 20) FP((m - 12) >> 1, 2 + (m & 1)); }
      if (n == 19) mb[0][0] = TB(0); if (n == 23) mb[0][1] = TB(16); if (n == 26) rd[0][0] = TB(1536); if (n == 27) rd[0][1] = TB(1552);
      {
        const int jj = n >> 3, h = (n >> 2) & 1, r = n & 3;
        cb[h][jj][r] = acc[2 + h][jj][r];
        VFMA(cb[h][jj][r], mb[1][h][r], cs[jj]);
        acc[2 + h][jj][r] = cb[h][jj][r];
      }
      SB();
    }
    WAIT_LGKM0();
    BAR();
    tag = rfl(tagv);
    u32x4 o;
#pragma unroll
    for (int n = 0; n < 32; ++n) {
      const int k = n >> 3, j = (n >> 1) & 3, ii = 2 + ((n ^ n >> 1) & 1);
      acc[ii][j] = MFMA(F[k].p[ii], F[k].q[j], acc[ii][j]);
      SB();
      {
        const int h = (n >> 2) & 1, r = n & 3;
        cb[h][k][r] = acc[h][k][r];
        VFMA(cb[h][k][r], mb[0][h][r], cs[k]);
        if (n) { const int n1 = n - 1; VMUL(cb[(n1 >> 2) & 1][n1 >> 3][n1 & 3], rd[0][(n1 >> 2) & 1][n1 & 3]); }
        if (n > 2 && (n & 1)) { const int n2 = n - 3;
          unsigned o_ = pack2(cb[(n2 >> 2) & 1][n2 >> 3][n2 & 3], cb[(n2 >> 2) & 1][n2 >> 3][(n2 & 3) + 1]); asm volatile("" : "+v"(o_)); o[(n2 >> 1) & 3] = o_; }
        if (n == 20) rd[1][0] = TB(1792); if (n == 22) rd[1][1] = TB(1808);
        if (n > 8 && (n & 7) == 2) {
          __builtin_amdgcn_raw_buffer_store_b128(o, rws, ao[(k - 1) >> 1] + ((k - 1) & 1) * 2048, 0, SC1);
          *reinterpret_cast<u32x4*>(lds + ow[(k - 1) >> 1] + ((k - 1) & 1) * 2048) = o;
        }
      }
      SB();
    }
    STAMP(2);
    VMUL(cb[1][3][3], rd[0][1][3]);
    o[3] = pack2(cb[1][3][2], cb[1][3][3]);
    __builtin_amdgcn_raw_buffer_store_b128(o, rws, ao[1] + 2048, 0, SC1);
    *reinterpret_cast<u32x4*>(lds + ow[1] + 2048) = o;
    asm volatile("" ::: "memory");
#pragma unroll
    for (int j = 0; j < 4; ++j) {
      o = pack8(acc[2][j] * rd[1][0], acc[3][j] * rd[1][1]);
      __builtin_amdgcn_raw_buffer_store_b128(o, rws, ao[j >> 1] + UB + (j & 1) * 2048, 0, SC1);
      *reinterpret_cast<u32x4*>(lds + ow[j >> 1] + SLOT + (j & 1) * 2048) = o;
    }
    STAMP(3);
  }
  f32x4 acb[4][4];
  bf16x8 pa[4], pb[4], q[8];
  int pp = wp * 8192 + lane * 16;
  const int f1o = WS_FLAG1 + (tq1 * 16 + tp1) * 128 + wid * 4;
#define FLAG1(i, n) do { wait_vm<n>(); __builtin_amdgcn_raw_buffer_store_b32(tag, rws, lane ? -1 : f1o + (i) * 32768, 0, SC1); if (i) STAMP(4); __builtin_amdgcn_sched_barrier(0); } while (0)
  auto fl0 = [&](int g) { if (g == 1) FLAG1(0, 4); if (g == 7) FLAG1(1, 1); };
  WAIT_LGKM0();
  BAR();
  STAMP(5);
  pa[0] = LDF(2 * UB + pp); q[0] = LDF(q0); pa[1] = LDF(2 * UB + pp + 2048); pa[2] = LDF(2 * UB + pp + 4096); pa[3] = LDF(2 * UB + pp + 6144);
  q[1] = LDF(q0 + 2048); q[2] = LDF(q0 + 4096); q[3] = LDF(q0 + 6144); q[4] = LDF(q0 + UB);
  __builtin_amdgcn_sched_barrier(0);
  auto none = [](int) {};
  big_half<false, false, true>(acc, acb, pa, pb, q, lds, q0, 2 * UB + pp + 1024, q1, q1 + UB, fl0);
  PIN4(q0, q1, pp, lane);
  {
    int sl = 0;
#pragma unroll 1
    for (int n = 0; n < 12; ++n) {
      const int sn = sl == 2 * SLOT ? 0 : sl + SLOT;
      big_half<true, false, false>(acc, acb, pb, pa, q, lds, sl + q1, sn + 2 * UB + pp, sn + q0, sn + q0 + UB, none);
      big_half<false, false, false>(acc, acb, pa, pb, q, lds, sn + q0, sn + 2 * UB + pp + 1024, sn + q1, sn + q1 + UB, none);
      sl = sn;
    }
  }
  int q0h = q0 + 4 * UB, q1h = q1 + 4 * UB, pph = pp + 5 * UB;
  asm volatile("" : "+v"(q0h), "+v"(q1h), "+v"(pph));
  big_half<true, true, false>(acc, acb, pb, pa, q, lds, q1, pph, q0h, 0, none);
  small_step<false>(acb, pa, pb, q, lds, pph + 1024, q1h, pph + 3 * UB, q0h + 3 * UB, 0);
  small_step<true>(acb, pa, pb, q, lds, pph + 3 * UB + 1024, q1h + 3 * UB, pph, 3 * UB + q0, 3 * UB + q1);
  int xw = WS_XCH + b * XB + wid * 4096 + lane * 16;
  asm volatile("" : "+v"(xw));
#define SEND_E(j) __builtin_amdgcn_raw_buffer_store_b128(pack8(acb[(j) < 4 ? 2 : 0][(j) & 3], acb[(j) < 4 ? 3 : 1][(j) & 3]), rws, xw + ((j) >> 2) * 16384 + ((j) & 3) * 1024, 0, SC1)
  STAMP(10);
  const int f2o = WS_FLAG2 + b * 128 + wid * 4;
#define PA(k) ((k) == 0 ? pph : (k) == 1 ? pph + 3 * UB : (k) == 2 ? pp + 2 * UB : pp + UB)
#define QA0(k) ((k) == 0 ? q0 + 3 * UB : (k) == 1 ? q0h + 2 * UB : (k) == 2 ? q0 : q0h)
#define QA1(k) ((k) == 0 ? q1 + 3 * UB : (k) == 1 ? q1h + 2 * UB : (k) == 2 ? q1 : q1h)
#define JOB(K, I0, pc, qc, LC, pn, KN, IN, qx, LN, EXTRA) do { _Pragma("unroll") for (int g = 0; g < 8; ++g) { \
    if (g < 2 && (LC)) qc[g + 6] = LDF(QA1(K) + (g + 2) * 2048); \
    if ((KN) >= 0) { \
      if (g >= 2 && (LN)) qx[g - 2] = LDF(g < 6 ? QA0(KN) + (g - 2) * 2048 : QA1(KN) + (g - 6) * 2048); \
      if (g < 4) pn[g] = LDF(PA(KN) + ((IN) + (g & 1)) * 2048 + (g >> 1) * 1024); } \
    acc[I0][g & 3] = MFMA(pc[(g >> 2) * 2], qc[g], acc[I0][g & 3]); \
    acc[(I0) + 1][g & 3] = MFMA(pc[(g >> 2) * 2 + 1], qc[g], acc[(I0) + 1][g & 3]); \
    EXTRA(g); \
    SGB(0x8, 1); SGB(0x100, 1); SGB(0x2, 2); SGB(0x8, 1); SGB(0x100, 1); SGB(0x2, 2); \
    __builtin_amdgcn_sched_barrier(0); } \
    if ((KN) < 0) { PIN4(acc[2][0], acc[2][1], acc[2][2], acc[2][3]); PIN4(acc[3][0], acc[3][1], acc[3][2], acc[3][3]); } } while (0)
#define X_NONE(g)
#define X_E0(g) if ((g) >= 1) SEND_E((g) - 1 & 7)
#define X_E1(g) if ((g) == 0) SEND_E(7)
#define X_Z(g) if ((g) >= 1 && (g) < 5) __builtin_amdgcn_raw_buffer_store_b128(pack8(acc[0][((g) - 1) & 3], acc[1][((g) - 1) & 3]), rws, xw + 32768 + (((g) - 1) & 3) * 1024, 0, SC1); \
    else if ((g) == 5) misc[8 + wid] = 1u
#define FLAG2(i) do { wait_vm<0>(); __builtin_amdgcn_raw_buffer_store_b32(tag, rws, lane ? -1 : f2o + (i) * 16, 0, SC1); STAMP((i) ? 1 : 9); } while (0)
  bf16x8 qb[8], qc_[8];
  JOB(0, 0, pa, q, 1, pb, 1, 0, q, 1, X_E0);
  JOB(1, 0, pb, q, 1, pa, 2, 0, qb, 1, X_E1);
  BAR();
  JOB(2, 0, pa, qb, 1, pb, 3, 0, qc_, 1, X_NONE);
  JOB(3, 0, pb, qc_, 1, pa, 3, 2, q, 0, X_NONE);
  FLAG2(0);
  JOB(3, 2, pa, qc_, 0, pb, 2, 2, qb, 0, X_Z);
  JOB(2, 2, pb, qb, 0, pa, 1, 2, q, 0, X_NONE);
  JOB(1, 2, pa, q, 0, pb, 0, 2, qb, 1, X_NONE);
  FLAG2(1);
  float tm[4], tr[4]; unsigned selv; u32x4 sv_;
  const char* tb = lds + L_MB + (wq * 64 + (lane >> 2)) * 4;
#define X_TAB(g) if ((g) >= 2 && (g) < 6) { tm[((g) - 2) & 3] = *reinterpret_cast<const float*>(tb + ((g) - 2) * 64); tr[((g) - 2) & 3] = *reinterpret_cast<const float*>(tb + 512 + ((g) - 2) * 64); } \
  else if ((g) == 6) { selv = misc[0]; sv_ = *(volatile LAS u32x4*)(lds + L_MISC + 32); }
  JOB(0, 2, pb, qb, 1, pa, -1, 0, q, 0, X_TAB);
  STAMP(6);
  if (__builtin_expect(!rfl(sv_[0] & sv_[1] & sv_[2] & sv_[3]), 0)) wait_sent((LAS char*)lds);
  asm volatile("" ::: "memory");
  {
    char* tw = lds + wid * 8192 + (lane & 15) * 64 + (((lane >> 4) ^ (lane >> 1)) & 3) * 16;
    const char* tr_ = lds + wid * 8192 + (lane >> 2) * 64 + ((lane ^ (lane >> 3)) & 3) * 16;
#pragma unroll
    for (int k = 0; k < 8; ++k) *reinterpret_cast<f32x4*>(tw + k * 1024) = acc[2 + (k >> 2)][k & 3];
    asm volatile("" ::: "memory");
#pragma unroll
    for (int k = 0; k < 8; ++k) acc[2 + (k >> 2)][k & 3] = *reinterpret_cast<const f32x4*>(tr_ + k * 1024);
    __builtin_amdgcn_sched_barrier(0);
  }
  PIN4(xv[0], xv[1], xv[2], xv[3]);
  f32x2 gf[4][4];
#pragma unroll
  for (int k = 0; k < 4; ++k) {
    const float tn = tm[k] * tr[k];
#pragma unroll
    for (int c = 0; c < 4; ++c) {
      const f32x2 n = f32x2{lo2f(xv[k][c]), hi2f(xv[k][c])} * tr[k] + tn, a = n * -1.44269504f;
      const f32x2 d = f32x2{__builtin_amdgcn_exp2f(a[0]), __builtin_amdgcn_exp2f(a[1])} + 1.0f;
      gf[k][c] = n * f32x2{__builtin_amdgcn_rcpf(d[0]), __builtin_amdgcn_rcpf(d[1])};
    }
    PIN4(gf[k][0], gf[k][1], gf[k][2], gf[k][3]);
  }
  f32x2 xf[4][4];
#pragma unroll
  for (int k = 0; k < 4; ++k) {
#pragma unroll
    for (int c = 0; c < 4; ++c) xf[k][c] = f32x2{lo2f(xv[k][c]), hi2f(xv[k][c])};
    PIN4(xf[k][0], xf[k][1], xf[k][2], xf[k][3]);
  }
  {
    unsigned sel = rfl(selv);
    if (__builtin_expect(!sel || (int)(sel - 1 >> 6) == tp1 * 2 + wq, 0)) {
#pragma unroll 1
      for (int spins = 0; spins < SPIN_CAP && !sel; ++spins) { sel = rfl(misc[0]); __builtin_amdgcn_s_sleep(2); }
      const int idx = (int)sel - 1;
      if ((idx >> 6) == tp1 * 2 + wq) {
        const int ks = (idx >> 4) & 3;
        const u32x4 xs = ks == 0 ? xv[0] : ks == 1 ? xv[1] : ks == 2 ? xv[2] : xv[3];
        const float ms = ks == 0 ? tm[0] : ks == 1 ? tm[1] : ks == 2 ? tm[2] : tm[3], rs = ks == 0 ? tr[0] : ks == 1 ? tr[1] : ks == 2 ? tr[2] : tr[3];
        u32x4 n;
#pragma unroll
        for (int c = 0; c < 4; ++c) n[c] = ln_pair(xs[c], ms, rs);
        asm volatile("" : "+v"(n));
#pragma unroll 1
        for (int spins = 0; spins < SPIN_CAP && !rfl(misc[1]); ++spins) __builtin_amdgcn_s_sleep(1);
        asm volatile("" ::: "memory");
        const LAS float* upd = (const LAS float*)(lds + L_UPD) + wp * 32 + (lane & 3) * 8;
#pragma unroll
        for (int c = 0; c < 4; ++c) {
          const unsigned u = pack2(lo2f(n[c]) + upd[2 * c], hi2f(n[c]) + upd[2 * c + 1]);
          const f32x2 gn = {silu_fast(lo2f(u)), silu_fast(hi2f(u))};
#pragma unroll
          for (int k = 0; k < 4; ++k) if (ks == k && (idx & 15) == (lane >> 2)) gf[k][c] = gn;
        }
      }
    }
  }
  int xo = ((tp1 * 128 + wq * 64 + (lane >> 2)) * D + te * 128 + z * 64 + wp * 32 + (lane & 3) * 8) * 2, le = wid * 4096 + (lane & 3) * 256 + ((lane >> 2) ^ (lane & 3) * 4) * 16 + 4 * UB;
  const unsigned one = (lane >> 4) == ((lane >> 2) & 3) ? (lane & 1 ? 0x3f800000u : 0x3f80u) : 0u;
  u32x4 s0 = {lane & 2 ? 0u : one, lane & 2 ? one : 0u, 0u, 0u}, s1 = {0u, 0u, s0[0], s0[1]};
  PIN4(xo, le, s0, s1);
  STAMP(7);
#pragma unroll 1
  for (int spins = 0; spins < SPIN_CAP; ++spins) { const u32x2 f = *(volatile LAS u32x2*)(lds + L_MISC + 48); if (rfl(f[0] & f[1])) break; }
  asm volatile("" ::: "memory");
  STAMP(11);
  bf16x8 pc[3][4];
#pragma unroll
  for (int k = 0; k < 4; ++k)
#pragma unroll
    for (int i = 0; i < 3; ++i) pc[i][k] = LDF(le + (i ? i + 1 : 0) * UB + k * 1024);
  __builtin_amdgcn_sched_barrier(0);
#pragma unroll
  for (int k = 0; k < 4; ++k)
#pragma unroll
    for (int i = 0; i < 3; ++i) { acc[2][k] = MFMA(BF8(s0), pc[i][k], acc[2][k]); acc[3][k] = MFMA(BF8(s1), pc[i][k], acc[3][k]); }
#pragma unroll
  for (int k = 0; k < 4; ++k) {
    u32x4 ov;
#pragma unroll
    for (int c = 0; c < 4; ++c) {
      const f32x2 r = f32x2{acc[2 + (c >> 1)][k][(c & 1) * 2], acc[2 + (c >> 1)][k][(c & 1) * 2 + 1]} * gf[k][c] + xf[k][c];
      ov[c] = pack2(r[0], r[1]);
    }
    __builtin_amdgcn_raw_buffer_store_b128(ov, rout, xo + k * 16 * D * 2, 0, SC1);
  }
  STAMP(8);
  STAMP_OUT();
}

#define BCAST(v, k) ((unsigned)__builtin_amdgcn_mov_dpp((int)(v), 0x150 + (k), 0xf, 0xf, true))
template <int K0, int K1>
__device__ __forceinline__ void expand(u32x4 (&dg)[16], unsigned xs, const unsigned (&sel)[4]) {
  if constexpr (K0 < K1) {
    const unsigned v = BCAST(xs, K0);
    dg[K0] = u32x4{__builtin_amdgcn_perm(0u, v, sel[0]), __builtin_amdgcn_perm(0u, v, sel[1]), __builtin_amdgcn_perm(0u, v, sel[2]), __builtin_amdgcn_perm(0u, v, sel[3])};
    asm volatile("" : "+v"(dg[K0]));
    expand<K0 + 1, K1>(dg, xs, sel);
  }
}
template <int K0, int K1, int N>
__device__ __forceinline__ void gemv(f32x4& ae, f32x4& ao, unsigned xs, const unsigned (&sel)[4], const u32x4 (&w)[N]) {
  if constexpr (K0 < K1) {
    const unsigned v = BCAST(xs, K0);
    const bf16x8 dg = BF8((u32x4{__builtin_amdgcn_perm(0u, v, sel[0]), __builtin_amdgcn_perm(0u, v, sel[1]),
                                 __builtin_amdgcn_perm(0u, v, sel[2]), __builtin_amdgcn_perm(0u, v, sel[3])}));
    ae = MFMA(dg, BF8(w[2 * K0]), ae); ao = MFMA(dg, BF8(w[2 * K0 + 1]), ao);
    gemv<K0 + 1, K1, N>(ae, ao, xs, sel, w);
  }
}

__device__ __forceinline__ void helper(int hw, int lane, int b, char* lds, rsrc_t rws, rsrc_t rx, const u16* wr,
                                       const u16* wgu, const u16* wdn, const u16* gq, const char* code, int tp1, int tq1 STAMP_ARG) {
  STAMP_DECL;
  int nb = 0, lim = 14;
  unsigned tch = 0;
  volatile LAS unsigned* misc = (volatile LAS unsigned*)(lds + L_MISC);
  __builtin_amdgcn_s_setprio(3);
  const int z = tq1 >> 3, te = tq1 & 7;
  const int lc = lane & 15, lq = lane >> 4;
  const int r1 = rot1(tp1, tq1), kb = r1 >> 2, amo = WS_AMAX + (tq1 * 16 + tp1) * 128;
#define JOIN() do { HPRE(); BAR(); HSTAMP(); ++nb; } while (0)
#define NEXT() do { if (nb < lim) JOIN(); } while (0)
#define UNTIL(m) do { _Pragma("unroll 1") while (nb < (m)) JOIN(); } while (0)
#define POLLVM(m) do { _Pragma("unroll 1") while (vm_now() > (m) && nb < lim) JOIN(); wait_vm<m>(); } while (0)
  unsigned tag, key, sg; int idx;
  float sw;
  const int rg = b / 28, cg = b - rg * 28;
  const int fg = b >> 3, eg = b & 7;
  u32x4 w[32], wd[14];
  const rsrc_t rgu = mk_rsrc(wgu + (size_t)(rg & 7) * 128 * F + cg * 128, b < 224 ? 128 * F * 2 : 0);
  const rsrc_t rd = mk_rsrc(wdn + (size_t)(fg * 112 + hw * 56) * D + eg * 128, 56 * D * 2);
  const int wvo = lq * 32 * F * 2 + lc * 16, dvo = lq * 14 * D * 2 + lc * 16;
#define LOADW(k0, k1) do { _Pragma("unroll") for (int k = k0; k < (k1); ++k) \
    w[k] = __builtin_amdgcn_raw_buffer_load_b128(rgu, wvo, k * F * 2, NT); asm volatile("" ::: "memory"); } while (0)
#define LOADD(k0, k1) do { _Pragma("unroll") for (int k = k0; k < (k1); ++k) \
    wd[k] = __builtin_amdgcn_raw_buffer_load_b128(rd, dvo, k * D * 2, NT); asm volatile("" ::: "memory"); } while (0)
  const rsrc_t rq = mk_rsrc(gq, QROWS * S * 2);
  const unsigned kt = ktab(tp1);
  const int trow = (te >> 2) * 1024 + (te & 3) * 32 + hw * 16;
  const int t1o = (trow + ((lane >> 1) & 15)) * (S * 2) + (lane & 1) * 128;
#define QTOUCH(o, so) asm volatile("buffer_load_dword %0, %1, %2, %3 offen" : "+v"(tch) : "v"(o), "s"(rq), "s"(so) : "memory")
#define TCH(m) do { _Pragma("unroll") for (int i_ = 0; i_ < 8; ++i_) if ((m) >> i_ & 1) { \
      if (i_ < 7) QTOUCH(t1o, kstep0(kt, 2 * i_) * 128); \
      else QTOUCH(lane & 16 ? ((tp1 >> 3) * 1024 + te * 16 + lc) * (S * 2) + (kstep(kt, 15) ^ hw * 16) * 128 : (trow + lc) * (S * 2) + kstep(kt, 14) * 128, 0); } } while (0)
#define TCH7(i) do { if (te == 7) { QTOUCH((lane >> 1) * (S * 2) + (lane & 1) * 128, (((i) >> 1) * 1024 + hw * 64 + ((i) & 1) * 32) * (S * 2) + kstep0(kt, 4) * 128); } } while (0)
  {
    const rsrc_t rr = mk_rsrc(wr, D * 2);
    u32x4 wf[8];
#pragma unroll
    for (int i = 0; i < 8; ++i) wf[i] = __builtin_amdgcn_raw_buffer_load_b128(rr, lq * 16 + (i & 3) * 64, ((r1 + (i >> 2) * 2) & 15) * 128, 0);
    const unsigned epoch = __builtin_amdgcn_raw_buffer_load_b32(rws, lane == 1 ? (hw ? amo : WS_FLAG2 + b * 128) : lane == 2 ? amo + (WS_FLAG1 - WS_AMAX) + hw * 32768 : lane == 3 ? WS_H + b * 64 : WS_EPOCH, 0, SC1);
    const u32x4 ra = __builtin_amdgcn_raw_buffer_load_b128(rr, lane * 16, 0, 0), rb2 = __builtin_amdgcn_raw_buffer_load_b128(rr, lane * 16 + 1024, 0, 0);
    misc[lc] = 0u;
    u32x4 ones = {0x3f803f80u, 0x3f803f80u, 0x3f803f80u, 0x3f803f80u};
    const int sx = (te >> 1) * 32 + hw * 16, sv = (tp1 >> 1 & 3) * 32 + hw * 16;
    int xa[2], wa[2];
    xa[0] = (sx + lc) * 128 + (lq ^ (lc & 7)) * 16; wa[0] = xa[0] + (sv - sx) * 128 + UB;
    xa[1] = xa[0] ^ 64; wa[1] = wa[0] ^ 64;
    asm volatile("" : "+v"(ones));
    f32x4 d1 = {0.f, 0.f, 0.f, 0.f}, d2 = d1, d3 = d1, d4 = d1;
    HST(9);
#pragma unroll
    for (int u = 0; u < 4; ++u) {
      if (u == 2) { wa[0] += 4 * UB; wa[1] += 4 * UB; }
      if (u == 3) { xa[0] += 7 * UB; xa[1] += 7 * UB; }
      PIN4(xa[0], xa[1], wa[0], wa[1]);
      WAIT_LGKM0();
      JOIN();
      const unsigned tj = (unsigned)__builtin_amdgcn_s_memrealtime();
      bf16x8 xf[2], vf[2];
#pragma unroll
      for (int h = 0; h < 2; ++h) { xf[h] = LDF(xa[h] + (u == 3 ? 0 : XI(u) * UB)); vf[h] = LDF(wa[h] + (u & 1) * 3 * UB); }
      __builtin_amdgcn_sched_barrier(0);
      if (u) LOADW((int)(SWT >> 8 * u & 63), (int)(SWT >> 8 * u + 8 & 63));
      if (u) TCH7(u - 1);
#pragma unroll
      for (int h = 0; h < 2; ++h) {
        d2 = MFMA(xf[h], xf[h], d2); d4 = MFMA(BF8(ones), xf[h], d4); d3 = MFMA(BF8(ones), vf[h], d3); d1 = MFMA(BF8(wf[2 * u + h]), xf[h], d1);
      }
      PIN4(d1, d2, d3, d4);
      if (u == 0) {
        tag = mk_tag(epoch);
        asm volatile("" : "+s"(tag));
        misc[2] = tag;
        const f32x4 d5 = MFMA(BF8(rb2), BF8(ones), MFMA(BF8(ra), BF8(ones), (f32x4{0.f, 0.f, 0.f, 0.f})));
        const int s4 = __builtin_bit_cast(int, (d5[0] + d5[1]) + (d5[2] + d5[3]));
#define RL(l) __builtin_bit_cast(float, __builtin_amdgcn_readlane(s4, l))
        sw = (RL(0) + RL(16)) + (RL(32) + RL(48));
        asm volatile("" : "+v"(sw));
        LOADW(0, (int)(SWT >> 8 & 63));
      }
      if (PI >> 8 * u & 255) {
#pragma unroll 1
        while ((unsigned)__builtin_amdgcn_s_memrealtime() - tj < (PI >> 8 * u & 255)) ;
      }
      __builtin_amdgcn_sched_barrier(0);
    }
    JOIN();
    const float sq = (lc & 2) ? ((lc & 1) ? d2[3] : d2[2]) : ((lc & 1) ? d2[1] : d2[0]);
    __builtin_amdgcn_raw_buffer_store_b128(u32x4{__float_as_uint(d4[0]), __float_as_uint(sq), __float_as_uint(d1[0]), tag}, rws,
                                           lq == lc >> 2 ? WS_XP + (tp1 * 4 + kb) * 2048 + (sx + lc) * 16 : -1, 0, SC1);
    __builtin_amdgcn_raw_buffer_store_b64(u32x2{__float_as_uint(d3[0]), tag}, rws, lane < 16 ? WS_WP + (tq1 * 4 + kb) * 1024 + (sv + lc) * 8 : -1, 0, SC1);
    __builtin_amdgcn_sched_barrier(0);
    LOADW((int)(SWT >> 32 & 63), (int)(SWT >> 40 & 63));
  }
  const int srow = hw * 32 + (lane & 3) + 2 * (lane & 44) + ((lane >> 2) & 4);
  {
    const int gxo = WS_XP + tp1 * 8192 + (hw * 64 + lane) * 16, gwo = WS_WP + tq1 * 4096 + (hw * 64 + lane) * 8;
    u32x4 gx[4]; u32x2 gw[4];
#define GATHER() asm volatile("buffer_load_dwordx4 %0, %8, %10, 0 offen sc1\n\tbuffer_load_dwordx4 %1, %8, %10, 0 offen offset:2048 sc1\n\t" \
                 "buffer_load_dwordx4 %2, %8, %10, %11 offen sc1\n\tbuffer_load_dwordx4 %3, %8, %10, %11 offen offset:2048 sc1\n\t" \
                 "buffer_load_dwordx2 %4, %9, %10, 0 offen sc1\n\tbuffer_load_dwordx2 %5, %9, %10, 0 offen offset:1024 sc1\n\t" \
                 "buffer_load_dwordx2 %6, %9, %10, 0 offen offset:2048 sc1\n\tbuffer_load_dwordx2 %7, %9, %10, 0 offen offset:3072 sc1" \
                 : "=&v"(gx[0]), "=&v"(gx[1]), "=&v"(gx[2]), "=&v"(gx[3]), "=&v"(gw[0]), "=&v"(gw[1]), "=&v"(gw[2]), "=&v"(gw[3]) \
                 : "v"(gxo), "v"(gwo), "s"(rws), "s"(4096) : "memory")
    UNTIL(sched(T1_T, te));
    GATHER();
    LOADW((int)(SWT >> 40), NWC);
    wait_vm<NWC - (int)(SWT >> 40)>();
    TCH7(3);
#pragma unroll 1
    for (int spins = 0;; ++spins) {
      asm volatile("" : "+v"(gx[0]), "+v"(gx[1]), "+v"(gx[2]), "+v"(gx[3]), "+v"(gw[0]), "+v"(gw[1]), "+v"(gw[2]), "+v"(gw[3]) :: "memory");
      unsigned bad = 0;
#pragma unroll
      for (int q = 0; q < 4; ++q) bad |= (gx[q][3] ^ tag) | (gw[q][1] ^ tag);
      if (__builtin_expect(__all(bad == 0) || spins > SPIN_CAP / 64, 1)) break;
      GATHER();
      NEXT();
      POLLVM(0);
    }
    HST(3);
#define SUM4(v, c) ((__uint_as_float(v[0][c]) + __uint_as_float(v[1][c])) + (__uint_as_float(v[2][c]) + __uint_as_float(v[3][c])))
    float sum = SUM4(gx, 0), sq = SUM4(gx, 1), dot = SUM4(gx, 2);
    *(LAS float*)(lds + L_MB + 1024 + srow * 4) = SUM4(gw, 0);
    asm volatile("" : "+v"(sum), "+v"(sq), "+v"(dot));
    const float mn = sum * (1.0f / D);
    const float var = fmaxf(fmaf(-mn, mn, sq * (1.0f / D)), 0.f);
    const float mbv = rbf(mn), den = rbf(__builtin_amdgcn_sqrtf(rbf(rbf(var) + 1e-5f))), rden = __builtin_amdgcn_rcpf(den);
    *(LAS float*)(lds + L_MB + srow * 4) = -mbv;
    *(LAS float*)(lds + L_MB + 512 + srow * 4) = rden;
    *(LAS float*)(lds + L_MB + 1536 + srow * 4) = z ? -rden : rden;
    asm volatile("" ::: "memory");
    misc[4 + hw] = 1u;
    float rs = (dot - mbv * sw) * rden;
    sg = pack2(mbv, den);
    asm volatile("" : "+v"(rs), "+v"(sg));
    lim = NBAR;
    const unsigned rb = pack2(rs, rs) & 0xffffu;
    unsigned srt = (rb & 0x8000u) ? (~rb & 0xffffu) : (rb | 0x8000u);
    if (rs != rs) srt = 0u;
    key = (srt << 16) | (unsigned)(0xffff - tp1 * 128 - srow);
    const unsigned mk = wave_maxu(key);
    if (key == mk) __builtin_amdgcn_raw_buffer_store_b128(u32x4{key, tag, sg, tag}, rws, amo + hw * 16, 0, SC1);
    HST(2);
    UNTIL(WB_AT + (WBS >> 4 * te & 3));
    TCH(3);
    LOADW(NWC, 32);
  }
  unsigned sel[4];
#pragma unroll
  for (int d = 0; d < 4; ++d) {
    const unsigned src = (lc & 2) ? 0x0302u : 0x0100u;
    sel[d] = (lc >> 2) == d ? ((lc & 1) ? (src << 16 | 0x0c0cu) : (0x0c0c0000u | src)) : 0x0c0c0c0cu;
  }
  PIN4(sel[0], sel[1], sel[2], sel[3]);
  JOIN();
  const int hc = fg * 112 + hw * 56 + (lq * 7 + min(lc, 6)) * 2;
  int hio = WS_H + (hc & ~127) * 4 + ((hc & 6) * 8 + ((hc >> 3) & 15)) * 8;
  const int hs = min(b, 223) * 8 + hw * 4 + ((lane >> 3) & 3);
  int guo = WS_GU + ((lane & 7) * 2 + (lane >> 5)) * F * 8 + hs * 16, hoo = b < 224 && !(lane & 39) ? WS_H + hs * 8 : -1;
  asm volatile("" : "+v"(hio), "+v"(hoo));
  int gao = WS_AMAX + tq1 * 2048 + ((lane >> 1) & 15) * 128 + (lane & 1) * 16, gso = b < 224 ? WS_GU + ((rg * 2 + hw) * F + cg * 128) * 8 + lane * 16 : -1;
  asm volatile("" : "+v"(guo), "+v"(gao), "+v"(gso));
  UNTIL(15);
#pragma unroll 1
  for (int spins = 0;; ++spins) {
    const u32x4 am = __builtin_amdgcn_raw_buffer_load_b128(rws, gao, 0, SC1);
    NEXT();
    UNTIL(17);
#pragma unroll 1
    while (vm_now() > 0 && nb < lim && nb != 19 && nb != 17) JOIN();
    key = am[0]; sg = am[2];
    if (__builtin_expect(__all(((am[1] ^ tag) | (am[3] ^ tag)) == 0) || spins > SPIN_CAP / 64, 1)) break;
  }
  {
    const unsigned mk = wave_maxu(key);
    sg = __builtin_amdgcn_readlane(sg, __builtin_ctzll(__ballot(key == mk)));
    key = mk;
  }
  idx = min(0xffff - (int)(key & 0xffffu), S - 1);
  const unsigned xe = __builtin_amdgcn_raw_buffer_load_b32(rx, (idx * D + (rg & 7) * 128) * 2 + lane * 4, 0, 0);
  misc[0] = idx + 1;
  HST(7);
  if (nb != 19) NEXT();
#pragma unroll 1
  while (vm_now() > 0 && nb < lim && nb != 19) JOIN();
  {
    const unsigned xs = ln_pair(xe, -lo2f(sg), __builtin_amdgcn_rcpf(hi2f(sg)));
    if (b == 0 && lane == 0 && hw == 1) __builtin_amdgcn_raw_buffer_store_b32(tag, rws, WS_EPOCH, 0, SC1);
    u32x4 dg[16];
    expand<0, 7>(dg, xs, sel);
    if (nb != 19) NEXT();
    expand<7, 16>(dg, xs, sel);
    UNTIL(19);
    const bool idle = nb == 19;
    if (!idle) NEXT();
    f32x4 ae = {0.f, 0.f, 0.f, 0.f}, ao = ae;
#pragma unroll
    for (int k = 0; k < 16; ++k) {
      ae = MFMA(BF8(dg[k]), BF8(w[2 * k]), ae); ao = MFMA(BF8(dg[k]), BF8(w[2 * k + 1]), ao);
      if (k == 7) { asm volatile("" : "+v"(ae), "+v"(ao)); if (__builtin_expect(!idle, 0)) NEXT(); }
    }
    __builtin_amdgcn_raw_buffer_store_b128(u32x4{__float_as_uint(ae[0] + ao[2]), tag, __float_as_uint(ae[1] + ao[3]), tag}, rws, gso, 0, SC1);
  }
  HST(4);
  if (nb == 19) JOIN();
  {
    const int m0_ = 20 + (te & 1);
#define LATE(i) do { UNTIL(m0_ + (i)); __builtin_amdgcn_s_sleep(BSL); } while (0)
    LATE(0);
    TCH(0x18);
    LATE(2);
    u32x4 gu;
    asm volatile("buffer_load_dwordx4 %0, %1, %2, 0 offen sc1" : "=&v"(gu) : "v"(guo), "s"(rws) : "memory");
    TCH(0x20);
    LOADD(0, WD1);
    NEXT();
    asm volatile("s_waitcnt vmcnt(%1)" : "+v"(gu) : "n"(WD1 + 1) : "memory");
#pragma unroll 1
    for (int spins = 0; !__all(((gu[1] ^ tag) | (gu[3] ^ tag)) == 0) && spins < SPIN_CAP / 64; ++spins) {
      asm volatile("buffer_load_dwordx4 %0, %1, %2, 0 offen sc1" : "=&v"(gu) : "v"(guo), "s"(rws) : "memory");
      NEXT();
      POLLVM(0);
      asm volatile("" : "+v"(gu) :: "memory");
    }
    {
      float s0 = __uint_as_float(gu[0]), s1 = __uint_as_float(gu[2]);
      DPP_ADD(s0, 0xb1, 0xf, true); DPP_ADD(s1, 0xb1, 0xf, true); DPP_ADD(s0, 0x4e, 0xf, true); DPP_ADD(s1, 0x4e, 0xf, true);
      DPP_ADD(s0, 0x141, 0xf, true); DPP_ADD(s1, 0x141, 0xf, true);
      const unsigned gp = pack2(s0, s1);
      const unsigned up = __builtin_amdgcn_permlane32_swap(gp, gp, false, false)[1];
      const unsigned sl2 = pack2(silu_fast(lo2f(gp)), silu_fast(hi2f(gp)));
      __builtin_amdgcn_raw_buffer_store_b64(u32x2{pack2(lo2f(sl2) * lo2f(up), hi2f(sl2) * hi2f(up)), tag}, rws, hoo, 0, SC1);
    }
    HST(8);
    LATE(4);
    TCH(0x40);
    LOADD(WD1, WD2);
    if (!hw && lane < (CODE_LINES + 7) / 8) {
      const int co = min(((b >> 3) & 7) * ((CODE_LINES + 7) / 8) + lane, CODE_LINES - 1) * 128;
      asm volatile("global_load_dword %0, %1, off" : "+v"(tch) : "v"(code + co) : "memory");
    }
    LATE(6);
    u32x2 hv;
    asm volatile("buffer_load_dwordx2 %0, %1, %2, 0 offen sc1" : "=&v"(hv) : "v"(hio), "s"(rws) : "memory");
    TCH(0x80);
    LOADD(WD2, 14);
    NEXT();
    asm volatile("s_waitcnt vmcnt(%1)" : "+v"(hv) : "n"(14 - WD2 + 1) : "memory");
#pragma unroll 1
    for (int spins = 0; !__all(hv[1] == tag) && spins < SPIN_CAP / 64; ++spins) {
      asm volatile("buffer_load_dwordx2 %0, %1, %2, 0 offen sc1" : "=&v"(hv) : "v"(hio), "s"(rws) : "memory");
      NEXT();
      POLLVM(0);
      asm volatile("" : "+v"(hv) :: "memory");
    }
    f32x4 ae = {0.f, 0.f, 0.f, 0.f}, ao = ae;
    gemv<0, 5>(ae, ao, hv[0], sel, wd);
    asm volatile("" : "+v"(ae), "+v"(ao));
    NEXT();
    POLLVM(0);
    gemv<5, 7>(ae, ao, hv[0], sel, wd);
    f32x2 a = {ae[0] + ao[2], ae[1] + ao[3]};
    HST(5);
    if (hw == 1) {
      *(LAS f32x2*)(lds + L_CMB + lane * 8) = a;
      asm volatile("" ::: "memory");
      misc[6] = 1u;
    } else {
      f32x2 o;
#pragma unroll 1
      for (int spins = 0; spins < SPIN_CAP; ++spins) {
        NEXT();
        const unsigned f = misc[6];
        o = *(volatile LAS f32x2*)(lds + L_CMB + lane * 8);
        if (rfl(f)) break;
      }
      __builtin_amdgcn_raw_buffer_store_b128(u32x4{__float_as_uint(a[0] + o[0]), tag, __float_as_uint(a[1] + o[1]), tag}, rws,
                                             WS_PART + (fg * D + eg * 128) * 8 + lane * 16, 0, SC1);
      HST(6);
    }
  }
  if (hw == 1 && (idx >> 7) == tp1) {
    const unsigned srt = key >> 16;
    const float r = lo2f((srt & 0x8000u) ? (srt & 0x7fffu) : (~srt & 0xffffu));
    const float rsel = rbf(1.0f / (1.0f + __expf(-r)));
    const int cp = lane & 31;
    const int e = z * 64 + cp * 2;
    const int po = WS_PART + ((lane >> 5) * D + te * 128) * 8 + ((e & 6) * 8 + (e >> 3)) * 16;
    float y0, y1;
    UNTIL(NBAR);
    wait_sent((LAS char*)lds);
#pragma unroll 1
    for (int spins = 0;; ++spins) {
#pragma unroll
      for (int k = 0; k < 16; ++k) w[k] = __builtin_amdgcn_raw_buffer_load_b128(rws, po, k * 2 * D * 8, SC1);
      asm volatile("" ::: "memory");
      unsigned bad = 0;
      y0 = 0.f; y1 = 0.f;
#pragma unroll
      for (int k = 0; k < 16; ++k) { bad |= (w[k][1] ^ tag) | (w[k][3] ^ tag); y0 += __uint_as_float(w[k][0]); y1 += __uint_as_float(w[k][2]); }
      if (__all(bad == 0) || spins > SPIN_CAP / 64) break;
    }
    { const auto p0 = __builtin_amdgcn_permlane32_swap(__float_as_uint(y0), __float_as_uint(y0), false, false);
      const auto p1 = __builtin_amdgcn_permlane32_swap(__float_as_uint(y1), __float_as_uint(y1), false, false);
      y0 = __uint_as_float(p0[0]) + __uint_as_float(p0[1]); y1 = __uint_as_float(p1[0]) + __uint_as_float(p1[1]); }
    *(LAS f32x2*)(lds + L_UPD + cp * 8) = f32x2{rbf(rbf(y0) * rsel), rbf(rbf(y1) * rsel)};
    asm volatile("" ::: "memory");
    misc[1] = 1u;
    HST(6);
  }
  HST(10);
  POLLVM(0);
  asm volatile("" : "+v"(tch));
  STAMP_OUT();
}

typedef __attribute__((ext_vector_type(8))) u64 u64x8;
typedef __attribute__((ext_vector_type(2))) u64 u64x2;
__global__ __launch_bounds__(512, 2) void fused(const u16*, const u16*, const u16*, const u16*, char*, const u16*, const u16*, const u16*,
                                                const u16*, const u16*, u16*) {
  extern __shared__ __attribute__((aligned(16))) char lds[];
  u64x8 a0; u64x2 a1; u64 a2;
  u64 pc;
  asm volatile("s_load_dwordx16 %0, %4, 0\n\ts_load_dwordx4 %1, %4, 64\n\ts_load_dwordx2 %2, %4, 80\n\ts_getpc_b64 %3"
               : "=&s"(a0), "=&s"(a1), "=&s"(a2), "=&s"(pc) : "s"(__builtin_amdgcn_kernarg_segment_ptr()) : "memory");
  __builtin_amdgcn_sched_barrier(0);
  const char* code = (const char*)(pc & ~255ull);
  const int wid = __builtin_amdgcn_readfirstlane(threadIdx.x >> 6), lane = threadIdx.x & 63;
  const int b = blockIdx.x, g = b & 7, i = b >> 3;
  const int tp1 = (g & 3) * 4 + (i & 3), tq1 = (g >> 2) * 8 + (i >> 2);
  const int z = tq1 >> 3, te = tq1 & 7;
  const int L = wid & 1, rsub = lane >> 3;
  int v1 = ((L ? te : tp1) * 128 + 8 * (rsub >> 2) + (rsub & 3)) * (D * 2) + ((lane & 7) ^ rsub) * 16, ko = rot1(tp1, tq1) * 128;
  unsigned g1n = (L ? D : S) * D * 2;
  int role = wid >> 1;
  asm volatile("" : "+v"(v1));
  asm volatile("" : "+s"(ko), "+s"(g1n), "+s"(role));
  __builtin_amdgcn_sched_barrier(0);
  asm volatile("s_waitcnt lgkmcnt(0)" : "+s"(a0), "+s"(a1), "+s"(a2) :: "memory");
  const u16 *x = (const u16*)a0[0], *wr = (const u16*)a0[3], *sre = (const u16*)a0[5], *sim = (const u16*)a0[6], *wg = (const u16*)a0[7];
  const u16 *wu = (const u16*)a1[0], *wdn = (const u16*)a1[1];
  char* ws = (char*)a0[4];
  u16* out = (u16*)a2;
  const rsrc_t rws = mk_rsrc(ws, WS_END);
  if (__builtin_expect(role == 2, 1)) {
    loader(L, lane, (LAS char*)lds, mk_rsrc((const void*)(L ? (z ? a0[2] : a0[1]) : a0[0]), g1n), v1, ko, (z ? sim : sre) + (size_t)(tp1 & 7) * 128 * S,
           ws + WS_ABT + (size_t)tq1 * ABT_ST, rws, WS_FLAG1 + tq1 * 2048, b, tp1 STAMP_PASS);
  } else if (wid < 4) {
    consumer(wid, lane, b, lds, rws, mk_rsrc(out, S * D * 2), code, tp1, tq1 STAMP_PASS);
  } else {
    const int hw = wid - 6;
    helper(hw, lane, b, lds, rws, mk_rsrc(x, S * D * 2), wr, hw ? wu : wg, wdn,
           (z ? sim : sre) + (size_t)(tp1 & 7) * 128 * S, code, tp1, tq1 STAMP_PASS);
  }
}

extern "C" void kernel_launch(void* const* d_in, const int* in_sizes, int n_in, void* d_out, int out_size,
                              void* d_ws, size_t ws_size, hipStream_t stream) {
  (void)hipFuncSetAttribute((const void*)fused, hipFuncAttributeMaxDynamicSharedMemorySize, LDS_BYTES);
  fused<<<256, 512, LDS_BYTES, stream>>>((const u16*)d_in[0], (const u16*)d_in[3], (const u16*)d_in[4], (const u16*)d_in[6], (char*)d_ws,
                                         (const u16*)d_in[1], (const u16*)d_in[2], (const u16*)d_in[7], (const u16*)d_in[8],
                                         (const u16*)d_in[9], (u16*)d_out);
}
```

```cpp
#define CODE_LINES 276
#include <hip/hip_runtime.h>
#include <hip/hip_bf16.h>
#include <stdint.h>

typedef __attribute__((ext_vector_type(8))) short bf16x8;
typedef __attribute__((ext_vector_type(4))) float f32x4;
typedef __attribute__((ext_vector_type(2))) float f32x2;
typedef __attribute__((ext_vector_type(4))) unsigned u32x4;
typedef __attribute__((ext_vector_type(2))) unsigned u32x2;
typedef __attribute__((ext_vector_type(2))) __bf16 bf16x2;
typedef unsigned short u16;
typedef unsigned long long u64;
typedef __amdgpu_buffer_rsrc_t rsrc_t;
#define LAS __attribute__((address_space(3)))

constexpr int S = 2048, D = 1024, F = 3584;
constexpr int UB = 16384;
constexpr int SLOT = 3 * UB;
constexpr int L_MISC = 9 * UB;
constexpr int L_UPD = L_MISC + 64;
constexpr int L_MB = L_MISC + 512;
constexpr int L_CMB = L_MB + 2048;
constexpr int LDS_BYTES = L_CMB + 512;
constexpr int NBAR = 17 + 14 + 3;
constexpr int SC1 = 16, NT = 2;

constexpr unsigned WS_EPOCH = 0;
constexpr unsigned WS_XP = 4096;
constexpr unsigned WS_WP = WS_XP + 16 * 8192;
constexpr unsigned WS_AMAX = WS_WP + 16 * 4096;
constexpr unsigned WS_FLAG1 = WS_AMAX + 32768;
constexpr unsigned WS_FLAG2 = WS_FLAG1 + 65536;
constexpr unsigned WS_GU = WS_FLAG2 + 32768;
constexpr unsigned WS_H = WS_GU + 8 * 2 * F * 8;
constexpr unsigned WS_PART = WS_H + 16384;
constexpr unsigned WS_XCH = WS_PART + 32 * D * 8;
constexpr int XB = 49152;
constexpr unsigned ABT_ST = 128 * S * 2;
constexpr unsigned WS_ABT = WS_XCH + 256 * XB;
constexpr unsigned WS_END = WS_ABT + 16 * ABT_ST;

#define STAMP(i)
#define HSTAMP()
#define HPRE()
#define HST(i)
#define STAMP_DECL
#define STAMP_ARG
#define STAMP_PASS
#define STAMP_OUT()

__device__ __forceinline__ float lo2f(unsigned v) { return __uint_as_float(v << 16); }
__device__ __forceinline__ float hi2f(unsigned v) { return __uint_as_float(v & 0xffff0000u); }
__device__ __forceinline__ unsigned pack2(float a, float b) {
  return __builtin_bit_cast(unsigned, __builtin_convertvector(f32x2{a, b}, bf16x2));
}
__device__ __forceinline__ float rbf(float f) { return lo2f(pack2(f, f)); }
__device__ __forceinline__ unsigned rfl(unsigned v) { return __builtin_amdgcn_readfirstlane(v); }
__device__ __forceinline__ rsrc_t mk_rsrc(const void* p, unsigned bytes) {
  return __builtin_amdgcn_make_buffer_rsrc((void*)p, 0, bytes, 0x00020000);
}
#define DPP_ADD(v, ctrl, rm, bc) v += __builtin_bit_cast(float, __builtin_amdgcn_update_dpp(0, __builtin_bit_cast(int, v), ctrl, rm, 0xf, bc))
#define DPP_MAXU(v, ctrl, rm, bc) v = max(v, (unsigned)__builtin_amdgcn_update_dpp(0, (int)v, ctrl, rm, 0xf, bc))
__device__ __forceinline__ unsigned wave_maxu(unsigned v) {
  DPP_MAXU(v, 0x111, 0xf, true); DPP_MAXU(v, 0x112, 0xf, true); DPP_MAXU(v, 0x114, 0xf, true); DPP_MAXU(v, 0x118, 0xf, true);
  DPP_MAXU(v, 0x142, 0xa, false); DPP_MAXU(v, 0x143, 0xc, false);
  return (unsigned)__builtin_amdgcn_readlane((int)v, 63);
}
template <int N> __device__ __forceinline__ void wait_vm() { asm volatile("s_waitcnt vmcnt(%0)" :: "n"(N) : "memory"); }
#define WAIT_LGKM0() asm volatile("s_waitcnt lgkmcnt(0)" ::: "memory")
#define BAR() do { asm volatile("" ::: "memory"); __builtin_amdgcn_s_barrier(); asm volatile("" ::: "memory"); } while (0)
#define MFMA(a, b, c) __builtin_amdgcn_mfma_f32_16x16x32_bf16(a, b, c, 0, 0, 0)
#define BF8(v) __builtin_bit_cast(bf16x8, v)
constexpr int SPIN_CAP = 1 << 20;
#define T1_T 0x090a09090a0a0909ull
#define NWC 30
#define WB_AT 12
#define WBS 0x11001100
#define SWT 0x100d0a070400ull
#define PI 0x2e32363c
#define BSL 13
#define WD1 5
#define WD2 10
#define CDIV 8
#define RPOL SC1
#define EPOL SC1
#define APOL SC1
#define OPOL SC1
#define PPOL SC1
__device__ __forceinline__ int sched(u64 tab, int te) { return (int)(tab >> (te * 8)) & 255; }
#define PIN4(a, b, c, d) asm volatile("" : "+v"(a), "+v"(b), "+v"(c), "+v"(d))
__device__ __forceinline__ unsigned vm_now() { const unsigned r = __builtin_amdgcn_s_getreg(7 | (31 << 11)); return (r & 15u) | ((r >> 18) & 0x30u); }
__device__ __forceinline__ unsigned mk_tag(unsigned epoch) { return rfl(epoch) * 0x9E3779B1u + 0x7F4A7C15u; }

template <int LD, int AUX, int MODE, int NP = 16, int J0 = 0>
__device__ __forceinline__ void issue(rsrc_t r, int voff, int koff, LAS char* dst) {
#pragma unroll
  for (int j = J0; j < NP; ++j) {
    const int so = MODE == 2 ? j * 1024 : MODE == 1 ? (32 * (j >> 3) + 64 * ((j >> 2) & 1) + 16 * (j & 1) + 4 * ((j >> 1) & 1)) * LD : j * 8 * LD;
    __builtin_amdgcn_raw_ptr_buffer_load_lds(r, (LAS void*)(dst + j * 1024), 16, voff, koff + so, 0, AUX);
  }
}
__device__ __forceinline__ unsigned ktab(int tp1) { return (tp1 * 0x01010101u | ((tp1 & 12) ^ 4) * 0x10101010u) ^ (tp1 & 2 ? 0x33122100u : 0x33221100u); }
__device__ __forceinline__ int rot1(int tp1, int tq1) { return (8 * (tp1 + tq1) + 2 * (((tp1 >> 2) + 2 * (tq1 >> 3)) & 3)) & 15; }
__device__ __forceinline__ int kstep0(unsigned kt, int n) { return (int)(kt >> (n >> 1) * 4 & 15) * 2 + (n & 1); }
__device__ __forceinline__ int kstep(unsigned kt, int n) { return kstep0(kt, n == 3 ? 4 : n == 4 ? 3 : n); }
constexpr int QROWS = 1024 + 128;

__device__ __forceinline__ void wait_sent(LAS char* lds) {
#pragma unroll 1
  for (int spins = 0; spins < SPIN_CAP; ++spins) {
    const u32x4 f = *(volatile LAS u32x4*)(lds + L_MISC + 32);
    if (rfl(f[0] & f[1] & f[2] & f[3])) break;
    __builtin_amdgcn_s_sleep(1);
  }
  asm volatile("" ::: "memory");
}

__device__ __forceinline__ void loader(int L, int lane, LAS char* lds, rsrc_t r1, int v1, int ko, const u16* gq, const char* gp, rsrc_t rws,
                                       int flag_off, int b, int tp1 STAMP_ARG) {
  STAMP_DECL;
  __builtin_amdgcn_s_setprio(2);
  const int rsub = lane >> 3, ch = ((lane & 7) ^ rsub) * 16;
  const rsrc_t rq = mk_rsrc(gq, QROWS * S * 2);
  const int h = tp1 >> 3;
  unsigned kt = ktab(tp1);
  int vq = rsub * (S * 2) + ch, vp = lane * 16;
  int qa = h * 1024 * (S * 2), qb = ((1 - h) * 1024 + L * 64) * (S * 2);
  int q_ = (L ? qa : qb) + tp1 * 256;
  LAS char* d_ = lds + (1 - L) * UB;
  volatile LAS unsigned* misc = (volatile LAS unsigned*)(lds + L_MISC);
  unsigned fl = 0, tag;
  {
    const unsigned utab = L ? 0x8541u : 0x7230u;
    STAMP(1);
    LAS char* dst = lds + L * UB;
#pragma unroll 1
    for (int t = 0; t < 4; ++t) {
      issue<D * 2, 0, 1>(r1, v1, ko, dst);
      ko = (ko + 128) & 2047;
      dst = lds + ((utab >> (t * 4 + 4)) & 15) * UB;
      if (t == 1 || t == 2) { wait_vm<16>(); BAR(); }
    }
    LAS char* dt[4];
#pragma unroll
    for (int u = 0; u < 4; ++u) dt[u] = lds + ((utab >> (u * 4)) & 15) * UB;
    asm volatile("" : "+s"(kt), "+s"(qa), "+s"(qb), "+s"(q_), "+s"(d_) : "s"(rq));
    asm volatile("" : "+v"(vq), "+v"(vp));
#pragma unroll 1
    for (int t = 0; t < 3; ++t) {
#pragma unroll
      for (int u = 0; u < 4; ++u) {
        issue<D * 2, 0, 1, 8>(r1, v1, ko, dt[u]);
        int kn = (ko + 128) & 2047;
        asm volatile("" : "+s"(kn), "+s"(dt[(u + 1) & 3]));
        if (u == 1) asm volatile("ds_read_b32 %0, %1" : "=v"(tag) : "v"(L_MISC + 8) : "memory");
        issue<D * 2, 0, 1, 16, 8>(r1, v1, ko, dt[u]);
        ko = kn;
        wait_vm<32>();
        BAR();
      }
    }
    asm volatile("s_waitcnt lgkmcnt(0)" : "+v"(tag) :: "memory");
  }
#define POLL(o) asm volatile("buffer_load_dword %0, %1, %2, 0 offen sc1" : "+v"(fl) : "v"(o), "s"(rws) : "memory")
#define POLLW(o) asm volatile("buffer_load_dword %0, %1, %2, 0 offen sc1\n\ts_waitcnt vmcnt(0)" : "+v"(fl) : "v"(o), "s"(rws) : "memory")
  {
    issue<S * 2, 0, 0>(rq, vq, q_, d_);
    const int q2 = qa + L * 64 * (S * 2) + kstep(kt, 2) * 128;
    wait_vm<32>();
    BAR();
    issue<S * 2, 0, 0, 8>(rq, vq, q2, lds + 2 * SLOT + L * 8192);
    wait_vm<24>();
    BAR();
    issue<S * 2, 0, 0>(rq, vq, q_ + 128, d_ + SLOT);
    tag = rfl(tag);
    BAR();
    wait_vm<16>();
    BAR();
    issue<S * 2, 0, 0, 8>(rq, vq, qb + kstep(kt, 2) * 128, lds + 2 * SLOT + UB + L * 8192);
    wait_vm<8>();
    BAR();
  }
  const int vx = (lane ^ (lane >> 2 & 12)) * 16;
  if (L) {
    int k_ = kstep(kt, 3) * 128, ka = qa + k_;
    LAS char* sl = lds;
#pragma unroll 1
    for (int n = 4; n < 16; ++n) {
      issue<S * 2, 0, 0>(rq, vq, ka, sl);
      const int kb = qb + k_;
      LAS char* const sb = sl + UB + 8192;
      k_ = kstep(kt, n) * 128; ka = qa + k_;
      sl = sl == lds + 2 * SLOT ? lds : sl + SLOT;
      asm volatile("" : "+s"(k_), "+s"(ka), "+s"(sl));
      issue<S * 2, 0, 0, 8>(rq, vq, kb, sb);
      wait_vm<24>();
      BAR();
    }
    issue<S * 2, 0, 0>(rq, vq, qa + (k_ ^ 2048), lds);
    const int f2o = WS_FLAG2 + (b ^ 4) * 128 + 16 + (lane & 3) * 4;
    wait_vm<16>();
    BAR();
    wait_vm<0>();
    BAR();
    issue<S * 2, 0, 0, 8>(rq, vq, qa + 64 * (S * 2) + k_, lds + 4 * UB + 8192);
    wait_vm<0>();
    BAR();
    wait_sent(lds);
#pragma unroll 1
    for (int spins = 0; spins < SPIN_CAP; ++spins) {
      POLLW(f2o);
      if (__all(fl == tag)) break;
      __builtin_amdgcn_s_sleep(1);
    }
    STAMP(4);
    issue<0, RPOL, 2>(rws, vx, WS_XCH + (b ^ 4) * XB + 32768, lds + 4 * UB);
    wait_vm<0>();
    misc[12] = 1u;
    STAMP(5);
  } else {
    const rsrc_t rp = mk_rsrc(gp, 128 * S * 2);
    unsigned mask = 0;
    const int fo = flag_off + 32768 + (((lane >> 2) & 7) == (tp1 & 7) ? (kt >> 28) ^ 8 : h * 8 + ((lane >> 2) & 7)) * 128 + (lane & 3) * 4;
#define KM_(k) (15u << 2 * ((k) & 14))
    const int fo1 = flag_off + (kt >> (lane & 4 ? 8 : 4) & 15) * 128 + (lane & 3) * 4;
    STAMP(2);
    {
      unsigned f1, f2;
      asm volatile("buffer_load_dword %0, %1, %2, 0 offen sc1" : "=&v"(f1) : "v"(fo1), "s"(rws) : "memory");
      issue<S * 2, 0, 0, 8>(rq, vq, qb + kstep(kt, 3) * 128, lds + UB);
      asm volatile("buffer_load_dword %0, %2, %3, 0 offen sc1\n\ts_waitcnt vmcnt(9)" : "=&v"(f2), "+v"(f1) : "v"(fo1), "s"(rws) : "memory");
      unsigned m1 = (unsigned)__ballot(f1 == tag);
#pragma unroll 1
      for (int spins = 0; (m1 & 15) != 15 && spins < SPIN_CAP; ++spins) {
        asm volatile("s_sleep 2\n\tbuffer_load_dword %0, %2, %3, 0 offen sc1\n\ts_waitcnt vmcnt(1)" : "+v"(f1), "+v"(f2) : "v"(fo1), "s"(rws) : "memory");
        m1 |= (unsigned)__ballot(f2 == tag);
        if ((m1 & 15) == 15) break;
        asm volatile("s_sleep 2\n\tbuffer_load_dword %0, %2, %3, 0 offen sc1\n\ts_waitcnt vmcnt(1)" : "+v"(f2), "+v"(f1) : "v"(fo1), "s"(rws) : "memory");
        m1 |= (unsigned)__ballot(f1 == tag);
      }
      STAMP(3);
      issue<0, PPOL, 2>(rp, vp, kstep(kt, 2) * UB, lds + 2 * SLOT + 2 * UB);
      asm volatile("s_waitcnt vmcnt(16)" : "+v"(f1), "+v"(f2) :: "memory");
      m1 |= (unsigned)(__ballot(f1 == tag) | __ballot(f2 == tag));
      const bool p3 = (m1 & 0xf0) == 0xf0;
      if (!p3) {
        POLL(fo1);
        wait_vm<1>();
        BAR();
        asm volatile("s_waitcnt vmcnt(0)" : "+v"(fl) :: "memory");
#pragma unroll 1
        for (int spins = 0; ((unsigned)__ballot(fl == tag) & 0xf0) != 0xf0 && spins < SPIN_CAP; ++spins) POLLW(fo1);
      }
      POLL(fo);
      issue<0, PPOL, 2>(rp, vp, kstep(kt, 3) * UB, lds + 2 * UB);
      if (p3) { wait_vm<17>(); BAR(); }
      asm volatile("s_waitcnt vmcnt(16)" : "+v"(fl) :: "memory");
    }
#define NEED(need) do { if (__builtin_expect(mask != ~0u, 0)) { asm volatile("" : "+v"(fl) :: "memory"); mask |= (unsigned)__ballot(fl == tag); \
      _Pragma("unroll 1") for (int spins = 0; (mask & (need)) != (need) && spins < SPIN_CAP; ++spins) { POLLW(fo); mask |= (unsigned)__ballot(fl == tag); } \
      if (mask != ~0u) POLL(fo); } } while (0)
    int k_ = kstep(kt, 4), kp = k_ * UB;
    LAS char* sl = lds + SLOT;
#pragma unroll 1
    for (int n = 5; n < 16; ++n) {
      NEED(KM_(k_));
      issue<0, PPOL, 2>(rp, vp, kp, sl + 2 * UB);
      const int kb = qb + k_ * 128;
      LAS char* const sb = sl + UB;
      k_ = kstep0(kt, n); kp = k_ * UB;
      sl = sl == lds + 2 * SLOT ? lds : sl + SLOT;
      asm volatile("" : "+s"(k_), "+s"(kp), "+s"(sl));
      issue<S * 2, 0, 0, 8>(rq, vq, kb, sb);
      wait_vm<24>();
      BAR();
    }
    NEED(~0u);
    issue<0, PPOL, 2>(rp, vp, (k_ ^ 16) * UB, lds + 2 * UB);
    wait_vm<16>();
    BAR();
    issue<0, PPOL, 2>(rp, vp, k_ * UB, lds + UB);
    wait_vm<16>();
    BAR();
    issue<S * 2, 0, 0, 8>(rq, vq, qa + k_ * 128, lds + 4 * UB);
    const int f2o = WS_FLAG2 + (b ^ 2 ^ (lane & 4)) * 128 + (lane & 3) * 4;
    wait_vm<0>();
    BAR();
    wait_sent(lds);
#pragma unroll 1
    for (int spins = 0; spins < SPIN_CAP; ++spins) {
      POLLW(f2o);
      if (__all(fl == tag)) break;
      __builtin_amdgcn_s_sleep(1);
    }
    issue<0, RPOL, 2>(rws, vx, WS_XCH + (b ^ 2) * XB, lds + 6 * UB);
    issue<0, RPOL, 2>(rws, vx, WS_XCH + (b ^ 6) * XB + 16384, lds + 7 * UB);
    STAMP(4);
    wait_vm<0>();
    misc[13] = 1u;
    STAMP(5);
  }
  STAMP_OUT();
}

#define LDF(off) (*reinterpret_cast<const bf16x8*>(lds + (off)))
struct Frag { bf16x8 p[4], q[4]; };
#define SGB(m, n) __builtin_amdgcn_sched_group_barrier(m, n, 0)
__device__ __forceinline__ float silu_fast(float z) { return z * __builtin_amdgcn_rcpf(1.0f + __expf(-z)); }
__device__ __forceinline__ u32x4 pack8(const f32x4& a, const f32x4& c) {
  return u32x4{pack2(a[0], a[1]), pack2(a[2], a[3]), pack2(c[0], c[1]), pack2(c[2], c[3])};
}
__device__ __forceinline__ unsigned ln_pair(unsigned x, float nmb, float rden) {
  const unsigned d = pack2(lo2f(x) + nmb, hi2f(x) + nmb);
  return pack2(lo2f(d) * rden, hi2f(d) * rden);
}

template <bool BARRIER, bool LAST, bool ZERO, class X>
__device__ __forceinline__ void big_half(f32x4 (&a0)[4][4], f32x4 (&a1)[4][4], const bf16x8 (&pc)[4], bf16x8 (&pn)[4], bf16x8 (&q)[8],
                                         const char* lds, int qc, int pa, int qn, int qn2, X extra) {
  const f32x4 zero = {0.f, 0.f, 0.f, 0.f};
#pragma unroll
  for (int g = 0; g < 8; ++g) {
    if (g == 0) { q[5] = LDF(qc + UB + 2048); q[6] = LDF(qc + UB + 4096); }
    if (g == 1) q[7] = LDF(qc + UB + 6144);
    if (g == 3) { pn[0] = LDF(pa); pn[1] = LDF(pa + 2048); }
    if (g == 4) { pn[2] = LDF(pa + 4096); pn[3] = LDF(pa + 6144); }
    if (g == 5) { q[0] = LDF(qn); q[1] = LDF(qn + 2048); }
    if (g == 6) { q[2] = LDF(qn + 4096); q[3] = LDF(qn + 6144); }
    if (g == 7 && !LAST) q[4] = LDF(qn2);
#pragma unroll
    for (int i = 0; i < 4; ++i) {
      const int ii = (g & 1) ? 3 - i : i;
      if (g < 4) a0[ii][g] = MFMA(pc[ii], q[g], ZERO ? zero : a0[ii][g]); else a1[ii][g - 4] = MFMA(pc[ii], q[g], ZERO ? zero : a1[ii][g - 4]);
    }
    SGB(0x8, 1); SGB(0x100, 1); SGB(0x8, 1); SGB(0x100, 1); SGB(0x8, 2);
    __builtin_amdgcn_sched_barrier(0);
    if (g == 2 && BARRIER) { WAIT_LGKM0(); BAR(); }
    extra(g);
  }
}

template <bool JOBNEXT>
__device__ __forceinline__ void small_step(f32x4 (&a)[4][4], bf16x8 (&p0)[4], bf16x8 (&p1)[4], bf16x8 (&q)[8], const char* lds,
                                           int ph, int qh, int pn, int qn, int qn2) {
#pragma unroll
  for (int g = 0; g < 8; ++g) {
    if (g == 0) { q[4] = LDF(qh); p1[0] = LDF(ph); }
    if (g == 1) { p1[1] = LDF(ph + 2048); p1[2] = LDF(ph + 4096); }
    if (g == 2) { p1[3] = LDF(ph + 6144); q[5] = LDF(qh + 2048); }
    if (g == 3) { q[6] = LDF(qh + 4096); q[7] = LDF(qh + 6144); }
    if (g == 4) { p0[0] = LDF(pn); q[0] = LDF(qn); }
    if (g == 5) { p0[1] = LDF(pn + 2048); p0[2] = LDF(pn + (JOBNEXT ? 1024 : 4096)); if (JOBNEXT) q[1] = LDF(qn + 2048); }
    if (g == 6) { p0[3] = LDF(pn + (JOBNEXT ? 3072 : 6144)); if (JOBNEXT) { q[2] = LDF(qn + 4096); q[3] = LDF(qn + 6144); } else q[1] = LDF(qn + 2048); }
    if (g == 7) { if (JOBNEXT) { q[4] = LDF(qn2); q[5] = LDF(qn2 + 2048); } else { q[2] = LDF(qn + 4096); q[3] = LDF(qn + 6144); } }
#pragma unroll
    for (int i = 0; i < 4; ++i) {
      const int ii = (g & 1) ? 3 - i : i;
      a[ii][g & 3] = MFMA(g < 4 ? p0[ii] : p1[ii], q[g], a[ii][g & 3]);
    }
    SGB(0x8, 1); SGB(0x100, 1); SGB(0x8, 1); SGB(0x100, 1); SGB(0x8, 1); SGB(0x100, 1); SGB(0x8, 1);
    __builtin_amdgcn_sched_barrier(0);
    if (g == 3) BAR();
  }
}

__device__ __forceinline__ void consumer(int wid, int lane, int b, char* lds, rsrc_t rws, rsrc_t rout,
                                         unsigned tch, int tp1, int tq1 STAMP_ARG) {
  STAMP_DECL;
  const int z = tq1 >> 3, te = tq1 & 7;
  const int wp = wid >> 1, wq = wid & 1, fr = lane & 15, fq = lane >> 4;
  const int c0 = (fq ^ (fr & 7)) * 16, c1 = ((fq + 4) ^ (fr & 7)) * 16;
  int q0 = (wq * 64 + fr) * 128 + c0, q1 = (wq * 64 + fr) * 128 + c1;
  volatile LAS unsigned* misc = (volatile LAS unsigned*)(lds + L_MISC);
  f32x4 acc[4][4];
#pragma unroll
  for (int i = 0; i < 4; ++i)
#pragma unroll
    for (int j = 0; j < 4; ++j) acc[i][j] = f32x4{0.f, 0.f, 0.f, 0.f};
  unsigned tag;
  u32x4 xv[4];
  {
    Frag f0, f1;
    const int xrow = wq * 32 + (lane & 16) + 4 * (lane >> 5) + ((lane >> 2) & 3);
    const int xr = xrow * 128 + (((wp * 4 + (lane & 3)) ^ (xrow & 7)) * 16);
#define XV(u) do { _Pragma("unroll") for (int k = 0; k < 4; ++k) \
      xv[k] = *reinterpret_cast<const u32x4*>(lds + (u) * UB + xr + (k >> 1) * 8192 + (k & 1) * 1024); } while (0)
    const int p0 = (wp * 64 + fr) * 128 + c0, p1 = (wp * 64 + fr) * 128 + c1;
    const int xt = (2 * te + z - rot1(tp1, tq1)) & 15;
#define G1H(fc, fn, po, qo, EXTRA) do { _Pragma("unroll") for (int g = 0; g < 8; ++g) { \
      EXTRA(g); { if (g == 0) { fn.q[0] = LDF(qo); fn.p[0] = LDF(po); } else if (g == 1) { fn.p[1] = LDF((po) + 2048); fn.p[2] = LDF((po) + 4096); } \
        else if (g == 2) { fn.p[3] = LDF((po) + 6144); fn.q[1] = LDF((qo) + 2048); } else if (g == 3) { fn.q[2] = LDF((qo) + 4096); fn.q[3] = LDF((qo) + 6144); } } \
      _Pragma("unroll") for (int e = 0; e < 2; ++e) { const int i0 = (g & 1) * 2 + e, ii = (g & 2) ? 3 - i0 : i0; \
        acc[ii][g >> 1] = MFMA(fc.p[ii], fc.q[g >> 1], acc[ii][g >> 1]); } \
      SGB(0x8, 1); SGB(0x100, 1); SGB(0x8, 1); SGB(0x100, 2); __builtin_amdgcn_sched_barrier(0); } } while (0)
#define X_NO(g)
#define XI(u) ((u) == 0 ? 0 : (u) == 1 ? 3 : (u) == 2 ? 2 : 7)
#define XVC(t, u) do { if (__builtin_expect((t) == xt, 0)) { XV(XI(u)); PIN4(xv[0], xv[1], xv[2], xv[3]); } __builtin_amdgcn_sched_barrier(0); } while (0)
    int pA[2] = {p0, p1}, pB[2] = {p0 + 7 * UB, p1 + 7 * UB}, qA[2] = {q0 + UB, q1 + UB}, qB[2] = {q0 + 5 * UB, q1 + 5 * UB};
    PIN4(pA[0], pA[1], pB[0], pB[1]); PIN4(qA[0], qA[1], qB[0], qB[1]);
#define XA(u, h) ((u) == 3 ? pB[h] : pA[h] + XI(u) * UB)
#define WA(u, h) ((u) < 2 ? qA[h] + (u) * 3 * UB : qB[h] + ((u) - 2) * 3 * UB)
    int ta = L_MB + (wp * 32 + fq * 8) * 4, tc = L_MB + 1024 + (wq * 32 + 8 * (fr >> 2) + (fr & 3)) * 4;
    const int flip = z ? 0 : 4096;
    int ao[2], ow[2];
#pragma unroll
    for (int h = 0; h < 2; ++h) {
      ao[h] = WS_ABT + tq1 * ABT_ST + tp1 * 2 * UB + wq * 8192 + wp * 1024 + lane * 16 + (h * 4096 ^ flip);
      ow[h] = 2 * UB + wq * 8192 + wp * 1024 + lane * 16 + (h * 4096 ^ flip);
    }
    PIN4(ao[0], ao[1], ow[0], ow[1]);
    asm volatile("" : "+v"(ta), "+v"(tc));
    BAR();
    XVC(0, 0);
    f0.q[0] = LDF(WA(0, 0));
#pragma unroll
    for (int i = 0; i < 4; ++i) f0.p[i] = LDF(XA(0, 0) + i * 2048);
#pragma unroll
    for (int i = 1; i < 4; ++i) f0.q[i] = LDF(WA(0, 0) + i * 2048);
    __builtin_amdgcn_sched_barrier(0);
#pragma unroll 1
    for (int t = 0; t < 12; t += 4) {
#pragma unroll
      for (int u = 0; u < 4; ++u) {
        G1H(f0, f1, XA(u, 1), WA(u, 1), X_NO);
        WAIT_LGKM0();
        BAR();
        XVC(t + u + 1, (u + 1) & 3);
        G1H(f1, f0, XA((u + 1) & 3, 0), WA((u + 1) & 3, 0), X_NO);
      }
    }
    u32x2 rdy;
#define X_RDY(g) if ((g) == 4) rdy = *(const volatile LAS u32x2*)(lds + L_MISC + 16)
    G1H(f0, f1, XA(0, 1), WA(0, 1), X_RDY);
    asm volatile("s_waitcnt vmcnt(0) lgkmcnt(0)" : "+v"(tch) :: "memory");
    BAR();
    XVC(13, 1);
    if (__builtin_expect(!rfl(rdy[0] & rdy[1]), 0)) {
#pragma unroll 1
      for (int spins = 0; spins < SPIN_CAP && !rfl(misc[4] & misc[5]); ++spins) __builtin_amdgcn_s_sleep(1);
    }
    asm volatile("" ::: "memory");
    f32x4 mb[2][2], rd[2][2]; float cs[4];
    unsigned tagv;
#define TB(o) (*reinterpret_cast<const f32x4*>(lds + ta + (o)))
#define X_T1(g) if ((g) >= 4) cs[(g) & 3] = *reinterpret_cast<const float*>(lds + tc + (64 * (((g) & 3) >> 1) + 4 * ((g) & 1)) * 4)
#define X_T2(g) if ((g) == 4) { mb[1][0] = TB(256); mb[1][1] = TB(272); } else if ((g) == 5) tagv = *(const LAS unsigned*)(lds + L_MISC + 8)
    G1H(f1, f0, XA(1, 0), WA(1, 0), X_T1);
    G1H(f0, f1, XA(1, 1), WA(1, 1), X_T2);
    WAIT_LGKM0();
    BAR();
    XVC(14, 2);
    Frag F[4];
#define SB() __builtin_amdgcn_sched_barrier(0)
#define FQ(k, i) F[k].q[i] = LDF(WA(2 + ((k) >> 1), (k) & 1) + (i) * 2048)
#define FP(k, i) F[k].p[i] = LDF(XA(2 + ((k) >> 1), (k) & 1) + (i) * 2048)
#define FA(k, r) do { if ((r) == 0) FQ(k, 0); else if ((r) < 3) FP(k, (r) - 1); else FQ(k, (r) - 2); } while (0)
#pragma unroll
    for (int n = 0; n < 16; ++n) {
      const int j = n >> 2, ii = (j & 1) ? 3 - (n & 3) : n & 3;
      acc[ii][j] = MFMA(f1.p[ii], f1.q[j], acc[ii][j]);
      SB();
      if ((n & 3) != 3) { const int m = n - (n >> 2); FA(m / 6, m % 6); }
      SB();
    }
    BAR();
    XVC(15, 3);
#define VFMA(d, a, c) asm volatile("v_fma_f32 %0, %1, %2, %0" : "+v"(d) : "v"(a), "v"(c))
#define VMUL(d, a) asm volatile("v_mul_f32 %0, %1, %0" : "+v"(d) : "v"(a))
    float cb[2][4][4];
#pragma unroll
    for (int n = 0; n < 32; ++n) {
      const int k = n >> 3, j = (n >> 1) & 3, ii = (n ^ n >> 1) & 1;
      acc[ii][j] = MFMA(F[k].p[ii], F[k].q[j], acc[ii][j]);
      SB();
      if ((n & 3) != 3) { const int m = n - (n >> 2); if (m < 12) FA(2 + m / 6, m % 6); else if (m < 20) FP((m - 12) >> 1, 2 + (m & 1)); }
      if (n == 19) mb[0][0] = TB(0); if (n == 23) mb[0][1] = TB(16); if (n == 26) rd[0][0] = TB(1536); if (n == 27) rd[0][1] = TB(1552);
      {
        const int jj = n >> 3, h = (n >> 2) & 1, r = n & 3;
        cb[h][jj][r] = acc[2 + h][jj][r];
        VFMA(cb[h][jj][r], mb[1][h][r], cs[jj]);
        acc[2 + h][jj][r] = cb[h][jj][r];
      }
      SB();
    }
    WAIT_LGKM0();
    BAR();
    tag = rfl(tagv);
    u32x4 o;
#pragma unroll
    for (int n = 0; n < 32; ++n) {
      const int k = n >> 3, j = (n >> 1) & 3, ii = 2 + ((n ^ n >> 1) & 1);
      acc[ii][j] = MFMA(F[k].p[ii], F[k].q[j], acc[ii][j]);
      SB();
      {
        const int h = (n >> 2) & 1, r = n & 3;
        cb[h][k][r] = acc[h][k][r];
        VFMA(cb[h][k][r], mb[0][h][r], cs[k]);
        if (n) { const int n1 = n - 1; VMUL(cb[(n1 >> 2) & 1][n1 >> 3][n1 & 3], rd[0][(n1 >> 2) & 1][n1 & 3]); }
        if (n > 2 && (n & 1)) { const int n2 = n - 3;
          unsigned o_ = pack2(cb[(n2 >> 2) & 1][n2 >> 3][n2 & 3], cb[(n2 >> 2) & 1][n2 >> 3][(n2 & 3) + 1]); asm volatile("" : "+v"(o_)); o[(n2 >> 1) & 3] = o_; }
        if (n == 20) rd[1][0] = TB(1792); if (n == 22) rd[1][1] = TB(1808);
        if (n > 8 && (n & 7) == 2) {
          __builtin_amdgcn_raw_buffer_store_b128(o, rws, ao[(k - 1) >> 1] + ((k - 1) & 1) * 2048, 0, APOL);
          *reinterpret_cast<u32x4*>(lds + ow[(k - 1) >> 1] + ((k - 1) & 1) * 2048) = o;
        }
      }
      SB();
    }
    STAMP(2);
    VMUL(cb[1][3][3], rd[0][1][3]);
    o[3] = pack2(cb[1][3][2], cb[1][3][3]);
    __builtin_amdgcn_raw_buffer_store_b128(o, rws, ao[1] + 2048, 0, APOL);
    *reinterpret_cast<u32x4*>(lds + ow[1] + 2048) = o;
    asm volatile("" ::: "memory");
#pragma unroll
    for (int j = 0; j < 4; ++j) {
      o = pack8(acc[2][j] * rd[1][0], acc[3][j] * rd[1][1]);
      __builtin_amdgcn_raw_buffer_store_b128(o, rws, ao[j >> 1] + UB + (j & 1) * 2048, 0, APOL);
      *reinterpret_cast<u32x4*>(lds + ow[j >> 1] + SLOT + (j & 1) * 2048) = o;
    }
    STAMP(3);
  }
  f32x4 acb[4][4];
  bf16x8 pa[4], pb[4], q[8];
  int pp = wp * 8192 + lane * 16;
  const int f1o = WS_FLAG1 + (tq1 * 16 + tp1) * 128 + wid * 4;
#define FLAG1(i, n) do { wait_vm<n>(); __builtin_amdgcn_raw_buffer_store_b32(tag, rws, lane ? -1 : f1o + (i) * 32768, 0, SC1); if (i) STAMP(4); __builtin_amdgcn_sched_barrier(0); } while (0)
  auto fl0 = [&](int g) { if (g == 1) FLAG1(0, 4); if (g == 7) FLAG1(1, 1); };
  WAIT_LGKM0();
  BAR();
  STAMP(5);
  pa[0] = LDF(2 * UB + pp); q[0] = LDF(q0); pa[1] = LDF(2 * UB + pp + 2048); pa[2] = LDF(2 * UB + pp + 4096); pa[3] = LDF(2 * UB + pp + 6144);
  q[1] = LDF(q0 + 2048); q[2] = LDF(q0 + 4096); q[3] = LDF(q0 + 6144); q[4] = LDF(q0 + UB);
  __builtin_amdgcn_sched_barrier(0);
  auto none = [](int) {};
  big_half<false, false, true>(acc, acb, pa, pb, q, lds, q0, 2 * UB + pp + 1024, q1, q1 + UB, fl0);
  PIN4(q0, q1, pp, lane);
  {
    int sl = 0;
#pragma unroll 1
    for (int n = 0; n < 12; ++n) {
      const int sn = sl == 2 * SLOT ? 0 : sl + SLOT;
      big_half<true, false, false>(acc, acb, pb, pa, q, lds, sl + q1, sn + 2 * UB + pp, sn + q0, sn + q0 + UB, none);
      big_half<false, false, false>(acc, acb, pa, pb, q, lds, sn + q0, sn + 2 * UB + pp + 1024, sn + q1, sn + q1 + UB, none);
      sl = sn;
    }
  }
  int q0h = q0 + 4 * UB, q1h = q1 + 4 * UB, pph = pp + 5 * UB;
  asm volatile("" : "+v"(q0h), "+v"(q1h), "+v"(pph));
  big_half<true, true, false>(acc, acb, pb, pa, q, lds, q1, pph, q0h, 0, none);
  small_step<false>(acb, pa, pb, q, lds, pph + 1024, q1h, pph + 3 * UB, q0h + 3 * UB, 0);
  small_step<true>(acb, pa, pb, q, lds, pph + 3 * UB + 1024, q1h + 3 * UB, pph, 3 * UB + q0, 3 * UB + q1);
  int xw = WS_XCH + b * XB + wid * 4096 + lane * 16;
  asm volatile("" : "+v"(xw));
#define SEND_E(j) __builtin_amdgcn_raw_buffer_store_b128(pack8(acb[(j) < 4 ? 2 : 0][(j) & 3], acb[(j) < 4 ? 3 : 1][(j) & 3]), rws, xw + ((j) >> 2) * 16384 + ((j) & 3) * 1024, 0, EPOL)
  STAMP(10);
  const int f2o = WS_FLAG2 + b * 128 + wid * 4;
#define PA(k) ((k) == 0 ? pph : (k) == 1 ? pph + 3 * UB : (k) == 2 ? pp + 2 * UB : pp + UB)
#define QA0(k) ((k) == 0 ? q0 + 3 * UB : (k) == 1 ? q0h + 2 * UB : (k) == 2 ? q0 : q0h)
#define QA1(k) ((k) == 0 ? q1 + 3 * UB : (k) == 1 ? q1h + 2 * UB : (k) == 2 ? q1 : q1h)
#define JOB(K, I0, pc, qc, LC, pn, KN, IN, qx, LN, EXTRA) do { _Pragma("unroll") for (int g = 0; g < 8; ++g) { \
    if (g < 2 && (LC)) qc[g + 6] = LDF(QA1(K) + (g + 2) * 2048); \
    if ((KN) >= 0) { \
      if (g >= 2 && (LN)) qx[g - 2] = LDF(g < 6 ? QA0(KN) + (g - 2) * 2048 : QA1(KN) + (g - 6) * 2048); \
      if (g < 4) pn[g] = LDF(PA(KN) + ((IN) + (g & 1)) * 2048 + (g >> 1) * 1024); } \
    acc[I0][g & 3] = MFMA(pc[(g >> 2) * 2], qc[g], acc[I0][g & 3]); \
    acc[(I0) + 1][g & 3] = MFMA(pc[(g >> 2) * 2 + 1], qc[g], acc[(I0) + 1][g & 3]); \
    EXTRA(g); \
    SGB(0x8, 1); SGB(0x100, 1); SGB(0x2, 2); SGB(0x8, 1); SGB(0x100, 1); SGB(0x2, 2); \
    __builtin_amdgcn_sched_barrier(0); } \
    if ((KN) < 0) { PIN4(acc[2][0], acc[2][1], acc[2][2], acc[2][3]); PIN4(acc[3][0], acc[3][1], acc[3][2], acc[3][3]); } } while (0)
#define X_NONE(g)
#define X_E0(g) if ((g) >= 1) SEND_E((g) - 1 & 7)
#define X_E1(g) if ((g) == 0) SEND_E(7)
#define X_Z(g) if ((g) >= 1 && (g) < 5) __builtin_amdgcn_raw_buffer_store_b128(pack8(acc[0][((g) - 1) & 3], acc[1][((g) - 1) & 3]), rws, xw + 32768 + (((g) - 1) & 3) * 1024, 0, EPOL); \
    else if ((g) == 5) misc[8 + wid] = 1u
#define FLAG2(i) do { wait_vm<0>(); __builtin_amdgcn_raw_buffer_store_b32(tag, rws, lane ? -1 : f2o + (i) * 16, 0, SC1); STAMP((i) ? 1 : 9); } while (0)
  bf16x8 qb[8], qc_[8];
  JOB(0, 0, pa, q, 1, pb, 1, 0, q, 1, X_E0);
  JOB(1, 0, pb, q, 1, pa, 2, 0, qb, 1, X_E1);
  BAR();
  JOB(2, 0, pa, qb, 1, pb, 3, 0, qc_, 1, X_NONE);
  JOB(3, 0, pb, qc_, 1, pa, 3, 2, q, 0, X_NONE);
  FLAG2(0);
  JOB(3, 2, pa, qc_, 0, pb, 2, 2, qb, 0, X_Z);
  JOB(2, 2, pb, qb, 0, pa, 1, 2, q, 0, X_NONE);
  JOB(1, 2, pa, q, 0, pb, 0, 2, qb, 1, X_NONE);
  FLAG2(1);
  float tm[4], tr[4]; unsigned selv; u32x4 sv_;
  const char* tb = lds + L_MB + (wq * 64 + (lane >> 2)) * 4;
#define X_TAB(g) if ((g) >= 2 && (g) < 6) { tm[((g) - 2) & 3] = *reinterpret_cast<const float*>(tb + ((g) - 2) * 64); tr[((g) - 2) & 3] = *reinterpret_cast<const float*>(tb + 512 + ((g) - 2) * 64); } \
  else if ((g) == 6) { selv = misc[0]; sv_ = *(volatile LAS u32x4*)(lds + L_MISC + 32); }
  JOB(0, 2, pb, qb, 1, pa, -1, 0, q, 0, X_TAB);
  STAMP(6);
  if (__builtin_expect(!rfl(sv_[0] & sv_[1] & sv_[2] & sv_[3]), 0)) wait_sent((LAS char*)lds);
  asm volatile("" ::: "memory");
  {
    char* tw = lds + wid * 8192 + (lane & 15) * 64 + (((lane >> 4) ^ (lane >> 1)) & 3) * 16;
    const char* tr_ = lds + wid * 8192 + (lane >> 2) * 64 + ((lane ^ (lane >> 3)) & 3) * 16;
#pragma unroll
    for (int k = 0; k < 8; ++k) *reinterpret_cast<f32x4*>(tw + k * 1024) = acc[2 + (k >> 2)][k & 3];
    asm volatile("" ::: "memory");
#pragma unroll
    for (int k = 0; k < 8; ++k) acc[2 + (k >> 2)][k & 3] = *reinterpret_cast<const f32x4*>(tr_ + k * 1024);
    __builtin_amdgcn_sched_barrier(0);
  }
  PIN4(xv[0], xv[1], xv[2], xv[3]);
  f32x2 gf[4][4];
#pragma unroll
  for (int k = 0; k < 4; ++k) {
    const float tn = tm[k] * tr[k];
#pragma unroll
    for (int c = 0; c < 4; ++c) {
      const f32x2 n = f32x2{lo2f(xv[k][c]), hi2f(xv[k][c])} * tr[k] + tn, a = n * -1.44269504f;
      const f32x2 d = f32x2{__builtin_amdgcn_exp2f(a[0]), __builtin_amdgcn_exp2f(a[1])} + 1.0f;
      gf[k][c] = n * f32x2{__builtin_amdgcn_rcpf(d[0]), __builtin_amdgcn_rcpf(d[1])};
    }
    PIN4(gf[k][0], gf[k][1], gf[k][2], gf[k][3]);
  }
  f32x2 xf[4][4];
#pragma unroll
  for (int k = 0; k < 4; ++k) {
#pragma unroll
    for (int c = 0; c < 4; ++c) xf[k][c] = f32x2{lo2f(xv[k][c]), hi2f(xv[k][c])};
    PIN4(xf[k][0], xf[k][1], xf[k][2], xf[k][3]);
  }
  {
    unsigned sel = rfl(selv);
    if (__builtin_expect(!sel || (int)(sel - 1 >> 6) == tp1 * 2 + wq, 0)) {
#pragma unroll 1
      for (int spins = 0; spins < SPIN_CAP && !sel; ++spins) { sel = rfl(misc[0]); __builtin_amdgcn_s_sleep(2); }
      const int idx = (int)sel - 1;
      if ((idx >> 6) == tp1 * 2 + wq) {
        const int ks = (idx >> 4) & 3;
        const u32x4 xs = ks == 0 ? xv[0] : ks == 1 ? xv[1] : ks == 2 ? xv[2] : xv[3];
        const float ms = ks == 0 ? tm[0] : ks == 1 ? tm[1] : ks == 2 ? tm[2] : tm[3], rs = ks == 0 ? tr[0] : ks == 1 ? tr[1] : ks == 2 ? tr[2] : tr[3];
        u32x4 n;
#pragma unroll
        for (int c = 0; c < 4; ++c) n[c] = ln_pair(xs[c], ms, rs);
        asm volatile("" : "+v"(n));
#pragma unroll 1
        for (int spins = 0; spins < SPIN_CAP && !rfl(misc[1]); ++spins) __builtin_amdgcn_s_sleep(1);
        asm volatile("" ::: "memory");
        const LAS float* upd = (const LAS float*)(lds + L_UPD) + wp * 32 + (lane & 3) * 8;
#pragma unroll
        for (int c = 0; c < 4; ++c) {
          const unsigned u = pack2(lo2f(n[c]) + upd[2 * c], hi2f(n[c]) + upd[2 * c + 1]);
          const f32x2 gn = {silu_fast(lo2f(u)), silu_fast(hi2f(u))};
#pragma unroll
          for (int k = 0; k < 4; ++k) if (ks == k && (idx & 15) == (lane >> 2)) gf[k][c] = gn;
        }
      }
    }
  }
  int xo = ((tp1 * 128 + wq * 64 + (lane >> 2)) * D + te * 128 + z * 64 + wp * 32 + (lane & 3) * 8) * 2, le = wid * 4096 + (lane & 3) * 256 + ((lane >> 2) ^ (lane & 3) * 4) * 16 + 4 * UB;
  const unsigned one = (lane >> 4) == ((lane >> 2) & 3) ? (lane & 1 ? 0x3f800000u : 0x3f80u) : 0u;
  u32x4 s0 = {lane & 2 ? 0u : one, lane & 2 ? one : 0u, 0u, 0u}, s1 = {0u, 0u, s0[0], s0[1]};
  PIN4(xo, le, s0, s1);
  STAMP(7);
#pragma unroll 1
  for (int spins = 0; spins < SPIN_CAP; ++spins) { const u32x2 f = *(volatile LAS u32x2*)(lds + L_MISC + 48); if (rfl(f[0] & f[1])) break; }
  asm volatile("" ::: "memory");
  STAMP(11);
  bf16x8 pc[3][4];
#pragma unroll
  for (int k = 0; k < 4; ++k)
#pragma unroll
    for (int i = 0; i < 3; ++i) pc[i][k] = LDF(le + (i ? i + 1 : 0) * UB + k * 1024);
  __builtin_amdgcn_sched_barrier(0);
#pragma unroll
  for (int k = 0; k < 4; ++k)
#pragma unroll
    for (int i = 0; i < 3; ++i) { acc[2][k] = MFMA(BF8(s0), pc[i][k], acc[2][k]); acc[3][k] = MFMA(BF8(s1), pc[i][k], acc[3][k]); }
#pragma unroll
  for (int k = 0; k < 4; ++k) {
    u32x4 ov;
#pragma unroll
    for (int c = 0; c < 4; ++c) {
      const f32x2 r = f32x2{acc[2 + (c >> 1)][k][(c & 1) * 2], acc[2 + (c >> 1)][k][(c & 1) * 2 + 1]} * gf[k][c] + xf[k][c];
      ov[c] = pack2(r[0], r[1]);
    }
    __builtin_amdgcn_raw_buffer_store_b128(ov, rout, xo + k * 16 * D * 2, 0, OPOL);
  }
  STAMP(8);
  STAMP_OUT();
}

#define BCAST(v, k) ((unsigned)__builtin_amdgcn_mov_dpp((int)(v), 0x150 + (k), 0xf, 0xf, true))
template <int K0, int K1>
__device__ __forceinline__ void expand(u32x4 (&dg)[16], unsigned xs, const unsigned (&sel)[4]) {
  if constexpr (K0 < K1) {
    const unsigned v = BCAST(xs, K0);
    dg[K0] = u32x4{__builtin_amdgcn_perm(0u, v, sel[0]), __builtin_amdgcn_perm(0u, v, sel[1]), __builtin_amdgcn_perm(0u, v, sel[2]), __builtin_amdgcn_perm(0u, v, sel[3])};
    asm volatile("" : "+v"(dg[K0]));
    expand<K0 + 1, K1>(dg, xs, sel);
  }
}
template <int K0, int K1, int N>
__device__ __forceinline__ void gemv(f32x4& ae, f32x4& ao, unsigned xs, const unsigned (&sel)[4], const u32x4 (&w)[N]) {
  if constexpr (K0 < K1) {
    const unsigned v = BCAST(xs, K0);
    const bf16x8 dg = BF8((u32x4{__builtin_amdgcn_perm(0u, v, sel[0]), __builtin_amdgcn_perm(0u, v, sel[1]),
                                 __builtin_amdgcn_perm(0u, v, sel[2]), __builtin_amdgcn_perm(0u, v, sel[3])}));
    ae = MFMA(dg, BF8(w[2 * K0]), ae); ao = MFMA(dg, BF8(w[2 * K0 + 1]), ao);
    gemv<K0 + 1, K1, N>(ae, ao, xs, sel, w);
  }
}

__device__ __forceinline__ void helper(int hw, int lane, int b, char* lds, rsrc_t rws, rsrc_t rx, const u16* wr,
                                       const u16* wgu, const u16* wdn, const u16* gq, const char* code, int tp1, int tq1 STAMP_ARG) {
  STAMP_DECL;
  int nb = 0, lim = 14;
  unsigned tch = 0;
  volatile LAS unsigned* misc = (volatile LAS unsigned*)(lds + L_MISC);
  __builtin_amdgcn_s_setprio(3);
  const int z = tq1 >> 3, te = tq1 & 7;
  const int lc = lane & 15, lq = lane >> 4;
  const int r1 = rot1(tp1, tq1), kb = r1 >> 2, amo = WS_AMAX + (tq1 * 16 + tp1) * 128;
#define JOIN() do { HPRE(); BAR(); HSTAMP(); ++nb; } while (0)
#define NEXT() do { if (nb < lim) JOIN(); } while (0)
#define UNTIL(m) do { _Pragma("unroll 1") while (nb < (m)) JOIN(); } while (0)
#define POLLVM(m) do { _Pragma("unroll 1") while (vm_now() > (m) && nb < lim) JOIN(); wait_vm<m>(); } while (0)
  unsigned tag, key, sg; int idx;
  float sw;
  const int rg = b / 28, cg = b - rg * 28;
  const int fg = b >> 3, eg = b & 7;
  u32x4 w[32], wd[14];
  const rsrc_t rgu = mk_rsrc(wgu + (size_t)(rg & 7) * 128 * F + cg * 128, b < 224 ? 128 * F * 2 : 0);
  const rsrc_t rd = mk_rsrc(wdn + (size_t)(fg * 112 + hw * 56) * D + eg * 128, 56 * D * 2);
  const int wvo = lq * 32 * F * 2 + lc * 16, dvo = lq * 14 * D * 2 + lc * 16;
#define LOADW(k0, k1) do { _Pragma("unroll") for (int k = k0; k < (k1); ++k) \
    w[k] = __builtin_amdgcn_raw_buffer_load_b128(rgu, wvo, k * F * 2, NT); asm volatile("" ::: "memory"); } while (0)
#define LOADD(k0, k1) do { _Pragma("unroll") for (int k = k0; k < (k1); ++k) \
    wd[k] = __builtin_amdgcn_raw_buffer_load_b128(rd, dvo, k * D * 2, NT); asm volatile("" ::: "memory"); } while (0)
  const rsrc_t rq = mk_rsrc(gq, QROWS * S * 2);
  const unsigned kt = ktab(tp1);
  const int trow = (te >> 2) * 1024 + (te & 3) * 32 + hw * 16;
  const int t1o = (trow + ((lane >> 1) & 15)) * (S * 2) + (lane & 1) * 128;
#define QTOUCH(o, so) asm volatile("buffer_load_dword %0, %1, %2, %3 offen" : "+v"(tch) : "v"(o), "s"(rq), "s"(so) : "memory")
#define TCH(m) do { _Pragma("unroll") for (int i_ = 0; i_ < 8; ++i_) if ((m) >> i_ & 1) { \
      if (i_ < 7) QTOUCH(t1o, kstep0(kt, 2 * i_) * 128); \
      else QTOUCH(lane & 16 ? ((tp1 >> 3) * 1024 + te * 16 + lc) * (S * 2) + (kstep(kt, 15) ^ hw * 16) * 128 : (trow + lc) * (S * 2) + kstep(kt, 14) * 128, 0); } } while (0)
#define TCH7(i) do { if (te == 7) { QTOUCH((lane >> 1) * (S * 2) + (lane & 1) * 128, (((i) >> 1) * 1024 + hw * 64 + ((i) & 1) * 32) * (S * 2) + kstep0(kt, 4) * 128); } } while (0)
  {
    const rsrc_t rr = mk_rsrc(wr, D * 2);
    u32x4 wf[8];
#pragma unroll
    for (int i = 0; i < 8; ++i) wf[i] = __builtin_amdgcn_raw_buffer_load_b128(rr, lq * 16 + (i & 3) * 64, ((r1 + (i >> 2) * 2) & 15) * 128, 0);
    const unsigned epoch = __builtin_amdgcn_raw_buffer_load_b32(rws, lane == 1 ? (hw ? amo : WS_FLAG2 + b * 128) : lane == 2 ? amo + (WS_FLAG1 - WS_AMAX) + hw * 32768 : lane == 3 ? WS_H + b * 64 : WS_EPOCH, 0, SC1);
    const u32x4 ra = __builtin_amdgcn_raw_buffer_load_b128(rr, lane * 16, 0, 0), rb2 = __builtin_amdgcn_raw_buffer_load_b128(rr, lane * 16 + 1024, 0, 0);
    misc[lc] = 0u;
    u32x4 ones = {0x3f803f80u, 0x3f803f80u, 0x3f803f80u, 0x3f803f80u};
    const int sx = (te >> 1) * 32 + hw * 16, sv = (tp1 >> 1 & 3) * 32 + hw * 16;
    int xa[2], wa[2];
    xa[0] = (sx + lc) * 128 + (lq ^ (lc & 7)) * 16; wa[0] = xa[0] + (sv - sx) * 128 + UB;
    xa[1] = xa[0] ^ 64; wa[1] = wa[0] ^ 64;
    asm volatile("" : "+v"(ones));
    f32x4 d1 = {0.f, 0.f, 0.f, 0.f}, d2 = d1, d3 = d1, d4 = d1;
    HST(9);
#pragma unroll
    for (int u = 0; u < 4; ++u) {
      if (u == 2) { wa[0] += 4 * UB; wa[1] += 4 * UB; }
      if (u == 3) { xa[0] += 7 * UB; xa[1] += 7 * UB; }
      PIN4(xa[0], xa[1], wa[0], wa[1]);
      WAIT_LGKM0();
      JOIN();
      const unsigned tj = (unsigned)__builtin_amdgcn_s_memrealtime();
      bf16x8 xf[2], vf[2];
#pragma unroll
      for (int h = 0; h < 2; ++h) { xf[h] = LDF(xa[h] + (u == 3 ? 0 : XI(u) * UB)); vf[h] = LDF(wa[h] + (u & 1) * 3 * UB); }
      __builtin_amdgcn_sched_barrier(0);
      if (u) LOADW((int)(SWT >> 8 * u & 63), (int)(SWT >> 8 * u + 8 & 63));
      if (u) TCH7(u - 1);
#pragma unroll
      for (int h = 0; h < 2; ++h) {
        d2 = MFMA(xf[h], xf[h], d2); d4 = MFMA(BF8(ones), xf[h], d4); d3 = MFMA(BF8(ones), vf[h], d3); d1 = MFMA(BF8(wf[2 * u + h]), xf[h], d1);
      }
      PIN4(d1, d2, d3, d4);
      if (u == 0) {
        tag = mk_tag(epoch);
        asm volatile("" : "+s"(tag));
        misc[2] = tag;
        const f32x4 d5 = MFMA(BF8(rb2), BF8(ones), MFMA(BF8(ra), BF8(ones), (f32x4{0.f, 0.f, 0.f, 0.f})));
        const int s4 = __builtin_bit_cast(int, (d5[0] + d5[1]) + (d5[2] + d5[3]));
#define RL(l) __builtin_bit_cast(float, __builtin_amdgcn_readlane(s4, l))
        sw = (RL(0) + RL(16)) + (RL(32) + RL(48));
        asm volatile("" : "+v"(sw));
        LOADW(0, (int)(SWT >> 8 & 63));
      }
      if (PI >> 8 * u & 255) {
#pragma unroll 1
        while ((unsigned)__builtin_amdgcn_s_memrealtime() - tj < (PI >> 8 * u & 255)) ;
      }
      __builtin_amdgcn_sched_barrier(0);
    }
    JOIN();
    const float sq = (lc & 2) ? ((lc & 1) ? d2[3] : d2[2]) : ((lc & 1) ? d2[1] : d2[0]);
    __builtin_amdgcn_raw_buffer_store_b128(u32x4{__float_as_uint(d4[0]), __float_as_uint(sq), __float_as_uint(d1[0]), tag}, rws,
                                           lq == lc >> 2 ? WS_XP + (tp1 * 4 + kb) * 2048 + (sx + lc) * 16 : -1, 0, SC1);
    __builtin_amdgcn_raw_buffer_store_b64(u32x2{__float_as_uint(d3[0]), tag}, rws, lane < 16 ? WS_WP + (tq1 * 4 + kb) * 1024 + (sv + lc) * 8 : -1, 0, SC1);
    __builtin_amdgcn_sched_barrier(0);
    LOADW((int)(SWT >> 32 & 63), (int)(SWT >> 40 & 63));
  }
  const int srow = hw * 32 + (lane & 3) + 2 * (lane & 44) + ((lane >> 2) & 4);
  {
    const int gxo = WS_XP + tp1 * 8192 + (hw * 64 + lane) * 16, gwo = WS_WP + tq1 * 4096 + (hw * 64 + lane) * 8;
    u32x4 gx[4]; u32x2 gw[4];
#define GATHER() asm volatile("buffer_load_dwordx4 %0, %8, %10, 0 offen sc1\n\tbuffer_load_dwordx4 %1, %8, %10, 0 offen offset:2048 sc1\n\t" \
                 "buffer_load_dwordx4 %2, %8, %10, %11 offen sc1\n\tbuffer_load_dwordx4 %3, %8, %10, %11 offen offset:2048 sc1\n\t" \
                 "buffer_load_dwordx2 %4, %9, %10, 0 offen sc1\n\tbuffer_load_dwordx2 %5, %9, %10, 0 offen offset:1024 sc1\n\t" \
                 "buffer_load_dwordx2 %6, %9, %10, 0 offen offset:2048 sc1\n\tbuffer_load_dwordx2 %7, %9, %10, 0 offen offset:3072 sc1" \
                 : "=&v"(gx[0]), "=&v"(gx[1]), "=&v"(gx[2]), "=&v"(gx[3]), "=&v"(gw[0]), "=&v"(gw[1]), "=&v"(gw[2]), "=&v"(gw[3]) \
                 : "v"(gxo), "v"(gwo), "s"(rws), "s"(4096) : "memory")
    UNTIL(sched(T1_T, te));
    GATHER();
    LOADW((int)(SWT >> 40), NWC);
    wait_vm<NWC - (int)(SWT >> 40)>();
    TCH7(3);
#pragma unroll 1
    for (int spins = 0;; ++spins) {
      asm volatile("" : "+v"(gx[0]), "+v"(gx[1]), "+v"(gx[2]), "+v"(gx[3]), "+v"(gw[0]), "+v"(gw[1]), "+v"(gw[2]), "+v"(gw[3]) :: "memory");
      unsigned bad = 0;
#pragma unroll
      for (int q = 0; q < 4; ++q) bad |= (gx[q][3] ^ tag) | (gw[q][1] ^ tag);
      if (__builtin_expect(__all(bad == 0) || spins > SPIN_CAP / 64, 1)) break;
      GATHER();
      NEXT();
      POLLVM(0);
    }
    HST(3);
#define SUM4(v, c) ((__uint_as_float(v[0][c]) + __uint_as_float(v[1][c])) + (__uint_as_float(v[2][c]) + __uint_as_float(v[3][c])))
    float sum = SUM4(gx, 0), sq = SUM4(gx, 1), dot = SUM4(gx, 2);
    *(LAS float*)(lds + L_MB + 1024 + srow * 4) = SUM4(gw, 0);
    asm volatile("" : "+v"(sum), "+v"(sq), "+v"(dot));
    const float mn = sum * (1.0f / D);
    const float var = fmaxf(fmaf(-mn, mn, sq * (1.0f / D)), 0.f);
    const float mbv = rbf(mn), den = rbf(__builtin_amdgcn_sqrtf(rbf(rbf(var) + 1e-5f))), rden = __builtin_amdgcn_rcpf(den);
    *(LAS float*)(lds + L_MB + srow * 4) = -mbv;
    *(LAS float*)(lds + L_MB + 512 + srow * 4) = rden;
    *(LAS float*)(lds + L_MB + 1536 + srow * 4) = z ? -rden : rden;
    asm volatile("" ::: "memory");
    misc[4 + hw] = 1u;
    float rs = (dot - mbv * sw) * rden;
    sg = pack2(mbv, den);
    asm volatile("" : "+v"(rs), "+v"(sg));
    lim = NBAR;
    const unsigned rb = pack2(rs, rs) & 0xffffu;
    unsigned srt = (rb & 0x8000u) ? (~rb & 0xffffu) : (rb | 0x8000u);
    if (rs != rs) srt = 0u;
    key = (srt << 16) | (unsigned)(0xffff - tp1 * 128 - srow);
    const unsigned mk = wave_maxu(key);
    if (key == mk) __builtin_amdgcn_raw_buffer_store_b128(u32x4{key, tag, sg, tag}, rws, amo + hw * 16, 0, SC1);
    HST(2);
    UNTIL(WB_AT + (WBS >> 4 * te & 3));
    TCH(3);
    LOADW(NWC, 32);
  }
  unsigned sel[4];
#pragma unroll
  for (int d = 0; d < 4; ++d) {
    const unsigned src = (lc & 2) ? 0x0302u : 0x0100u;
    sel[d] = (lc >> 2) == d ? ((lc & 1) ? (src << 16 | 0x0c0cu) : (0x0c0c0000u | src)) : 0x0c0c0c0cu;
  }
  PIN4(sel[0], sel[1], sel[2], sel[3]);
  JOIN();
  const int hc = fg * 112 + hw * 56 + (lq * 7 + min(lc, 6)) * 2;
  int hio = WS_H + (hc & ~127) * 4 + ((hc & 6) * 8 + ((hc >> 3) & 15)) * 8;
  const int hs = min(b, 223) * 8 + hw * 4 + ((lane >> 3) & 3);
  int guo = WS_GU + ((lane & 7) * 2 + (lane >> 5)) * F * 8 + hs * 16, hoo = b < 224 && !(lane & 39) ? WS_H + hs * 8 : -1;
  asm volatile("" : "+v"(hio), "+v"(hoo));
  int gao = WS_AMAX + tq1 * 2048 + ((lane >> 1) & 15) * 128 + (lane & 1) * 16, gso = b < 224 ? WS_GU + ((rg * 2 + hw) * F + cg * 128) * 8 + lane * 16 : -1;
  asm volatile("" : "+v"(guo), "+v"(gao), "+v"(gso));
  UNTIL(15);
#pragma unroll 1
  for (int spins = 0;; ++spins) {
    const u32x4 am = __builtin_amdgcn_raw_buffer_load_b128(rws, gao, 0, SC1);
    NEXT();
    UNTIL(17);
#pragma unroll 1
    while (vm_now() > 0 && nb < lim && nb != 19 && nb != 17) JOIN();
    key = am[0]; sg = am[2];
    if (__builtin_expect(__all(((am[1] ^ tag) | (am[3] ^ tag)) == 0) || spins > SPIN_CAP / 64, 1)) break;
  }
  {
    const unsigned mk = wave_maxu(key);
    sg = __builtin_amdgcn_readlane(sg, __builtin_ctzll(__ballot(key == mk)));
    key = mk;
  }
  idx = min(0xffff - (int)(key & 0xffffu), S - 1);
  const unsigned xe = __builtin_amdgcn_raw_buffer_load_b32(rx, (idx * D + (rg & 7) * 128) * 2 + lane * 4, 0, 0);
  misc[0] = idx + 1;
  HST(7);
  if (nb != 19) NEXT();
#pragma unroll 1
  while (vm_now() > 0 && nb < lim && nb != 19) JOIN();
  {
    const unsigned xs = ln_pair(xe, -lo2f(sg), __builtin_amdgcn_rcpf(hi2f(sg)));
    if (b == 0 && lane == 0 && hw == 1) __builtin_amdgcn_raw_buffer_store_b32(tag, rws, WS_EPOCH, 0, SC1);
    u32x4 dg[16];
    expand<0, 7>(dg, xs, sel);
    if (nb != 19) NEXT();
    expand<7, 16>(dg, xs, sel);
    UNTIL(19);
    const bool idle = nb == 19;
    if (!idle) NEXT();
    f32x4 ae = {0.f, 0.f, 0.f, 0.f}, ao = ae;
#pragma unroll
    for (int k = 0; k < 16; ++k) {
      ae = MFMA(BF8(dg[k]), BF8(w[2 * k]), ae); ao = MFMA(BF8(dg[k]), BF8(w[2 * k + 1]), ao);
      if (k == 7) { asm volatile("" : "+v"(ae), "+v"(ao)); if (__builtin_expect(!idle, 0)) NEXT(); }
    }
    __builtin_amdgcn_raw_buffer_store_b128(u32x4{__float_as_uint(ae[0] + ao[2]), tag, __float_as_uint(ae[1] + ao[3]), tag}, rws, gso, 0, SC1);
  }
  HST(4);
  if (nb == 19) JOIN();
  {
    const int m0_ = 20 + (te & 1);
#define LATE(i) do { UNTIL(m0_ + (i)); __builtin_amdgcn_s_sleep(BSL); } while (0)
    LATE(0);
    TCH(0x18);
    LATE(2);
    u32x4 gu;
    asm volatile("buffer_load_dwordx4 %0, %1, %2, 0 offen sc1" : "=&v"(gu) : "v"(guo), "s"(rws) : "memory");
    TCH(0x20);
    LOADD(0, WD1);
    NEXT();
    asm volatile("s_waitcnt vmcnt(%1)" : "+v"(gu) : "n"(WD1 + 1) : "memory");
#pragma unroll 1
    for (int spins = 0; !__all(((gu[1] ^ tag) | (gu[3] ^ tag)) == 0) && spins < SPIN_CAP / 64; ++spins) {
      asm volatile("buffer_load_dwordx4 %0, %1, %2, 0 offen sc1" : "=&v"(gu) : "v"(guo), "s"(rws) : "memory");
      NEXT();
      POLLVM(0);
      asm volatile("" : "+v"(gu) :: "memory");
    }
    {
      float s0 = __uint_as_float(gu[0]), s1 = __uint_as_float(gu[2]);
      DPP_ADD(s0, 0xb1, 0xf, true); DPP_ADD(s1, 0xb1, 0xf, true); DPP_ADD(s0, 0x4e, 0xf, true); DPP_ADD(s1, 0x4e, 0xf, true);
      DPP_ADD(s0, 0x141, 0xf, true); DPP_ADD(s1, 0x141, 0xf, true);
      const unsigned gp = pack2(s0, s1);
      const unsigned up = __builtin_amdgcn_permlane32_swap(gp, gp, false, false)[1];
      const unsigned sl2 = pack2(silu_fast(lo2f(gp)), silu_fast(hi2f(gp)));
      __builtin_amdgcn_raw_buffer_store_b64(u32x2{pack2(lo2f(sl2) * lo2f(up), hi2f(sl2) * hi2f(up)), tag}, rws, hoo, 0, SC1);
    }
    HST(8);
    LATE(4);
    TCH(0x40);
    LOADD(WD1, WD2);
    if (!hw && lane < (CODE_LINES + 7) / 8) {
      const int co = min(((b >> 3) & 7) * ((CODE_LINES + 7) / 8) + lane, CODE_LINES - 1) * 128;
      asm volatile("global_load_dword %0, %1, off" : "+v"(tch) : "v"(code + co) : "memory");
    }
    LATE(6);
    u32x2 hv;
    asm volatile("buffer_load_dwordx2 %0, %1, %2, 0 offen sc1" : "=&v"(hv) : "v"(hio), "s"(rws) : "memory");
    TCH(0x80);
    LOADD(WD2, 14);
    NEXT();
    asm volatile("s_waitcnt vmcnt(%1)" : "+v"(hv) : "n"(14 - WD2 + 1) : "memory");
#pragma unroll 1
    for (int spins = 0; !__all(hv[1] == tag) && spins < SPIN_CAP / 64; ++spins) {
      asm volatile("buffer_load_dwordx2 %0, %1, %2, 0 offen sc1" : "=&v"(hv) : "v"(hio), "s"(rws) : "memory");
      NEXT();
      POLLVM(0);
      asm volatile("" : "+v"(hv) :: "memory");
    }
    f32x4 ae = {0.f, 0.f, 0.f, 0.f}, ao = ae;
    gemv<0, 5>(ae, ao, hv[0], sel, wd);
    asm volatile("" : "+v"(ae), "+v"(ao));
    NEXT();
    POLLVM(0);
    gemv<5, 7>(ae, ao, hv[0], sel, wd);
    f32x2 a = {ae[0] + ao[2], ae[1] + ao[3]};
    HST(5);
    if (hw == 1) {
      *(LAS f32x2*)(lds + L_CMB + lane * 8) = a;
      asm volatile("" ::: "memory");
      misc[6] = 1u;
    } else {
      f32x2 o;
#pragma unroll 1
      for (int spins = 0; spins < SPIN_CAP; ++spins) {
        NEXT();
        const unsigned f = misc[6];
        o = *(volatile LAS f32x2*)(lds + L_CMB + lane * 8);
        if (rfl(f)) break;
      }
      __builtin_amdgcn_raw_buffer_store_b128(u32x4{__float_as_uint(a[0] + o[0]), tag, __float_as_uint(a[1] + o[1]), tag}, rws,
                                             WS_PART + (fg * D + eg * 128) * 8 + lane * 16, 0, SC1);
      HST(6);
    }
  }
  if (hw == 1 && (idx >> 7) == tp1) {
    const unsigned srt = key >> 16;
    const float r = lo2f((srt & 0x8000u) ? (srt & 0x7fffu) : (~srt & 0xffffu));
    const float rsel = rbf(1.0f / (1.0f + __expf(-r)));
    const int cp = lane & 31;
    const int e = z * 64 + cp * 2;
    const int po = WS_PART + ((lane >> 5) * D + te * 128) * 8 + ((e & 6) * 8 + (e >> 3)) * 16;
    float y0, y1;
    UNTIL(NBAR);
    wait_sent((LAS char*)lds);
#pragma unroll 1
    for (int spins = 0;; ++spins) {
#pragma unroll
      for (int k = 0; k < 16; ++k) w[k] = __builtin_amdgcn_raw_buffer_load_b128(rws, po, k * 2 * D * 8, SC1);
      asm volatile("" ::: "memory");
      unsigned bad = 0;
      y0 = 0.f; y1 = 0.f;
#pragma unroll
      for (int k = 0; k < 16; ++k) { bad |= (w[k][1] ^ tag) | (w[k][3] ^ tag); y0 += __uint_as_float(w[k][0]); y1 += __uint_as_float(w[k][2]); }
      if (__all(bad == 0) || spins > SPIN_CAP / 64) break;
    }
    { const auto p0 = __builtin_amdgcn_permlane32_swap(__float_as_uint(y0), __float_as_uint(y0), false, false);
      const auto p1 = __builtin_amdgcn_permlane32_swap(__float_as_uint(y1), __float_as_uint(y1), false, false);
      y0 = __uint_as_float(p0[0]) + __uint_as_float(p0[1]); y1 = __uint_as_float(p1[0]) + __uint_as_float(p1[1]); }
    *(LAS f32x2*)(lds + L_UPD + cp * 8) = f32x2{rbf(rbf(y0) * rsel), rbf(rbf(y1) * rsel)};
    asm volatile("" ::: "memory");
    misc[1] = 1u;
    HST(6);
  }
  HST(10);
  POLLVM(0);
  asm volatile("" : "+v"(tch));
  STAMP_OUT();
}

typedef __attribute__((ext_vector_type(8))) u64 u64x8;
typedef __attribute__((ext_vector_type(2))) u64 u64x2;
__global__ __launch_bounds__(512, 2) void fused(const u16*, const u16*, const u16*, const u16*, char*, const u16*, const u16*, const u16*,
                                                const u16*, const u16*, u16*) {
  extern __shared__ __attribute__((aligned(16))) char lds[];
  u64x8 a0; u64x2 a1; u64 a2;
  u64 pc;
  asm volatile("s_load_dwordx16 %0, %4, 0\n\ts_load_dwordx4 %1, %4, 64\n\ts_load_dwordx2 %2, %4, 80\n\ts_getpc_b64 %3"
               : "=&s"(a0), "=&s"(a1), "=&s"(a2), "=&s"(pc) : "s"(__builtin_amdgcn_kernarg_segment_ptr()) : "memory");
  __builtin_amdgcn_sched_barrier(0);
  const char* code = (const char*)(pc & ~255ull);
  unsigned tch = 0;
  if (threadIdx.x < (CODE_LINES + CDIV - 1) / CDIV) {
    const int co = min((int)(((blockIdx.x >> 3) & (CDIV - 1)) + threadIdx.x * CDIV), CODE_LINES - 1) * 128;
    asm volatile("global_load_dword %0, %1, %2" : "+v"(tch) : "v"(co), "s"(code) : "memory");
  }
  __builtin_amdgcn_sched_barrier(0);
  const int wid = __builtin_amdgcn_readfirstlane(threadIdx.x >> 6), lane = threadIdx.x & 63;
  const int b = blockIdx.x, g = b & 7, i = b >> 3;
  const int tp1 = (g & 3) * 4 + (i & 3), tq1 = (g >> 2) * 8 + (i >> 2);
  const int z = tq1 >> 3, te = tq1 & 7;
  const int L = wid & 1, rsub = lane >> 3;
  int v1 = ((L ? te : tp1) * 128 + 8 * (rsub >> 2) + (rsub & 3)) * (D * 2) + ((lane & 7) ^ rsub) * 16, ko = rot1(tp1, tq1) * 128;
  unsigned g1n = (L ? D : S) * D * 2;
  int role = wid >> 1;
  asm volatile("" : "+v"(v1));
  asm volatile("" : "+s"(ko), "+s"(g1n), "+s"(role));
  __builtin_amdgcn_sched_barrier(0);
  asm volatile("s_waitcnt lgkmcnt(0)" : "+s"(a0), "+s"(a1), "+s"(a2) :: "memory");
  const u16 *x = (const u16*)a0[0], *wr = (const u16*)a0[3], *sre = (const u16*)a0[5], *sim = (const u16*)a0[6], *wg = (const u16*)a0[7];
  const u16 *wu = (const u16*)a1[0], *wdn = (const u16*)a1[1];
  char* ws = (char*)a0[4];
  u16* out = (u16*)a2;
  const rsrc_t rws = mk_rsrc(ws, WS_END);
  if (__builtin_expect(role == 2, 1)) {
    loader(L, lane, (LAS char*)lds, mk_rsrc((const void*)(L ? (z ? a0[2] : a0[1]) : a0[0]), g1n), v1, ko, (z ? sim : sre) + (size_t)(tp1 & 7) * 128 * S,
           ws + WS_ABT + (size_t)tq1 * ABT_ST, rws, WS_FLAG1 + tq1 * 2048, b, tp1 STAMP_PASS);
  } else if (wid < 4) {
    consumer(wid, lane, b, lds, rws, mk_rsrc(out, S * D * 2), tch, tp1, tq1 STAMP_PASS);
  } else {
    const int hw = wid - 6;
    helper(hw, lane, b, lds, rws, mk_rsrc(x, S * D * 2), wr, hw ? wu : wg, wdn,
           (z ? sim : sre) + (size_t)(tp1 & 7) * 128 * S, code, tp1, tq1 STAMP_PASS);
  }
}

extern "C" void kernel_launch(void* const* d_in, const int* in_sizes, int n_in, void* d_out, int out_size,
                              void* d_ws, size_t ws_size, hipStream_t stream) {
  (void)hipFuncSetAttribute((const void*)fused, hipFuncAttributeMaxDynamicSharedMemorySize, LDS_BYTES);
  fused<<<256, 512, LDS_BYTES, stream>>>((const u16*)d_in[0], (const u16*)d_in[3], (const u16*)d_in[4], (const u16*)d_in[6], (char*)d_ws,
                                         (const u16*)d_in[1], (const u16*)d_in[2], (const u16*)d_in[7], (const u16*)d_in[8],
                                         (const u16*)d_in[9], (u16*)d_out);
}
```

```cpp
#define CODE_LINES 281
#include <hip/hip_runtime.h>
#include <hip/hip_bf16.h>
#include <stdint.h>

typedef __attribute__((ext_vector_type(8))) short bf16x8;
typedef __attribute__((ext_vector_type(4))) float f32x4;
typedef __attribute__((ext_vector_type(2))) float f32x2;
typedef __attribute__((ext_vector_type(4))) unsigned u32x4;
typedef __attribute__((ext_vector_type(2))) unsigned u32x2;
typedef __attribute__((ext_vector_type(2))) __bf16 bf16x2;
typedef unsigned short u16;
typedef unsigned long long u64;
typedef __amdgpu_buffer_rsrc_t rsrc_t;
#define LAS __attribute__((address_space(3)))

constexpr int S = 2048, D = 1024, F = 3584;
constexpr int UB = 16384;
constexpr int SLOT = 3 * UB;
constexpr int L_MISC = 9 * UB;
constexpr int L_UPD = L_MISC + 64;
constexpr int L_MB = L_MISC + 512;
constexpr int L_CMB = L_MB + 2048;
constexpr int LDS_BYTES = L_CMB + 512;
constexpr int NBAR = 17 + 14 + 3;
constexpr int SC1 = 16, NT = 2;

constexpr unsigned WS_EPOCH = 0;
constexpr unsigned WS_XP = 4096;
constexpr unsigned WS_WP = WS_XP + 16 * 8192;
constexpr unsigned WS_AMAX = WS_WP + 16 * 4096;
constexpr unsigned WS_FLAG1 = WS_AMAX + 32768;
constexpr unsigned WS_FLAG2 = WS_FLAG1 + 65536;
constexpr unsigned WS_GU = WS_FLAG2 + 32768;
constexpr unsigned WS_H = WS_GU + 8 * 2 * F * 8;
constexpr unsigned WS_PART = WS_H + 16384;
constexpr unsigned ABT_ST = 128 * S * 2;
constexpr unsigned WS_ABT = WS_PART + 32 * D * 8;
constexpr unsigned WS_XCH = (WS_ABT + 16 * ABT_ST + 0x1ffff) & ~0x1ffffu;
constexpr int XB = 131072;
constexpr unsigned WS_END = WS_XCH + 256 * XB;

#define STAMP(i)
#define HSTAMP()
#define HPRE()
#define HST(i)
#define STAMP_DECL
#define STAMP_ARG
#define STAMP_PASS
#define STAMP_OUT()

__device__ __forceinline__ float lo2f(unsigned v) { return __uint_as_float(v << 16); }
__device__ __forceinline__ float hi2f(unsigned v) { return __uint_as_float(v & 0xffff0000u); }
__device__ __forceinline__ unsigned pack2(float a, float b) {
  return __builtin_bit_cast(unsigned, __builtin_convertvector(f32x2{a, b}, bf16x2));
}
__device__ __forceinline__ float rbf(float f) { return lo2f(pack2(f, f)); }
__device__ __forceinline__ unsigned rfl(unsigned v) { return __builtin_amdgcn_readfirstlane(v); }
__device__ __forceinline__ rsrc_t mk_rsrc(const void* p, unsigned bytes) {
  return __builtin_amdgcn_make_buffer_rsrc((void*)p, 0, bytes, 0x00020000);
}
#define DPP_ADD(v, ctrl, rm, bc) v += __builtin_bit_cast(float, __builtin_amdgcn_update_dpp(0, __builtin_bit_cast(int, v), ctrl, rm, 0xf, bc))
#define DPP_MAXU(v, ctrl, rm, bc) v = max(v, (unsigned)__builtin_amdgcn_update_dpp(0, (int)v, ctrl, rm, 0xf, bc))
__device__ __forceinline__ unsigned wave_maxu(unsigned v) {
  DPP_MAXU(v, 0x111, 0xf, true); DPP_MAXU(v, 0x112, 0xf, true); DPP_MAXU(v, 0x114, 0xf, true); DPP_MAXU(v, 0x118, 0xf, true);
  DPP_MAXU(v, 0x142, 0xa, false); DPP_MAXU(v, 0x143, 0xc, false);
  return (unsigned)__builtin_amdgcn_readlane((int)v, 63);
}
template <int N> __device__ __forceinline__ void wait_vm() { asm volatile("s_waitcnt vmcnt(%0)" :: "n"(N) : "memory"); }
#define WAIT_LGKM0() asm volatile("s_waitcnt lgkmcnt(0)" ::: "memory")
#define BAR() do { asm volatile("" ::: "memory"); __builtin_amdgcn_s_barrier(); asm volatile("" ::: "memory"); } while (0)
#define MFMA(a, b, c) __builtin_amdgcn_mfma_f32_16x16x32_bf16(a, b, c, 0, 0, 0)
#define BF8(v) __builtin_bit_cast(bf16x8, v)
constexpr int SPIN_CAP = 1 << 20;
#define T1_T 0x090a09090a0a0909ull
#define NWC 30
#define WB_AT 12
#define WBS 0x11001100
#define SWT 0x100d0a070400ull
#define PI 0x2e32363c
#define BSL 13
#define WD1 5
#define WD2 10
constexpr int BZ = 1, BH = 2, BD = 3;
__device__ __forceinline__ int sched(u64 tab, int te) { return (int)(tab >> (te * 8)) & 255; }
#define PIN4(a, b, c, d) asm volatile("" : "+v"(a), "+v"(b), "+v"(c), "+v"(d))
__device__ __forceinline__ unsigned vm_now() { const unsigned r = __builtin_amdgcn_s_getreg(7 | (31 << 11)); return (r & 15u) | ((r >> 18) & 0x30u); }
__device__ __forceinline__ unsigned mk_tag(unsigned epoch) { return rfl(epoch) * 0x9E3779B1u + 0x7F4A7C15u; }

template <int LD, int AUX, int MODE, int NP = 16, int J0 = 0>
__device__ __forceinline__ void issue(rsrc_t r, int voff, int koff, LAS char* dst) {
#pragma unroll
  for (int j = J0; j < NP; ++j) {
    const int so = MODE == 2 ? j * 1024 : MODE == 1 ? (32 * (j >> 3) + 64 * ((j >> 2) & 1) + 16 * (j & 1) + 4 * ((j >> 1) & 1)) * LD : j * 8 * LD;
    __builtin_amdgcn_raw_ptr_buffer_load_lds(r, (LAS void*)(dst + j * 1024), 16, voff, koff + so, 0, AUX);
  }
}
__device__ __forceinline__ unsigned ktab(int tp1) { return (tp1 * 0x01010101u | ((tp1 & 12) ^ 4) * 0x10101010u) ^ (tp1 & 2 ? 0x33122100u : 0x33221100u); }
__device__ __forceinline__ int rot1(int tp1, int tq1) { return (8 * (tp1 + tq1) + 2 * (((tp1 >> 2) + 2 * (tq1 >> 3)) & 3)) & 15; }
__device__ __forceinline__ int kstep0(unsigned kt, int n) { return (int)(kt >> (n >> 1) * 4 & 15) * 2 + (n & 1); }
__device__ __forceinline__ int kstep(unsigned kt, int n) { return kstep0(kt, n == 3 ? 4 : n == 4 ? 3 : n); }
constexpr int QROWS = 1024 + 128;

__device__ __forceinline__ void wait_sent(LAS char* lds) {
#pragma unroll 1
  for (int spins = 0; spins < SPIN_CAP; ++spins) {
    const u32x4 f = *(volatile LAS u32x4*)(lds + L_MISC + 32);
    if (rfl(f[0] & f[1] & f[2] & f[3])) break;
    __builtin_amdgcn_s_sleep(1);
  }
  asm volatile("" ::: "memory");
}

__device__ __forceinline__ unsigned wait_flags(rsrc_t rws, int o, unsigned tag) {
  unsigned f;
#pragma unroll 1
  for (int spins = 0; spins < SPIN_CAP; ++spins) {
    asm volatile("buffer_load_dword %0, %1, %2, 0 offen sc1\n\ts_waitcnt vmcnt(0)" : "=&v"(f) : "v"(o), "s"(rws) : "memory");
    if (__all((f ^ tag) < 2u)) break;
    __builtin_amdgcn_s_sleep(1);
  }
  return (unsigned)__ballot(f != tag);
}
__device__ __forceinline__ void fetch_piece(rsrc_t rws, int vx, int o, unsigned places, LAS char* dst) {
#pragma unroll
  for (int w = 0; w < 4; ++w) issue<0, SC1, 2, 4>(rws, vx, o + (w >> 1) * 16384 + (w & 1) * 4096 + (places >> w & 1) * 8192, dst + w * 4096);
}

__device__ __forceinline__ void loader(int L, int lane, LAS char* lds, rsrc_t r1, int v1, int ko, const u16* gq, const char* gp, rsrc_t rws,
                                       int flag_off, int b, int tp1 STAMP_ARG) {
  STAMP_DECL;
  __builtin_amdgcn_s_setprio(2);
  const int rsub = lane >> 3, ch = ((lane & 7) ^ rsub) * 16;
  const rsrc_t rq = mk_rsrc(gq, QROWS * S * 2);
  const int h = tp1 >> 3;
  unsigned kt = ktab(tp1);
  int vq = rsub * (S * 2) + ch, vp = lane * 16;
  int qa = h * 1024 * (S * 2), qb = ((1 - h) * 1024 + L * 64) * (S * 2);
  int q_ = (L ? qa : qb) + tp1 * 256;
  LAS char* d_ = lds + (1 - L) * UB;
  volatile LAS unsigned* misc = (volatile LAS unsigned*)(lds + L_MISC);
  unsigned fl = 0, tag;
  {
    const unsigned utab = L ? 0x8541u : 0x7230u;
    STAMP(1);
    LAS char* dst = lds + L * UB;
#pragma unroll 1
    for (int t = 0; t < 4; ++t) {
      issue<D * 2, 0, 1>(r1, v1, ko, dst);
      ko = (ko + 128) & 2047;
      dst = lds + ((utab >> (t * 4 + 4)) & 15) * UB;
      if (t == 1 || t == 2) { wait_vm<16>(); BAR(); }
    }
    LAS char* dt[4];
#pragma unroll
    for (int u = 0; u < 4; ++u) dt[u] = lds + ((utab >> (u * 4)) & 15) * UB;
    asm volatile("" : "+s"(kt), "+s"(qa), "+s"(qb), "+s"(q_), "+s"(d_) : "s"(rq));
    asm volatile("" : "+v"(vq), "+v"(vp));
#pragma unroll 1
    for (int t = 0; t < 3; ++t) {
#pragma unroll
      for (int u = 0; u < 4; ++u) {
        issue<D * 2, 0, 1, 8>(r1, v1, ko, dt[u]);
        int kn = (ko + 128) & 2047;
        asm volatile("" : "+s"(kn), "+s"(dt[(u + 1) & 3]));
        if (u == 1) asm volatile("ds_read_b32 %0, %1" : "=v"(tag) : "v"(L_MISC + 8) : "memory");
        issue<D * 2, 0, 1, 16, 8>(r1, v1, ko, dt[u]);
        ko = kn;
        wait_vm<32>();
        BAR();
      }
    }
    asm volatile("s_waitcnt lgkmcnt(0)" : "+v"(tag) :: "memory");
  }
#define POLL(o) asm volatile("buffer_load_dword %0, %1, %2, 0 offen sc1" : "+v"(fl) : "v"(o), "s"(rws) : "memory")
#define POLLW(o) asm volatile("buffer_load_dword %0, %1, %2, 0 offen sc1\n\ts_waitcnt vmcnt(0)" : "+v"(fl) : "v"(o), "s"(rws) : "memory")
  {
    issue<S * 2, 0, 0>(rq, vq, q_, d_);
    const int q2 = qa + L * 64 * (S * 2) + kstep(kt, 2) * 128;
    wait_vm<32>();
    BAR();
    issue<S * 2, 0, 0, 8>(rq, vq, q2, lds + 2 * SLOT + L * 8192);
    wait_vm<24>();
    BAR();
    issue<S * 2, 0, 0>(rq, vq, q_ + 128, d_ + SLOT);
    tag = rfl(tag);
    BAR();
    wait_vm<16>();
    BAR();
    issue<S * 2, 0, 0, 8>(rq, vq, qb + kstep(kt, 2) * 128, lds + 2 * SLOT + UB + L * 8192);
    wait_vm<8>();
    BAR();
  }
  const int vx = (lane ^ (lane >> 2 & 12)) * 16;
  if (L) {
    int k_ = kstep(kt, 3) * 128, ka = qa + k_;
    LAS char* sl = lds;
#pragma unroll 1
    for (int n = 4; n < 16; ++n) {
      issue<S * 2, 0, 0>(rq, vq, ka, sl);
      const int kb = qb + k_;
      LAS char* const sb = sl + UB + 8192;
      k_ = kstep(kt, n) * 128; ka = qa + k_;
      sl = sl == lds + 2 * SLOT ? lds : sl + SLOT;
      asm volatile("" : "+s"(k_), "+s"(ka), "+s"(sl));
      issue<S * 2, 0, 0, 8>(rq, vq, kb, sb);
      wait_vm<24>();
      BAR();
    }
    issue<S * 2, 0, 0>(rq, vq, qa + (k_ ^ 2048), lds);
    const int f2o = WS_FLAG2 + (b ^ BZ) * 128 + 16 + (lane & 3) * 4;
    wait_vm<16>();
    BAR();
    wait_vm<0>();
    BAR();
    issue<S * 2, 0, 0, 8>(rq, vq, qa + 64 * (S * 2) + k_, lds + 4 * UB + 8192);
    wait_vm<0>();
    BAR();
    wait_sent(lds);
    const unsigned pl = wait_flags(rws, f2o, tag);
    STAMP(4);
    fetch_piece(rws, vx, WS_XCH + (b ^ BZ) * XB + 65536, pl, lds + 4 * UB);
    wait_vm<0>();
    misc[12] = 1u;
    STAMP(5);
  } else {
    const rsrc_t rp = mk_rsrc(gp, 128 * S * 2);
    unsigned mask = 0;
    const int fo = flag_off + 32768 + (((lane >> 2) & 7) == (tp1 & 7) ? (kt >> 28) ^ 8 : h * 8 + ((lane >> 2) & 7)) * 128 + (lane & 3) * 4;
#define KM_(k) (15u << 2 * ((k) & 14))
    const int fo1 = flag_off + (kt >> (lane & 4 ? 8 : 4) & 15) * 128 + (lane & 3) * 4;
    STAMP(2);
    {
      unsigned f1, f2;
      asm volatile("buffer_load_dword %0, %1, %2, 0 offen sc1" : "=&v"(f1) : "v"(fo1), "s"(rws) : "memory");
      issue<S * 2, 0, 0, 8>(rq, vq, qb + kstep(kt, 3) * 128, lds + UB);
      asm volatile("buffer_load_dword %0, %2, %3, 0 offen sc1\n\ts_waitcnt vmcnt(9)" : "=&v"(f2), "+v"(f1) : "v"(fo1), "s"(rws) : "memory");
      unsigned m1 = (unsigned)__ballot(f1 == tag);
#pragma unroll 1
      for (int spins = 0; (m1 & 15) != 15 && spins < SPIN_CAP; ++spins) {
        asm volatile("s_sleep 2\n\tbuffer_load_dword %0, %2, %3, 0 offen sc1\n\ts_waitcnt vmcnt(1)" : "+v"(f1), "+v"(f2) : "v"(fo1), "s"(rws) : "memory");
        m1 |= (unsigned)__ballot(f2 == tag);
        if ((m1 & 15) == 15) break;
        asm volatile("s_sleep 2\n\tbuffer_load_dword %0, %2, %3, 0 offen sc1\n\ts_waitcnt vmcnt(1)" : "+v"(f2), "+v"(f1) : "v"(fo1), "s"(rws) : "memory");
        m1 |= (unsigned)__ballot(f1 == tag);
      }
      STAMP(3);
      issue<0, SC1, 2>(rp, vp, kstep(kt, 2) * UB, lds + 2 * SLOT + 2 * UB);
      asm volatile("s_waitcnt vmcnt(16)" : "+v"(f1), "+v"(f2) :: "memory");
      m1 |= (unsigned)(__ballot(f1 == tag) | __ballot(f2 == tag));
      const bool p3 = (m1 & 0xf0) == 0xf0;
      if (!p3) {
        POLL(fo1);
        wait_vm<1>();
        BAR();
        asm volatile("s_waitcnt vmcnt(0)" : "+v"(fl) :: "memory");
#pragma unroll 1
        for (int spins = 0; ((unsigned)__ballot(fl == tag) & 0xf0) != 0xf0 && spins < SPIN_CAP; ++spins) POLLW(fo1);
      }
      POLL(fo);
      issue<0, SC1, 2>(rp, vp, kstep(kt, 3) * UB, lds + 2 * UB);
      if (p3) { wait_vm<17>(); BAR(); }
      asm volatile("s_waitcnt vmcnt(16)" : "+v"(fl) :: "memory");
    }
#define NEED(need) do { if (__builtin_expect(mask != ~0u, 0)) { asm volatile("" : "+v"(fl) :: "memory"); mask |= (unsigned)__ballot(fl == tag); \
      _Pragma("unroll 1") for (int spins = 0; (mask & (need)) != (need) && spins < SPIN_CAP; ++spins) { POLLW(fo); mask |= (unsigned)__ballot(fl == tag); } \
      if (mask != ~0u) POLL(fo); } } while (0)
    int k_ = kstep(kt, 4), kp = k_ * UB;
    LAS char* sl = lds + SLOT;
#pragma unroll 1
    for (int n = 5; n < 16; ++n) {
      NEED(KM_(k_));
      issue<0, SC1, 2>(rp, vp, kp, sl + 2 * UB);
      const int kb = qb + k_ * 128;
      LAS char* const sb = sl + UB;
      k_ = kstep0(kt, n); kp = k_ * UB;
      sl = sl == lds + 2 * SLOT ? lds : sl + SLOT;
      asm volatile("" : "+s"(k_), "+s"(kp), "+s"(sl));
      issue<S * 2, 0, 0, 8>(rq, vq, kb, sb);
      wait_vm<24>();
      BAR();
    }
    NEED(~0u);
    issue<0, SC1, 2>(rp, vp, (k_ ^ 16) * UB, lds + 2 * UB);
    wait_vm<16>();
    BAR();
    issue<0, SC1, 2>(rp, vp, k_ * UB, lds + UB);
    wait_vm<16>();
    BAR();
    issue<S * 2, 0, 0, 8>(rq, vq, qa + k_ * 128, lds + 4 * UB);
    const int f2o = WS_FLAG2 + (b ^ (lane & 4 ? BD : BH)) * 128 + (lane & 3) * 4;
    wait_vm<0>();
    BAR();
    wait_sent(lds);
    const unsigned pl = wait_flags(rws, f2o, tag);
    fetch_piece(rws, vx, WS_XCH + (b ^ BH) * XB, pl, lds + 6 * UB);
    fetch_piece(rws, vx, WS_XCH + (b ^ BD) * XB + 32768, pl >> 4, lds + 7 * UB);
    STAMP(4);
    wait_vm<0>();
    misc[13] = 1u;
    STAMP(5);
  }
  STAMP_OUT();
}

#define LDF(off) (*reinterpret_cast<const bf16x8*>(lds + (off)))
struct Frag { bf16x8 p[4], q[4]; };
#define SGB(m, n) __builtin_amdgcn_sched_group_barrier(m, n, 0)
__device__ __forceinline__ float silu_fast(float z) { return z * __builtin_amdgcn_rcpf(1.0f + __expf(-z)); }
__device__ __forceinline__ u32x4 pack8(const f32x4& a, const f32x4& c) {
  return u32x4{pack2(a[0], a[1]), pack2(a[2], a[3]), pack2(c[0], c[1]), pack2(c[2], c[3])};
}
__device__ __forceinline__ unsigned ln_pair(unsigned x, float nmb, float rden) {
  const unsigned d = pack2(lo2f(x) + nmb, hi2f(x) + nmb);
  return pack2(lo2f(d) * rden, hi2f(d) * rden);
}

template <bool BARRIER, bool LAST, bool ZERO, class X>
__device__ __forceinline__ void big_half(f32x4 (&a0)[4][4], f32x4 (&a1)[4][4], const bf16x8 (&pc)[4], bf16x8 (&pn)[4], bf16x8 (&q)[8],
                                         const char* lds, int qc, int pa, int qn, int qn2, X extra) {
  const f32x4 zero = {0.f, 0.f, 0.f, 0.f};
#pragma unroll
  for (int g = 0; g < 8; ++g) {
    if (g == 0) { q[5] = LDF(qc + UB + 2048); q[6] = LDF(qc + UB + 4096); }
    if (g == 1) q[7] = LDF(qc + UB + 6144);
    if (g == 3) { pn[0] = LDF(pa); pn[1] = LDF(pa + 2048); }
    if (g == 4) { pn[2] = LDF(pa + 4096); pn[3] = LDF(pa + 6144); }
    if (g == 5) { q[0] = LDF(qn); q[1] = LDF(qn + 2048); }
    if (g == 6) { q[2] = LDF(qn + 4096); q[3] = LDF(qn + 6144); }
    if (g == 7 && !LAST) q[4] = LDF(qn2);
#pragma unroll
    for (int i = 0; i < 4; ++i) {
      const int ii = (g & 1) ? 3 - i : i;
      if (g < 4) a0[ii][g] = MFMA(pc[ii], q[g], ZERO ? zero : a0[ii][g]); else a1[ii][g - 4] = MFMA(pc[ii], q[g], ZERO ? zero : a1[ii][g - 4]);
    }
    SGB(0x8, 1); SGB(0x100, 1); SGB(0x8, 1); SGB(0x100, 1); SGB(0x8, 2);
    __builtin_amdgcn_sched_barrier(0);
    if (g == 2 && BARRIER) { WAIT_LGKM0(); BAR(); }
    extra(g);
  }
}

template <bool JOBNEXT>
__device__ __forceinline__ void small_step(f32x4 (&a)[4][4], bf16x8 (&p0)[4], bf16x8 (&p1)[4], bf16x8 (&q)[8], const char* lds,
                                           int ph, int qh, int pn, int qn, int qn2) {
#pragma unroll
  for (int g = 0; g < 8; ++g) {
    if (g == 0) { q[4] = LDF(qh); p1[0] = LDF(ph); }
    if (g == 1) { p1[1] = LDF(ph + 2048); p1[2] = LDF(ph + 4096); }
    if (g == 2) { p1[3] = LDF(ph + 6144); q[5] = LDF(qh + 2048); }
    if (g == 3) { q[6] = LDF(qh + 4096); q[7] = LDF(qh + 6144); }
    if (g == 4) { p0[0] = LDF(pn); q[0] = LDF(qn); }
    if (g == 5) { p0[1] = LDF(pn + 2048); p0[2] = LDF(pn + (JOBNEXT ? 1024 : 4096)); if (JOBNEXT) q[1] = LDF(qn + 2048); }
    if (g == 6) { p0[3] = LDF(pn + (JOBNEXT ? 3072 : 6144)); if (JOBNEXT) { q[2] = LDF(qn + 4096); q[3] = LDF(qn + 6144); } else q[1] = LDF(qn + 2048); }
    if (g == 7) { if (JOBNEXT) { q[4] = LDF(qn2); q[5] = LDF(qn2 + 2048); } else { q[2] = LDF(qn + 4096); q[3] = LDF(qn + 6144); } }
#pragma unroll
    for (int i = 0; i < 4; ++i) {
      const int ii = (g & 1) ? 3 - i : i;
      a[ii][g & 3] = MFMA(g < 4 ? p0[ii] : p1[ii], q[g], a[ii][g & 3]);
    }
    SGB(0x8, 1); SGB(0x100, 1); SGB(0x8, 1); SGB(0x100, 1); SGB(0x8, 1); SGB(0x100, 1); SGB(0x8, 1);
    __builtin_amdgcn_sched_barrier(0);
    if (g == 3) BAR();
  }
}

__device__ __forceinline__ void consumer(int wid, int lane, int b, char* lds, rsrc_t rws, rsrc_t rout,
                                         unsigned tch, int tp1, int tq1 STAMP_ARG) {
  STAMP_DECL;
  const int z = tq1 >> 3, te = tq1 & 7;
  const int wp = wid >> 1, wq = wid & 1, fr = lane & 15, fq = lane >> 4;
  const int c0 = (fq ^ (fr & 7)) * 16, c1 = ((fq + 4) ^ (fr & 7)) * 16;
  int q0 = (wq * 64 + fr) * 128 + c0, q1 = (wq * 64 + fr) * 128 + c1;
  volatile LAS unsigned* misc = (volatile LAS unsigned*)(lds + L_MISC);
  f32x4 acc[4][4];
#pragma unroll
  for (int i = 0; i < 4; ++i)
#pragma unroll
    for (int j = 0; j < 4; ++j) acc[i][j] = f32x4{0.f, 0.f, 0.f, 0.f};
  unsigned tag;
  u32x4 xv[4];
  {
    Frag f0, f1;
    const int xrow = wq * 32 + (lane & 16) + 4 * (lane >> 5) + ((lane >> 2) & 3);
    const int xr = xrow * 128 + (((wp * 4 + (lane & 3)) ^ (xrow & 7)) * 16);
#define XV(u) do { _Pragma("unroll") for (int k = 0; k < 4; ++k) \
      xv[k] = *reinterpret_cast<const u32x4*>(lds + (u) * UB + xr + (k >> 1) * 8192 + (k & 1) * 1024); } while (0)
    const int p0 = (wp * 64 + fr) * 128 + c0, p1 = (wp * 64 + fr) * 128 + c1;
    const int xt = (2 * te + z - rot1(tp1, tq1)) & 15;
#define G1H(fc, fn, po, qo, EXTRA) do { _Pragma("unroll") for (int g = 0; g < 8; ++g) { \
      EXTRA(g); { if (g == 0) { fn.q[0] = LDF(qo); fn.p[0] = LDF(po); } else if (g == 1) { fn.p[1] = LDF((po) + 2048); fn.p[2] = LDF((po) + 4096); } \
        else if (g == 2) { fn.p[3] = LDF((po) + 6144); fn.q[1] = LDF((qo) + 2048); } else if (g == 3) { fn.q[2] = LDF((qo) + 4096); fn.q[3] = LDF((qo) + 6144); } } \
      _Pragma("unroll") for (int e = 0; e < 2; ++e) { const int i0 = (g & 1) * 2 + e, ii = (g & 2) ? 3 - i0 : i0; \
        acc[ii][g >> 1] = MFMA(fc.p[ii], fc.q[g >> 1], acc[ii][g >> 1]); } \
      SGB(0x8, 1); SGB(0x100, 1); SGB(0x8, 1); SGB(0x100, 2); __builtin_amdgcn_sched_barrier(0); } } while (0)
#define X_NO(g)
#define XI(u) ((u) == 0 ? 0 : (u) == 1 ? 3 : (u) == 2 ? 2 : 7)
#define XVC(t, u) do { if (__builtin_expect((t) == xt, 0)) { XV(XI(u)); PIN4(xv[0], xv[1], xv[2], xv[3]); } __builtin_amdgcn_sched_barrier(0); } while (0)
    int pA[2] = {p0, p1}, pB[2] = {p0 + 7 * UB, p1 + 7 * UB}, qA[2] = {q0 + UB, q1 + UB}, qB[2] = {q0 + 5 * UB, q1 + 5 * UB};
    PIN4(pA[0], pA[1], pB[0], pB[1]); PIN4(qA[0], qA[1], qB[0], qB[1]);
#define XA(u, h) ((u) == 3 ? pB[h] : pA[h] + XI(u) * UB)
#define WA(u, h) ((u) < 2 ? qA[h] + (u) * 3 * UB : qB[h] + ((u) - 2) * 3 * UB)
    int ta = L_MB + (wp * 32 + fq * 8) * 4, tc = L_MB + 1024 + (wq * 32 + 8 * (fr >> 2) + (fr & 3)) * 4;
    const int flip = z ? 0 : 4096;
    int ao[2], ow[2];
#pragma unroll
    for (int h = 0; h < 2; ++h) {
      ao[h] = WS_ABT + tq1 * ABT_ST + tp1 * 2 * UB + wq * 8192 + wp * 1024 + lane * 16 + (h * 4096 ^ flip);
      ow[h] = 2 * UB + wq * 8192 + wp * 1024 + lane * 16 + (h * 4096 ^ flip);
    }
    PIN4(ao[0], ao[1], ow[0], ow[1]);
    asm volatile("" : "+v"(ta), "+v"(tc));
    BAR();
    XVC(0, 0);
    f0.q[0] = LDF(WA(0, 0));
#pragma unroll
    for (int i = 0; i < 4; ++i) f0.p[i] = LDF(XA(0, 0) + i * 2048);
#pragma unroll
    for (int i = 1; i < 4; ++i) f0.q[i] = LDF(WA(0, 0) + i * 2048);
    __builtin_amdgcn_sched_barrier(0);
#pragma unroll 1
    for (int t = 0; t < 12; t += 4) {
#pragma unroll
      for (int u = 0; u < 4; ++u) {
        G1H(f0, f1, XA(u, 1), WA(u, 1), X_NO);
        WAIT_LGKM0();
        BAR();
        XVC(t + u + 1, (u + 1) & 3);
        G1H(f1, f0, XA((u + 1) & 3, 0), WA((u + 1) & 3, 0), X_NO);
      }
    }
    u32x2 rdy;
#define X_RDY(g) if ((g) == 4) rdy = *(const volatile LAS u32x2*)(lds + L_MISC + 16)
    G1H(f0, f1, XA(0, 1), WA(0, 1), X_RDY);
    asm volatile("s_waitcnt vmcnt(0) lgkmcnt(0)" : "+v"(tch) :: "memory");
    BAR();
    XVC(13, 1);
    if (__builtin_expect(!rfl(rdy[0] & rdy[1]), 0)) {
#pragma unroll 1
      for (int spins = 0; spins < SPIN_CAP && !rfl(misc[4] & misc[5]); ++spins) __builtin_amdgcn_s_sleep(1);
    }
    asm volatile("" ::: "memory");
    f32x4 mb[2][2], rd[2][2]; float cs[4];
    unsigned tagv;
#define TB(o) (*reinterpret_cast<const f32x4*>(lds + ta + (o)))
#define X_T1(g) if ((g) >= 4) cs[(g) & 3] = *reinterpret_cast<const float*>(lds + tc + (64 * (((g) & 3) >> 1) + 4 * ((g) & 1)) * 4)
#define X_T2(g) if ((g) == 4) { mb[1][0] = TB(256); mb[1][1] = TB(272); } else if ((g) == 5) tagv = *(const LAS unsigned*)(lds + L_MISC + 8)
    G1H(f1, f0, XA(1, 0), WA(1, 0), X_T1);
    G1H(f0, f1, XA(1, 1), WA(1, 1), X_T2);
    WAIT_LGKM0();
    BAR();
    XVC(14, 2);
    Frag F[4];
#define SB() __builtin_amdgcn_sched_barrier(0)
#define FQ(k, i) F[k].q[i] = LDF(WA(2 + ((k) >> 1), (k) & 1) + (i) * 2048)
#define FP(k, i) F[k].p[i] = LDF(XA(2 + ((k) >> 1), (k) & 1) + (i) * 2048)
#define FA(k, r) do { if ((r) == 0) FQ(k, 0); else if ((r) < 3) FP(k, (r) - 1); else FQ(k, (r) - 2); } while (0)
#pragma unroll
    for (int n = 0; n < 16; ++n) {
      const int j = n >> 2, ii = (j & 1) ? 3 - (n & 3) : n & 3;
      acc[ii][j] = MFMA(f1.p[ii], f1.q[j], acc[ii][j]);
      SB();
      if ((n & 3) != 3) { const int m = n - (n >> 2); FA(m / 6, m % 6); }
      SB();
    }
    BAR();
    XVC(15, 3);
#define VFMA(d, a, c) asm volatile("v_fma_f32 %0, %1, %2, %0" : "+v"(d) : "v"(a), "v"(c))
#define VMUL(d, a) asm volatile("v_mul_f32 %0, %1, %0" : "+v"(d) : "v"(a))
    float cb[2][4][4];
#pragma unroll
    for (int n = 0; n < 32; ++n) {
      const int k = n >> 3, j = (n >> 1) & 3, ii = (n ^ n >> 1) & 1;
      acc[ii][j] = MFMA(F[k].p[ii], F[k].q[j], acc[ii][j]);
      SB();
      if ((n & 3) != 3) { const int m = n - (n >> 2); if (m < 12) FA(2 + m / 6, m % 6); else if (m < 20) FP((m - 12) >> 1, 2 + (m & 1)); }
      if (n == 19) mb[0][0] = TB(0); if (n == 23) mb[0][1] = TB(16); if (n == 26) rd[0][0] = TB(1536); if (n == 27) rd[0][1] = TB(1552);
      {
        const int jj = n >> 3, h = (n >> 2) & 1, r = n & 3;
        cb[h][jj][r] = acc[2 + h][jj][r];
        VFMA(cb[h][jj][r], mb[1][h][r], cs[jj]);
        acc[2 + h][jj][r] = cb[h][jj][r];
      }
      SB();
    }
    WAIT_LGKM0();
    BAR();
    tag = rfl(tagv);
    u32x4 o;
#pragma unroll
    for (int n = 0; n < 32; ++n) {
      const int k = n >> 3, j = (n >> 1) & 3, ii = 2 + ((n ^ n >> 1) & 1);
      acc[ii][j] = MFMA(F[k].p[ii], F[k].q[j], acc[ii][j]);
      SB();
      {
        const int h = (n >> 2) & 1, r = n & 3;
        cb[h][k][r] = acc[h][k][r];
        VFMA(cb[h][k][r], mb[0][h][r], cs[k]);
        if (n) { const int n1 = n - 1; VMUL(cb[(n1 >> 2) & 1][n1 >> 3][n1 & 3], rd[0][(n1 >> 2) & 1][n1 & 3]); }
        if (n > 2 && (n & 1)) { const int n2 = n - 3;
          unsigned o_ = pack2(cb[(n2 >> 2) & 1][n2 >> 3][n2 & 3], cb[(n2 >> 2) & 1][n2 >> 3][(n2 & 3) + 1]); asm volatile("" : "+v"(o_)); o[(n2 >> 1) & 3] = o_; }
        if (n == 20) rd[1][0] = TB(1792); if (n == 22) rd[1][1] = TB(1808);
        if (n > 8 && (n & 7) == 2) {
          __builtin_amdgcn_raw_buffer_store_b128(o, rws, ao[(k - 1) >> 1] + ((k - 1) & 1) * 2048, 0, SC1);
          *reinterpret_cast<u32x4*>(lds + ow[(k - 1) >> 1] + ((k - 1) & 1) * 2048) = o;
        }
      }
      SB();
    }
    STAMP(2);
    VMUL(cb[1][3][3], rd[0][1][3]);
    o[3] = pack2(cb[1][3][2], cb[1][3][3]);
    __builtin_amdgcn_raw_buffer_store_b128(o, rws, ao[1] + 2048, 0, SC1);
    *reinterpret_cast<u32x4*>(lds + ow[1] + 2048) = o;
    asm volatile("" ::: "memory");
#pragma unroll
    for (int j = 0; j < 4; ++j) {
      o = pack8(acc[2][j] * rd[1][0], acc[3][j] * rd[1][1]);
      __builtin_amdgcn_raw_buffer_store_b128(o, rws, ao[j >> 1] + UB + (j & 1) * 2048, 0, SC1);
      *reinterpret_cast<u32x4*>(lds + ow[j >> 1] + SLOT + (j & 1) * 2048) = o;
    }
    STAMP(3);
  }
  f32x4 acb[4][4];
  bf16x8 pa[4], pb[4], q[8];
  int pp = wp * 8192 + lane * 16;
  const int f1o = WS_FLAG1 + (tq1 * 16 + tp1) * 128 + wid * 4;
#define FLAG1(i, n) do { wait_vm<n>(); __builtin_amdgcn_raw_buffer_store_b32(tag, rws, lane ? -1 : f1o + (i) * 32768, 0, SC1); if (i) STAMP(4); __builtin_amdgcn_sched_barrier(0); } while (0)
  auto fl0 = [&](int g) { if (g == 1) FLAG1(0, 4); if (g == 7) FLAG1(1, 1); };
  WAIT_LGKM0();
  BAR();
  STAMP(5);
  pa[0] = LDF(2 * UB + pp); q[0] = LDF(q0); pa[1] = LDF(2 * UB + pp + 2048); pa[2] = LDF(2 * UB + pp + 4096); pa[3] = LDF(2 * UB + pp + 6144);
  q[1] = LDF(q0 + 2048); q[2] = LDF(q0 + 4096); q[3] = LDF(q0 + 6144); q[4] = LDF(q0 + UB);
  __builtin_amdgcn_sched_barrier(0);
  auto none = [](int) {};
  big_half<false, false, true>(acc, acb, pa, pb, q, lds, q0, 2 * UB + pp + 1024, q1, q1 + UB, fl0);
  PIN4(q0, q1, pp, lane);
  {
    int sl = 0;
#pragma unroll 1
    for (int n = 0; n < 12; ++n) {
      const int sn = sl == 2 * SLOT ? 0 : sl + SLOT;
      big_half<true, false, false>(acc, acb, pb, pa, q, lds, sl + q1, sn + 2 * UB + pp, sn + q0, sn + q0 + UB, none);
      big_half<false, false, false>(acc, acb, pa, pb, q, lds, sn + q0, sn + 2 * UB + pp + 1024, sn + q1, sn + q1 + UB, none);
      sl = sn;
    }
  }
  int q0h = q0 + 4 * UB, q1h = q1 + 4 * UB, pph = pp + 5 * UB;
  asm volatile("" : "+v"(q0h), "+v"(q1h), "+v"(pph));
  unsigned plv;
  auto rdpl = [&](int g) { if (g == 6) plv = misc[3]; };
  big_half<true, true, false>(acc, acb, pb, pa, q, lds, q1, pph, q0h, 0, rdpl);
  small_step<false>(acb, pa, pb, q, lds, pph + 1024, q1h, pph + 3 * UB, q0h + 3 * UB, 0);
  small_step<true>(acb, pa, pb, q, lds, pph + 3 * UB + 1024, q1h + 3 * UB, pph, 3 * UB + q0, 3 * UB + q1);
  const unsigned place = rfl(plv) ? rfl(plv) & 1 : wp;
  int xw = WS_XCH + b * XB + wp * 16384 + place * 8192 + wq * 4096 + lane * 16, xz = xw + 65536;
  asm volatile("" : "+v"(xw), "+v"(xz));
#define SEND_E(j) __builtin_amdgcn_raw_buffer_store_b128(pack8(acb[(j) < 4 ? 2 : 0][(j) & 3], acb[(j) < 4 ? 3 : 1][(j) & 3]), rws, xw + ((j) >> 2) * 32768 + ((j) & 3) * 1024, 0, SC1)
  STAMP(10);
  const int f2o = WS_FLAG2 + b * 128 + wid * 4;
#define PA(k) ((k) == 0 ? pph : (k) == 1 ? pph + 3 * UB : (k) == 2 ? pp + 2 * UB : pp + UB)
#define QA0(k) ((k) == 0 ? q0 + 3 * UB : (k) == 1 ? q0h + 2 * UB : (k) == 2 ? q0 : q0h)
#define QA1(k) ((k) == 0 ? q1 + 3 * UB : (k) == 1 ? q1h + 2 * UB : (k) == 2 ? q1 : q1h)
#define JOB(K, I0, pc, qc, LC, pn, KN, IN, qx, LN, EXTRA) do { _Pragma("unroll") for (int g = 0; g < 8; ++g) { \
    if (g < 2 && (LC)) qc[g + 6] = LDF(QA1(K) + (g + 2) * 2048); \
    if ((KN) >= 0) { \
      if (g >= 2 && (LN)) qx[g - 2] = LDF(g < 6 ? QA0(KN) + (g - 2) * 2048 : QA1(KN) + (g - 6) * 2048); \
      if (g < 4) pn[g] = LDF(PA(KN) + ((IN) + (g & 1)) * 2048 + (g >> 1) * 1024); } \
    acc[I0][g & 3] = MFMA(pc[(g >> 2) * 2], qc[g], acc[I0][g & 3]); \
    acc[(I0) + 1][g & 3] = MFMA(pc[(g >> 2) * 2 + 1], qc[g], acc[(I0) + 1][g & 3]); \
    EXTRA(g); \
    SGB(0x8, 1); SGB(0x100, 1); SGB(0x2, 2); SGB(0x8, 1); SGB(0x100, 1); SGB(0x2, 2); \
    __builtin_amdgcn_sched_barrier(0); } \
    if ((KN) < 0) { PIN4(acc[2][0], acc[2][1], acc[2][2], acc[2][3]); PIN4(acc[3][0], acc[3][1], acc[3][2], acc[3][3]); } } while (0)
#define X_NONE(g)
#define X_E0(g) if ((g) >= 1) SEND_E((g) - 1 & 7)
#define X_E1(g) if ((g) == 0) SEND_E(7)
#define X_Z(g) if ((g) >= 1 && (g) < 5) __builtin_amdgcn_raw_buffer_store_b128(pack8(acc[0][((g) - 1) & 3], acc[1][((g) - 1) & 3]), rws, xz + (((g) - 1) & 3) * 1024, 0, SC1); \
    else if ((g) == 5) misc[8 + wid] = 1u
#define FLAG2(i) do { wait_vm<0>(); __builtin_amdgcn_raw_buffer_store_b32(tag ^ place, rws, lane ? -1 : f2o + (i) * 16, 0, SC1); STAMP((i) ? 1 : 9); } while (0)
  float tm[4], tr[4]; unsigned selv; u32x4 sv_;
  const char* tb = lds + L_MB + (wq * 64 + (lane >> 2)) * 4;
#define X_TAB(g) if ((g) >= 4) { tm[(g) & 3] = *reinterpret_cast<const float*>(tb + ((g) & 3) * 64); tr[(g) & 3] = *reinterpret_cast<const float*>(tb + 512 + ((g) & 3) * 64); }
  bf16x8 qb[8], qc_[8];
  JOB(0, 0, pa, q, 1, pb, 1, 0, q, 1, X_E0);
  JOB(1, 0, pb, q, 1, pa, 2, 0, qb, 1, X_E1);
  BAR();
  JOB(2, 0, pa, qb, 1, pb, 3, 0, qc_, 1, X_NONE);
  JOB(3, 0, pb, qc_, 1, pa, 3, 2, q, 0, X_TAB);
  FLAG2(0);
  JOB(3, 2, pa, qc_, 0, pb, 2, 2, qb, 0, X_Z);
  JOB(2, 2, pb, qb, 0, pa, 1, 2, q, 0, X_NONE);
  JOB(1, 2, pa, q, 0, pb, 0, 2, qb, 1, X_NONE);
  FLAG2(1);
#define X_SV(g) if ((g) == 6) { selv = misc[0]; sv_ = *(volatile LAS u32x4*)(lds + L_MISC + 32); }
  JOB(0, 2, pb, qb, 1, pa, -1, 0, q, 0, X_SV);
  STAMP(6);
  if (__builtin_expect(!rfl(sv_[0] & sv_[1] & sv_[2] & sv_[3]), 0)) wait_sent((LAS char*)lds);
  asm volatile("" ::: "memory");
  {
    char* tw = lds + wid * 8192 + (lane & 15) * 64 + (((lane >> 4) ^ (lane >> 1)) & 3) * 16;
    const char* tr_ = lds + wid * 8192 + (lane >> 2) * 64 + ((lane ^ (lane >> 3)) & 3) * 16;
#pragma unroll
    for (int k = 0; k < 8; ++k) *reinterpret_cast<f32x4*>(tw + k * 1024) = acc[2 + (k >> 2)][k & 3];
    asm volatile("" ::: "memory");
#pragma unroll
    for (int k = 0; k < 8; ++k) acc[2 + (k >> 2)][k & 3] = *reinterpret_cast<const f32x4*>(tr_ + k * 1024);
    __builtin_amdgcn_sched_barrier(0);
  }
  f32x2 gf[4][4];
#pragma unroll
  for (int k = 0; k < 4; ++k) {
    asm volatile("" : "+v"(xv[k]));
    const float tn = tm[k] * tr[k];
#pragma unroll
    for (int c = 0; c < 4; ++c) {
      const f32x2 n = f32x2{lo2f(xv[k][c]), hi2f(xv[k][c])} * tr[k] + tn, a = n * -1.44269504f;
      const f32x2 d = f32x2{__builtin_amdgcn_exp2f(a[0]), __builtin_amdgcn_exp2f(a[1])} + 1.0f;
      gf[k][c] = n * f32x2{__builtin_amdgcn_rcpf(d[0]), __builtin_amdgcn_rcpf(d[1])};
    }
    PIN4(gf[k][0], gf[k][1], gf[k][2], gf[k][3]);
    __builtin_amdgcn_sched_barrier(0);
  }
  f32x2 xf[4][4];
#pragma unroll
  for (int k = 0; k < 4; ++k) {
#pragma unroll
    for (int c = 0; c < 4; ++c) xf[k][c] = f32x2{lo2f(xv[k][c]), hi2f(xv[k][c])};
    PIN4(xf[k][0], xf[k][1], xf[k][2], xf[k][3]);
  }
  {
    unsigned sel = rfl(selv);
    if (__builtin_expect(!sel || (int)(sel - 1 >> 6) == tp1 * 2 + wq, 0)) {
#pragma unroll 1
      for (int spins = 0; spins < SPIN_CAP && !sel; ++spins) { sel = rfl(misc[0]); __builtin_amdgcn_s_sleep(2); }
      const int idx = (int)sel - 1;
      if ((idx >> 6) == tp1 * 2 + wq) {
        const int ks = (idx >> 4) & 3;
        const u32x4 xs = ks == 0 ? xv[0] : ks == 1 ? xv[1] : ks == 2 ? xv[2] : xv[3];
        const float ms = ks == 0 ? tm[0] : ks == 1 ? tm[1] : ks == 2 ? tm[2] : tm[3], rs = ks == 0 ? tr[0] : ks == 1 ? tr[1] : ks == 2 ? tr[2] : tr[3];
        u32x4 n;
#pragma unroll
        for (int c = 0; c < 4; ++c) n[c] = ln_pair(xs[c], ms, rs);
        asm volatile("" : "+v"(n));
#pragma unroll 1
        for (int spins = 0; spins < SPIN_CAP && !rfl(misc[1]); ++spins) __builtin_amdgcn_s_sleep(1);
        asm volatile("" ::: "memory");
        const LAS float* upd = (const LAS float*)(lds + L_UPD) + wp * 32 + (lane & 3) * 8;
#pragma unroll
        for (int c = 0; c < 4; ++c) {
          const unsigned u = pack2(lo2f(n[c]) + upd[2 * c], hi2f(n[c]) + upd[2 * c + 1]);
          const f32x2 gn = {silu_fast(lo2f(u)), silu_fast(hi2f(u))};
#pragma unroll
          for (int k = 0; k < 4; ++k) if (ks == k && (idx & 15) == (lane >> 2)) gf[k][c] = gn;
        }
      }
    }
  }
  int xo = ((tp1 * 128 + wq * 64 + (lane >> 2)) * D + te * 128 + z * 64 + wp * 32 + (lane & 3) * 8) * 2, le = wid * 4096 + (lane & 3) * 256 + ((lane >> 2) ^ (lane & 3) * 4) * 16 + 4 * UB;
  const unsigned one = (lane >> 4) == ((lane >> 2) & 3) ? (lane & 1 ? 0x3f800000u : 0x3f80u) : 0u;
  u32x4 s0 = {lane & 2 ? 0u : one, lane & 2 ? one : 0u, 0u, 0u}, s1 = {0u, 0u, s0[0], s0[1]};
  PIN4(xo, le, s0, s1);
  STAMP(7);
#pragma unroll 1
  for (int spins = 0; spins < SPIN_CAP; ++spins) { const u32x2 f = *(volatile LAS u32x2*)(lds + L_MISC + 48); if (rfl(f[0] & f[1])) break; }
  asm volatile("" ::: "memory");
  STAMP(11);
  bf16x8 pc[3][4];
#pragma unroll
  for (int k = 0; k < 4; ++k)
#pragma unroll
    for (int i = 0; i < 3; ++i) pc[i][k] = LDF(le + (i ? i + 1 : 0) * UB + k * 1024);
  __builtin_amdgcn_sched_barrier(0);
#pragma unroll
  for (int k = 0; k < 4; ++k)
#pragma unroll
    for (int i = 0; i < 3; ++i) { acc[2][k] = MFMA(BF8(s0), pc[i][k], acc[2][k]); acc[3][k] = MFMA(BF8(s1), pc[i][k], acc[3][k]); }
#pragma unroll
  for (int k = 0; k < 4; ++k) {
    u32x4 ov;
#pragma unroll
    for (int c = 0; c < 4; ++c) {
      const f32x2 r = f32x2{acc[2 + (c >> 1)][k][(c & 1) * 2], acc[2 + (c >> 1)][k][(c & 1) * 2 + 1]} * gf[k][c] + xf[k][c];
      ov[c] = pack2(r[0], r[1]);
    }
    __builtin_amdgcn_raw_buffer_store_b128(ov, rout, xo + k * 16 * D * 2, 0, SC1);
  }
  STAMP(8);
  STAMP_OUT();
}

#define BCAST(v, k) ((unsigned)__builtin_amdgcn_mov_dpp((int)(v), 0x150 + (k), 0xf, 0xf, true))
template <int K0, int K1>
__device__ __forceinline__ void expand(u32x4 (&dg)[16], unsigned xs, const unsigned (&sel)[4]) {
  if constexpr (K0 < K1) {
    const unsigned v = BCAST(xs, K0);
    dg[K0] = u32x4{__builtin_amdgcn_perm(0u, v, sel[0]), __builtin_amdgcn_perm(0u, v, sel[1]), __builtin_amdgcn_perm(0u, v, sel[2]), __builtin_amdgcn_perm(0u, v, sel[3])};
    asm volatile("" : "+v"(dg[K0]));
    expand<K0 + 1, K1>(dg, xs, sel);
  }
}
template <int K0, int K1, int N>
__device__ __forceinline__ void gemv(f32x4& ae, f32x4& ao, unsigned xs, const unsigned (&sel)[4], const u32x4 (&w)[N]) {
  if constexpr (K0 < K1) {
    const unsigned v = BCAST(xs, K0);
    const bf16x8 dg = BF8((u32x4{__builtin_amdgcn_perm(0u, v, sel[0]), __builtin_amdgcn_perm(0u, v, sel[1]),
                                 __builtin_amdgcn_perm(0u, v, sel[2]), __builtin_amdgcn_perm(0u, v, sel[3])}));
    ae = MFMA(dg, BF8(w[2 * K0]), ae); ao = MFMA(dg, BF8(w[2 * K0 + 1]), ao);
    gemv<K0 + 1, K1, N>(ae, ao, xs, sel, w);
  }
}

__device__ __forceinline__ void helper(int hw, int lane, int b, char* lds, rsrc_t rws, rsrc_t rx, const u16* wr,
                                       const u16* wgu, const u16* wdn, const u16* gq, const char* code, const char* prb, int tp1, int tq1 STAMP_ARG) {
  STAMP_DECL;
  int nb = 0, lim = 14;
  unsigned tch = 0;
  volatile LAS unsigned* misc = (volatile LAS unsigned*)(lds + L_MISC);
  __builtin_amdgcn_s_setprio(3);
  const int z = tq1 >> 3, te = tq1 & 7;
  const int lc = lane & 15, lq = lane >> 4;
  const int r1 = rot1(tp1, tq1), kb = r1 >> 2, amo = WS_AMAX + (tq1 * 16 + tp1) * 128;
#define JOIN() do { HPRE(); BAR(); HSTAMP(); ++nb; } while (0)
#define NEXT() do { if (nb < lim) JOIN(); } while (0)
#define UNTIL(m) do { _Pragma("unroll 1") while (nb < (m)) JOIN(); } while (0)
#define POLLVM(m) do { _Pragma("unroll 1") while (vm_now() > (m) && nb < lim) JOIN(); wait_vm<m>(); } while (0)
  unsigned tag, key, sg; int idx;
  float sw;
  const int rg = b / 28, cg = b - rg * 28;
  const int fg = b >> 3, eg = b & 7;
  u32x4 w[32], wd[14];
  const rsrc_t rgu = mk_rsrc(wgu + (size_t)(rg & 7) * 128 * F + cg * 128, b < 224 ? 128 * F * 2 : 0);
  const rsrc_t rd = mk_rsrc(wdn + (size_t)(fg * 112 + hw * 56) * D + eg * 128, 56 * D * 2);
  const int wvo = lq * 32 * F * 2 + lc * 16, dvo = lq * 14 * D * 2 + lc * 16;
#define LOADW(k0, k1) do { _Pragma("unroll") for (int k = k0; k < (k1); ++k) \
    w[k] = __builtin_amdgcn_raw_buffer_load_b128(rgu, wvo, k * F * 2, NT); asm volatile("" ::: "memory"); } while (0)
#define LOADD(k0, k1) do { _Pragma("unroll") for (int k = k0; k < (k1); ++k) \
    wd[k] = __builtin_amdgcn_raw_buffer_load_b128(rd, dvo, k * D * 2, NT); asm volatile("" ::: "memory"); } while (0)
  const rsrc_t rq = mk_rsrc(gq, QROWS * S * 2);
  const unsigned kt = ktab(tp1);
  const int trow = (te >> 2) * 1024 + (te & 3) * 32 + hw * 16;
  const int t1o = (trow + ((lane >> 1) & 15)) * (S * 2) + (lane & 1) * 128;
#define QTOUCH(o, so) asm volatile("buffer_load_dword %0, %1, %2, %3 offen" : "+v"(tch) : "v"(o), "s"(rq), "s"(so) : "memory")
#define TCH(m) do { _Pragma("unroll") for (int i_ = 0; i_ < 8; ++i_) if ((m) >> i_ & 1) { \
      if (i_ < 7) QTOUCH(t1o, kstep0(kt, 2 * i_) * 128); \
      else QTOUCH(lane & 16 ? ((tp1 >> 3) * 1024 + te * 16 + lc) * (S * 2) + (kstep(kt, 15) ^ hw * 16) * 128 : (trow + lc) * (S * 2) + kstep(kt, 14) * 128, 0); } } while (0)
#define TCH7(i) do { if (te == 7) { QTOUCH((lane >> 1) * (S * 2) + (lane & 1) * 128, (((i) >> 1) * 1024 + hw * 64 + ((i) & 1) * 32) * (S * 2) + kstep0(kt, 4) * 128); } } while (0)
  {
    const rsrc_t rr = mk_rsrc(wr, D * 2);
    u32x4 wf[8];
#pragma unroll
    for (int i = 0; i < 8; ++i) wf[i] = __builtin_amdgcn_raw_buffer_load_b128(rr, lq * 16 + (i & 3) * 64, ((r1 + (i >> 2) * 2) & 15) * 128, 0);
    const unsigned epoch = __builtin_amdgcn_raw_buffer_load_b32(rws, lane == 1 ? (hw ? amo : WS_FLAG2 + b * 128) : lane == 2 ? amo + (WS_FLAG1 - WS_AMAX) + hw * 32768 : lane == 3 ? WS_H + b * 64 : WS_EPOCH, 0, SC1);
    const u32x4 ra = __builtin_amdgcn_raw_buffer_load_b128(rr, lane * 16, 0, 0), rb2 = __builtin_amdgcn_raw_buffer_load_b128(rr, lane * 16 + 1024, 0, 0);
    misc[lc] = 0u;
    u32x4 ones = {0x3f803f80u, 0x3f803f80u, 0x3f803f80u, 0x3f803f80u};
    const int sx = (te >> 1) * 32 + hw * 16, sv = (tp1 >> 1 & 3) * 32 + hw * 16;
    int xa[2], wa[2];
    xa[0] = (sx + lc) * 128 + (lq ^ (lc & 7)) * 16; wa[0] = xa[0] + (sv - sx) * 128 + UB;
    xa[1] = xa[0] ^ 64; wa[1] = wa[0] ^ 64;
    asm volatile("" : "+v"(ones));
    f32x4 d1 = {0.f, 0.f, 0.f, 0.f}, d2 = d1, d3 = d1, d4 = d1;
    HST(9);
#pragma unroll
    for (int u = 0; u < 4; ++u) {
      if (u == 2) { wa[0] += 4 * UB; wa[1] += 4 * UB; }
      if (u == 3) { xa[0] += 7 * UB; xa[1] += 7 * UB; }
      PIN4(xa[0], xa[1], wa[0], wa[1]);
      WAIT_LGKM0();
      JOIN();
      const unsigned tj = (unsigned)__builtin_amdgcn_s_memrealtime();
      bf16x8 xf[2], vf[2];
#pragma unroll
      for (int h = 0; h < 2; ++h) { xf[h] = LDF(xa[h] + (u == 3 ? 0 : XI(u) * UB)); vf[h] = LDF(wa[h] + (u & 1) * 3 * UB); }
      __builtin_amdgcn_sched_barrier(0);
      if (u) LOADW((int)(SWT >> 8 * u & 63), (int)(SWT >> 8 * u + 8 & 63));
      if (u) TCH7(u - 1);
#pragma unroll
      for (int h = 0; h < 2; ++h) {
        d2 = MFMA(xf[h], xf[h], d2); d4 = MFMA(BF8(ones), xf[h], d4); d3 = MFMA(BF8(ones), vf[h], d3); d1 = MFMA(BF8(wf[2 * u + h]), xf[h], d1);
      }
      PIN4(d1, d2, d3, d4);
      if (u == 0) {
        tag = mk_tag(epoch);
        asm volatile("" : "+s"(tag));
        misc[2] = tag;
        const f32x4 d5 = MFMA(BF8(rb2), BF8(ones), MFMA(BF8(ra), BF8(ones), (f32x4{0.f, 0.f, 0.f, 0.f})));
        const int s4 = __builtin_bit_cast(int, (d5[0] + d5[1]) + (d5[2] + d5[3]));
#define RL(l) __builtin_bit_cast(float, __builtin_amdgcn_readlane(s4, l))
        sw = (RL(0) + RL(16)) + (RL(32) + RL(48));
        asm volatile("" : "+v"(sw));
        LOADW(0, (int)(SWT >> 8 & 63));
      }
      if (PI >> 8 * u & 255) {
#pragma unroll 1
        while ((unsigned)__builtin_amdgcn_s_memrealtime() - tj < (PI >> 8 * u & 255)) ;
      }
      __builtin_amdgcn_sched_barrier(0);
    }
    JOIN();
    const float sq = (lc & 2) ? ((lc & 1) ? d2[3] : d2[2]) : ((lc & 1) ? d2[1] : d2[0]);
    __builtin_amdgcn_raw_buffer_store_b128(u32x4{__float_as_uint(d4[0]), __float_as_uint(sq), __float_as_uint(d1[0]), tag}, rws,
                                           lq == lc >> 2 ? WS_XP + (tp1 * 4 + kb) * 2048 + (sx + lc) * 16 : -1, 0, SC1);
    __builtin_amdgcn_raw_buffer_store_b64(u32x2{__float_as_uint(d3[0]), tag}, rws, lane < 16 ? WS_WP + (tq1 * 4 + kb) * 1024 + (sv + lc) * 8 : WS_XCH + b * XB + 98304 + hw * 8192 + lq * 2304 + lc * 8, 0, SC1);
    __builtin_amdgcn_sched_barrier(0);
    LOADW((int)(SWT >> 32 & 63), (int)(SWT >> 40 & 63));
  }
  const int srow = hw * 32 + (lane & 3) + 2 * (lane & 44) + ((lane >> 2) & 4);
  {
    const int gxo = WS_XP + tp1 * 8192 + (hw * 64 + lane) * 16, gwo = WS_WP + tq1 * 4096 + (hw * 64 + lane) * 8;
    u32x4 gx[4]; u32x2 gw[4];
#define GATHER() asm volatile("buffer_load_dwordx4 %0, %8, %10, 0 offen sc1\n\tbuffer_load_dwordx4 %1, %8, %10, 0 offen offset:2048 sc1\n\t" \
                 "buffer_load_dwordx4 %2, %8, %10, %11 offen sc1\n\tbuffer_load_dwordx4 %3, %8, %10, %11 offen offset:2048 sc1\n\t" \
                 "buffer_load_dwordx2 %4, %9, %10, 0 offen sc1\n\tbuffer_load_dwordx2 %5, %9, %10, 0 offen offset:1024 sc1\n\t" \
                 "buffer_load_dwordx2 %6, %9, %10, 0 offen offset:2048 sc1\n\tbuffer_load_dwordx2 %7, %9, %10, 0 offen offset:3072 sc1" \
                 : "=&v"(gx[0]), "=&v"(gx[1]), "=&v"(gx[2]), "=&v"(gx[3]), "=&v"(gw[0]), "=&v"(gw[1]), "=&v"(gw[2]), "=&v"(gw[3]) \
                 : "v"(gxo), "v"(gwo), "s"(rws), "s"(4096) : "memory")
    UNTIL(sched(T1_T, te));
    GATHER();
    LOADW((int)(SWT >> 40), NWC);
    wait_vm<NWC - (int)(SWT >> 40)>();
    TCH7(3);
#pragma unroll 1
    for (int spins = 0;; ++spins) {
      asm volatile("" : "+v"(gx[0]), "+v"(gx[1]), "+v"(gx[2]), "+v"(gx[3]), "+v"(gw[0]), "+v"(gw[1]), "+v"(gw[2]), "+v"(gw[3]) :: "memory");
      unsigned bad = 0;
#pragma unroll
      for (int q = 0; q < 4; ++q) bad |= (gx[q][3] ^ tag) | (gw[q][1] ^ tag);
      if (__builtin_expect(__all(bad == 0) || spins > SPIN_CAP / 64, 1)) break;
      GATHER();
      NEXT();
      POLLVM(0);
    }
    HST(3);
#define SUM4(v, c) ((__uint_as_float(v[0][c]) + __uint_as_float(v[1][c])) + (__uint_as_float(v[2][c]) + __uint_as_float(v[3][c])))
    float sum = SUM4(gx, 0), sq = SUM4(gx, 1), dot = SUM4(gx, 2);
    *(LAS float*)(lds + L_MB + 1024 + srow * 4) = SUM4(gw, 0);
    asm volatile("" : "+v"(sum), "+v"(sq), "+v"(dot));
    const float mn = sum * (1.0f / D);
    const float var = fmaxf(fmaf(-mn, mn, sq * (1.0f / D)), 0.f);
    const float mbv = rbf(mn), den = rbf(__builtin_amdgcn_sqrtf(rbf(rbf(var) + 1e-5f))), rden = __builtin_amdgcn_rcpf(den);
    *(LAS float*)(lds + L_MB + srow * 4) = -mbv;
    *(LAS float*)(lds + L_MB + 512 + srow * 4) = rden;
    *(LAS float*)(lds + L_MB + 1536 + srow * 4) = z ? -rden : rden;
    asm volatile("" ::: "memory");
    misc[4 + hw] = 1u;
    float rs = (dot - mbv * sw) * rden;
    sg = pack2(mbv, den);
    asm volatile("" : "+v"(rs), "+v"(sg));
    lim = NBAR;
    const unsigned rb = pack2(rs, rs) & 0xffffu;
    unsigned srt = (rb & 0x8000u) ? (~rb & 0xffffu) : (rb | 0x8000u);
    if (rs != rs) srt = 0u;
    key = (srt << 16) | (unsigned)(0xffff - tp1 * 128 - srow);
    const unsigned mk = wave_maxu(key);
    if (key == mk) __builtin_amdgcn_raw_buffer_store_b128(u32x4{key, tag, sg, tag}, rws, amo + hw * 16, 0, SC1);
    HST(2);
    UNTIL(WB_AT + (WBS >> 4 * te & 3));
    TCH(3);
    LOADW(NWC, 32);
  }
  unsigned sel[4];
#pragma unroll
  for (int d = 0; d < 4; ++d) {
    const unsigned src = (lc & 2) ? 0x0302u : 0x0100u;
    sel[d] = (lc >> 2) == d ? ((lc & 1) ? (src << 16 | 0x0c0cu) : (0x0c0c0000u | src)) : 0x0c0c0c0cu;
  }
  PIN4(sel[0], sel[1], sel[2], sel[3]);
  JOIN();
  const int hc = fg * 112 + hw * 56 + (lq * 7 + min(lc, 6)) * 2;
  int hio = WS_H + (hc & ~127) * 4 + ((hc & 6) * 8 + ((hc >> 3) & 15)) * 8;
  const int hs = min(b, 223) * 8 + hw * 4 + ((lane >> 3) & 3);
  int guo = WS_GU + ((lane & 7) * 2 + (lane >> 5)) * F * 8 + hs * 16, hoo = b < 224 && !(lane & 39) ? WS_H + hs * 8 : -1;
  asm volatile("" : "+v"(hio), "+v"(hoo));
  int gao = WS_AMAX + tq1 * 2048 + ((lane >> 1) & 15) * 128 + (lane & 1) * 16, gso = b < 224 ? WS_GU + ((rg * 2 + hw) * F + cg * 128) * 8 + lane * 16 : -1;
  asm volatile("" : "+v"(guo), "+v"(gao), "+v"(gso));
  UNTIL(15);
#pragma unroll 1
  for (int spins = 0;; ++spins) {
    const u32x4 am = __builtin_amdgcn_raw_buffer_load_b128(rws, gao, 0, SC1);
    NEXT();
    UNTIL(17);
#pragma unroll 1
    while (vm_now() > 0 && nb < lim && nb != 19 && nb != 17) JOIN();
    key = am[0]; sg = am[2];
    if (__builtin_expect(__all(((am[1] ^ tag) | (am[3] ^ tag)) == 0) || spins > SPIN_CAP / 64, 1)) break;
  }
  {
    const unsigned mk = wave_maxu(key);
    sg = __builtin_amdgcn_readlane(sg, __builtin_ctzll(__ballot(key == mk)));
    key = mk;
  }
  idx = min(0xffff - (int)(key & 0xffffu), S - 1);
  const unsigned xe = __builtin_amdgcn_raw_buffer_load_b32(rx, (idx * D + (rg & 7) * 128) * 2 + lane * 4, 0, 0);
  misc[0] = idx + 1;
  HST(7);
  if (nb != 19) NEXT();
#pragma unroll 1
  while (vm_now() > 0 && nb < lim && nb != 19) JOIN();
  {
    const unsigned xs = ln_pair(xe, -lo2f(sg), __builtin_amdgcn_rcpf(hi2f(sg)));
    if (b == 0 && lane == 0 && hw == 1) __builtin_amdgcn_raw_buffer_store_b32(tag, rws, WS_EPOCH, 0, SC1);
    u32x4 dg[16];
    expand<0, 7>(dg, xs, sel);
    if (nb != 19) NEXT();
    expand<7, 16>(dg, xs, sel);
    UNTIL(19);
    const bool idle = nb == 19;
    if (!idle) NEXT();
    f32x4 ae = {0.f, 0.f, 0.f, 0.f}, ao = ae;
#pragma unroll
    for (int k = 0; k < 16; ++k) {
      ae = MFMA(BF8(dg[k]), BF8(w[2 * k]), ae); ao = MFMA(BF8(dg[k]), BF8(w[2 * k + 1]), ao);
      if (k == 7) { asm volatile("" : "+v"(ae), "+v"(ao)); if (__builtin_expect(!idle, 0)) NEXT(); }
    }
    __builtin_amdgcn_raw_buffer_store_b128(u32x4{__float_as_uint(ae[0] + ao[2]), tag, __float_as_uint(ae[1] + ao[3]), tag}, rws, gso, 0, SC1);
  }
  HST(4);
  if (nb == 19) JOIN();
  {
    const int m0_ = 20 + (te & 1);
    unsigned lat = ~0u;
#define LATE(i) do { UNTIL(m0_ + (i)); if ((i) == 0) __builtin_amdgcn_s_sleep(BSL); else { u64 t0_, t1_; unsigned d_; \
      const bool s_ = (hw ^ b >> 3 ^ (i) >> 1) & 1;       \
      if (s_) __builtin_amdgcn_s_sleep(3); \
      asm volatile("s_memtime %0\n\ts_waitcnt lgkmcnt(0)\n\ts_load_dword %2, %3, %4\n\ts_waitcnt lgkmcnt(0)\n\ts_memtime %1\n\ts_waitcnt lgkmcnt(0)" \
                   : "=&s"(t0_), "=&s"(t1_), "=&s"(d_) : "s"(prb), "n"((i) / 2 * 2304) : "memory"); \
      lat = min(lat, (unsigned)(t1_ - t0_)); \
      _Pragma("unroll 1") while ((unsigned)__builtin_amdgcn_s_memtime() - (unsigned)t0_ < (s_ ? BSL * 64 - 270 : BSL * 64 - 70)) ; } } while (0)
    LATE(0);
    TCH(0x18);
    LATE(2);
    u32x4 gu;
    asm volatile("buffer_load_dwordx4 %0, %1, %2, 0 offen sc1" : "=&v"(gu) : "v"(guo), "s"(rws) : "memory");
    TCH(0x20);
    LOADD(0, WD1);
    NEXT();
    asm volatile("s_waitcnt vmcnt(%1)" : "+v"(gu) : "n"(WD1 + 1) : "memory");
#pragma unroll 1
    for (int spins = 0; !__all(((gu[1] ^ tag) | (gu[3] ^ tag)) == 0) && spins < SPIN_CAP / 64; ++spins) {
      asm volatile("buffer_load_dwordx4 %0, %1, %2, 0 offen sc1" : "=&v"(gu) : "v"(guo), "s"(rws) : "memory");
      NEXT();
      POLLVM(0);
      asm volatile("" : "+v"(gu) :: "memory");
    }
    {
      float s0 = __uint_as_float(gu[0]), s1 = __uint_as_float(gu[2]);
      DPP_ADD(s0, 0xb1, 0xf, true); DPP_ADD(s1, 0xb1, 0xf, true); DPP_ADD(s0, 0x4e, 0xf, true); DPP_ADD(s1, 0x4e, 0xf, true);
      DPP_ADD(s0, 0x141, 0xf, true); DPP_ADD(s1, 0x141, 0xf, true);
      const unsigned gp = pack2(s0, s1);
      const unsigned up = __builtin_amdgcn_permlane32_swap(gp, gp, false, false)[1];
      const unsigned sl2 = pack2(silu_fast(lo2f(gp)), silu_fast(hi2f(gp)));
      __builtin_amdgcn_raw_buffer_store_b64(u32x2{pack2(lo2f(sl2) * lo2f(up), hi2f(sl2) * hi2f(up)), tag}, rws, hoo, 0, SC1);
    }
    HST(8);
    LATE(4);
    TCH(0x40);
    LOADD(WD1, WD2);
    if (!hw && lane < (CODE_LINES + 7) / 8) {
      const int co = min(((b >> 3) & 7) * ((CODE_LINES + 7) / 8) + lane, CODE_LINES - 1) * 128;
      asm volatile("global_load_dword %0, %1, off" : "+v"(tch) : "v"(code + co) : "memory");
    }
    LATE(6);
    u32x2 hv;
    asm volatile("buffer_load_dwordx2 %0, %1, %2, 0 offen sc1" : "=&v"(hv) : "v"(hio), "s"(rws) : "memory");
    TCH(0x80);
    LOADD(WD2, 14);
    NEXT();
    asm volatile("s_waitcnt vmcnt(%1)" : "+v"(hv) : "n"(14 - WD2 + 1) : "memory");
#pragma unroll 1
    for (int spins = 0; !__all(hv[1] == tag) && spins < SPIN_CAP / 64; ++spins) {
      asm volatile("buffer_load_dwordx2 %0, %1, %2, 0 offen sc1" : "=&v"(hv) : "v"(hio), "s"(rws) : "memory");
      NEXT();
      POLLVM(0);
      asm volatile("" : "+v"(hv) :: "memory");
    }
    f32x4 ae = {0.f, 0.f, 0.f, 0.f}, ao = ae;
    gemv<0, 5>(ae, ao, hv[0], sel, wd);
    asm volatile("" : "+v"(ae), "+v"(ao));
    NEXT();
    POLLVM(0);
    gemv<5, 7>(ae, ao, hv[0], sel, wd);
    f32x2 a = {ae[0] + ao[2], ae[1] + ao[3]};
    HST(5);
    if (hw == 1) {
      *(LAS f32x2*)(lds + L_CMB + lane * 8) = a;
      asm volatile("" ::: "memory");
      misc[6] = lat;
    } else {
      f32x2 o; unsigned f;
#pragma unroll 1
      for (int spins = 0; spins < SPIN_CAP; ++spins) {
        NEXT();
        f = misc[6];
        o = *(volatile LAS f32x2*)(lds + L_CMB + lane * 8);
        if (rfl(f)) break;
      }
      misc[3] = f < lat ? 3u : 2u;
      __builtin_amdgcn_raw_buffer_store_b128(u32x4{__float_as_uint(a[0] + o[0]), tag, __float_as_uint(a[1] + o[1]), tag}, rws,
                                             WS_PART + (fg * D + eg * 128) * 8 + lane * 16, 0, SC1);
      HST(6);
    }
  }
  if (hw == 1 && (idx >> 7) == tp1) {
    const unsigned srt = key >> 16;
    const float r = lo2f((srt & 0x8000u) ? (srt & 0x7fffu) : (~srt & 0xffffu));
    const float rsel = rbf(1.0f / (1.0f + __expf(-r)));
    const int cp = lane & 31;
    const int e = z * 64 + cp * 2;
    const int po = WS_PART + ((lane >> 5) * D + te * 128) * 8 + ((e & 6) * 8 + (e >> 3)) * 16;
    float y0, y1;
    UNTIL(NBAR);
    wait_sent((LAS char*)lds);
#pragma unroll 1
    for (int spins = 0;; ++spins) {
#pragma unroll
      for (int k = 0; k < 16; ++k) w[k] = __builtin_amdgcn_raw_buffer_load_b128(rws, po, k * 2 * D * 8, SC1);
      asm volatile("" ::: "memory");
      unsigned bad = 0;
      y0 = 0.f; y1 = 0.f;
#pragma unroll
      for (int k = 0; k < 16; ++k) { bad |= (w[k][1] ^ tag) | (w[k][3] ^ tag); y0 += __uint_as_float(w[k][0]); y1 += __uint_as_float(w[k][2]); }
      if (__all(bad == 0) || spins > SPIN_CAP / 64) break;
    }
    { const auto p0 = __builtin_amdgcn_permlane32_swap(__float_as_uint(y0), __float_as_uint(y0), false, false);
      const auto p1 = __builtin_amdgcn_permlane32_swap(__float_as_uint(y1), __float_as_uint(y1), false, false);
      y0 = __uint_as_float(p0[0]) + __uint_as_float(p0[1]); y1 = __uint_as_float(p1[0]) + __uint_as_float(p1[1]); }
    *(LAS f32x2*)(lds + L_UPD + cp * 8) = f32x2{rbf(rbf(y0) * rsel), rbf(rbf(y1) * rsel)};
    asm volatile("" ::: "memory");
    misc[1] = 1u;
    HST(6);
  }
  HST(10);
  POLLVM(0);
  asm volatile("" : "+v"(tch));
  STAMP_OUT();
}

typedef __attribute__((ext_vector_type(8))) u64 u64x8;
typedef __attribute__((ext_vector_type(2))) u64 u64x2;
__global__ __launch_bounds__(512, 2) void fused(const u16*, const u16*, const u16*, const u16*, char*, const u16*, const u16*, const u16*,
                                                const u16*, const u16*, u16*) {
  extern __shared__ __attribute__((aligned(16))) char lds[];
  u64x8 a0; u64x2 a1; u64 a2;
  u64 pc;
  asm volatile("s_load_dwordx16 %0, %4, 0\n\ts_load_dwordx4 %1, %4, 64\n\ts_load_dwordx2 %2, %4, 80\n\ts_getpc_b64 %3"
               : "=&s"(a0), "=&s"(a1), "=&s"(a2), "=&s"(pc) : "s"(__builtin_amdgcn_kernarg_segment_ptr()) : "memory");
  __builtin_amdgcn_sched_barrier(0);
  const char* code = (const char*)(pc & ~255ull);
  unsigned tch = 0;
  if (threadIdx.x < (CODE_LINES + 8 - 1) / 8) {
    const int co = min((int)(((blockIdx.x >> 3) & (8 - 1)) + threadIdx.x * 8), CODE_LINES - 1) * 128;
    asm volatile("global_load_dword %0, %1, %2" : "+v"(tch) : "v"(co), "s"(code) : "memory");
  }
  __builtin_amdgcn_sched_barrier(0);
  const int wid = __builtin_amdgcn_readfirstlane(threadIdx.x >> 6), lane = threadIdx.x & 63;
  const int b = blockIdx.x, g = b & 7, i = b >> 3;
  const int tp1 = (g & BH ? 8 : 0) + (g & (7 ^ BD) ? 4 : 0) + (i & 3), tq1 = (g & BZ ? 8 : 0) + (i >> 2);
  const int z = tq1 >> 3, te = tq1 & 7;
  const int L = wid & 1, rsub = lane >> 3;
  int v1 = ((L ? te : tp1) * 128 + 8 * (rsub >> 2) + (rsub & 3)) * (D * 2) + ((lane & 7) ^ rsub) * 16, ko = rot1(tp1, tq1) * 128;
  unsigned g1n = (L ? D : S) * D * 2;
  int role = wid >> 1;
  asm volatile("" : "+v"(v1));
  asm volatile("" : "+s"(ko), "+s"(g1n), "+s"(role));
  __builtin_amdgcn_sched_barrier(0);
  asm volatile("s_waitcnt lgkmcnt(0)" : "+s"(a0), "+s"(a1), "+s"(a2) :: "memory");
  const u16 *x = (const u16*)a0[0], *wr = (const u16*)a0[3], *sre = (const u16*)a0[5], *sim = (const u16*)a0[6], *wg = (const u16*)a0[7];
  const u16 *wu = (const u16*)a1[0], *wdn = (const u16*)a1[1];
  char* ws = (char*)((a0[4] + 0x1ffff) & ~0x1ffffull);
  u16* out = (u16*)a2;
  const rsrc_t rws = mk_rsrc(ws, WS_END);
  if (__builtin_expect(role == 2, 1)) {
    loader(L, lane, (LAS char*)lds, mk_rsrc((const void*)(L ? (z ? a0[2] : a0[1]) : a0[0]), g1n), v1, ko, (z ? sim : sre) + (size_t)(tp1 & 7) * 128 * S,
           ws + WS_ABT + (size_t)tq1 * ABT_ST, rws, WS_FLAG1 + tq1 * 2048, b, tp1 STAMP_PASS);
  } else if (wid < 4) {
    consumer(wid, lane, b, lds, rws, mk_rsrc(out, S * D * 2), tch, tp1, tq1 STAMP_PASS);
  } else {
    const int hw = wid - 6;
    helper(hw, lane, b, lds, rws, mk_rsrc(x, S * D * 2), wr, hw ? wu : wg, wdn,
           (z ? sim : sre) + (size_t)(tp1 & 7) * 128 * S, code, ws + WS_XCH + b * XB + 98304 + (wid & 1) * 8192, tp1, tq1 STAMP_PASS);
  }
}

extern "C" void kernel_launch(void* const* d_in, const int* in_sizes, int n_in, void* d_out, int out_size,
                              void* d_ws, size_t ws_size, hipStream_t stream) {
  (void)hipFuncSetAttribute((const void*)fused, hipFuncAttributeMaxDynamicSharedMemorySize, LDS_BYTES);
  fused<<<256, 512, LDS_BYTES, stream>>>((const u16*)d_in[0], (const u16*)d_in[3], (const u16*)d_in[4], (const u16*)d_in[6], (char*)d_ws,
                                         (const u16*)d_in[1], (const u16*)d_in[2], (const u16*)d_in[7], (const u16*)d_in[8],
                                         (const u16*)d_in[9], (u16*)d_out);
}
```
